```python
import torch

def _softmax(x, axis=-1):
    return torch.softmax(x, dim=axis)
def _gelu(x):
    return torch.nn.functional.gelu(x, approximate="tanh")
def _silu(x):
    return torch.nn.functional.silu(x)
def _sigmoid(x):
    return torch.sigmoid(x)
def _layernorm(x, axis=-1, eps=1e-5):
    m = x.mean(dim=axis, keepdim=True)
    v = x.var(dim=axis, keepdim=True, unbiased=False)
    return (x - m) / torch.sqrt(v + eps)
def _scatter_add(tgt, idx, src, axis=0):
    return tgt.clone().index_add_(axis, idx, src)
def _segment_sum(src, seg, n):
    out = torch.zeros((n,) + src.shape[1:], dtype=src.dtype)
    return out.index_add_(0, seg, src)
def _pad(x, pw):
    flat = tuple(v for pair in reversed(pw) for v in pair)
    return torch.nn.functional.pad(x, flat)

kernel_name = 'gated_mix__mha_block__linear_kernel_hedgehog__prompt'

def setup_inputs():

    _w = (lambda a: (lambda t: t.to(torch.bfloat16) if t.is_floating_point() else t)(torch.as_tensor(a)))
    def _A_setup():
        B, S, D, H = 1, 2048, 512, 32
        return {
            "x": _w((lambda *s: torch.randn(*s, dtype=torch.bfloat16) * 0.05)(B, S, D)),
            "wqkv": _w((lambda *s: torch.randn(*s, dtype=torch.bfloat16) * 0.8)(D, 3 * D)),
            "wo": _w((lambda *s: torch.randn(*s, dtype=torch.bfloat16) * 0.05)(D, D)),
        }
    _A_in = _A_setup()

    def _B_setup():
        B, S, D, H = 1, 2048, 512, 32
        Dh = D // H
        return {
            "x": _w((lambda *s: torch.randn(*s, dtype=torch.bfloat16) * 0.05)(B, S, D)),
            "wqkv": _w((lambda *s: torch.randn(*s, dtype=torch.bfloat16) * 1.6)(D, 3 * D)),
            "w_phi": _w((lambda *s: torch.randn(*s, dtype=torch.bfloat16) * 1.6)(H, Dh, Dh) * (Dh ** -0.5)),
            "wo": _w((lambda *s: torch.randn(*s, dtype=torch.bfloat16) * 0.05)(D, D)),
        }
    _B_in = _B_setup()

    return {
        "A_x": _A_in["x"],
        "A_wqkv": _A_in["wqkv"],
        "A_wo": _A_in["wo"],
        "B_x": _B_in["x"],
        "B_wqkv": _B_in["wqkv"],
        "B_w_phi": _B_in["w_phi"],
        "B_wo": _B_in["wo"],
        "gate_w": _w((lambda *s: torch.randn(*s, dtype=torch.bfloat16) * 0.05)(512) * 16.0),
    }

def reference(A_x, A_wqkv, A_wo, B_x, B_wqkv, B_w_phi, B_wo, gate_w):
    def _A_ref(x, wqkv, wo):
        B, S, D = x.shape
        H = 32
        Dh = D // H
        qkv = (lambda x, *d: x.permute(*d))((x @ wqkv).reshape(B, S, 3, H, Dh), 2, 0, 3, 1, 4)
        q, k, v = qkv[0], qkv[1], qkv[2]
        scores = torch.einsum("bhqd,bhkd->bhqk", q, k) * (Dh ** -0.5)
        p = _softmax(scores, axis=-1)
        o = torch.einsum("bhqk,bhkd->bhqd", p, v)
        return (lambda x, *d: x.permute(*d))(o, 0, 2, 1, 3).reshape(B, S, D) @ wo

    def _B_ref(x, wqkv, w_phi, wo):
        B, S, D = x.shape
        H = 32; Dh = D // H
        qkv = (lambda x, *d: x.permute(*d))((x @ wqkv).reshape(B, S, 3, H, Dh), 2, 0, 3, 1, 4)
        q, k, v = qkv[0], qkv[1], qkv[2]
        pq = torch.einsum("bhsd,hdf->bhsf", q, w_phi)
        pk = torch.einsum("bhsd,hdf->bhsf", k, w_phi)
        phi_q = torch.concatenate([_softmax(pq, axis=-1), _softmax(-pq, axis=-1)], axis=-1)
        phi_k = torch.concatenate([_softmax(pk, axis=-1), _softmax(-pk, axis=-1)], axis=-1)
        kv = torch.einsum("bhsf,bhsd->bhfd", phi_k, v)
        z  = torch.einsum("bhsf->bhf", phi_k)
        num = torch.einsum("bhqf,bhfd->bhqd", phi_q, kv)
        den = torch.einsum("bhqf,bhf->bhq", phi_q, z)[..., None] + 1e-6
        o = num / den
        return (lambda x, *d: x.permute(*d))(o, 0, 2, 1, 3).reshape(B, S, D) @ wo

    x = A_x
    g = _sigmoid((x * gate_w).sum(-1, keepdims=True))
    a = _A_ref(x, A_wqkv, A_wo)
    b = _B_ref(x, B_wqkv, B_w_phi, B_wo)
    return g * a + (1 - g) * b
```

```cpp
#include <hip/hip_runtime.h>
#include <hip/hip_bf16.h>
#include <stdint.h>

namespace {

typedef __bf16 bf16x8 __attribute__((ext_vector_type(8)));
typedef __bf16 bf16x2 __attribute__((ext_vector_type(2)));
typedef float f32x16 __attribute__((ext_vector_type(16)));
typedef float f32x8 __attribute__((ext_vector_type(8)));
typedef float f32x4 __attribute__((ext_vector_type(4)));
typedef float f32x2 __attribute__((ext_vector_type(2)));
typedef unsigned u32x16 __attribute__((ext_vector_type(16)));
typedef unsigned u32x8 __attribute__((ext_vector_type(8)));
typedef unsigned u32x4 __attribute__((ext_vector_type(4)));
typedef unsigned u32x2 __attribute__((ext_vector_type(2)));
typedef short s16x4 __attribute__((ext_vector_type(4)));
typedef __amdgpu_buffer_rsrc_t rsrc_t;
#define LAS __attribute__((address_space(3)))
#define DI __device__ __forceinline__

constexpr unsigned WS_EPOCH = 0;
constexpr unsigned WS_FKV = 0x1000;
constexpr unsigned WS_FKP = 0x2000;
constexpr unsigned WS_FKR = 0x3000;
constexpr unsigned WS_FOB = 0x4000;
constexpr unsigned WS_FOA = 0x8000;
constexpr unsigned WS_KN = 0xC000;
constexpr unsigned WS_FPQ = 0xD000;
constexpr unsigned WS_KR = 0x10000;
constexpr unsigned WS_KV = 0x100000;
constexpr unsigned WS_KP = 0x500000;
constexpr unsigned WS_OB = 0x900000;
constexpr unsigned WS_OA = 0xB00000;
constexpr unsigned WS_END = 0xD00000;

constexpr unsigned L_X = 0;
constexpr unsigned L_RING = 65536;
constexpr unsigned SLOT = 24576;
constexpr unsigned L_WOB = 0;
constexpr unsigned L_WOA = 65536;
constexpr unsigned L_RED = 0;
constexpr unsigned L_PQ = 131072;
constexpr unsigned L_WPHI = 139264;
constexpr unsigned L_GW = 141312;
constexpr unsigned L_DUMMY = 142336;
constexpr unsigned L_Q = 147456;
constexpr unsigned L_VB = 155648;
constexpr unsigned L_BYTES = 163840;

constexpr float LOG2E = 1.4426950408889634f;
constexpr float SAFE_SHIFT = 48.0f;
constexpr unsigned ONES = 0x3F803F80u;
constexpr unsigned SPIN_CAP = 1u << 18;
constexpr int SC1 = 16;
constexpr unsigned OOB = 0xF0000000u;

#define MFMA(a, b, c) __builtin_amdgcn_mfma_f32_32x32x16_bf16(__builtin_bit_cast(bf16x8, a), __builtin_bit_cast(bf16x8, b), c, 0, 0, 0)

DI unsigned pk2(float a, float b) { f32x2 v = {a, b}; return __builtin_bit_cast(unsigned, __builtin_convertvector(v, bf16x2)); }
template <int O> DI u32x4 pack8(const f32x16& v) {
  return u32x4{pk2(v[O], v[O + 1]), pk2(v[O + 2], v[O + 3]), pk2(v[O + 4], v[O + 5]), pk2(v[O + 6], v[O + 7])};
}
template <int O> DI u32x4 pack8s(const f32x16& v, float s) {
  return u32x4{pk2(v[O] * s, v[O + 1] * s), pk2(v[O + 2] * s, v[O + 3] * s), pk2(v[O + 4] * s, v[O + 5] * s), pk2(v[O + 6] * s, v[O + 7] * s)};
}
DI float dot2(unsigned a, unsigned b, float c) {
  return __builtin_amdgcn_fdot2_f32_bf16(__builtin_bit_cast(bf16x2, a), __builtin_bit_cast(bf16x2, b), c, false);
}
DI float sumsq8(const u32x4& f) { return dot2(f[3], f[3], dot2(f[2], f[2], dot2(f[1], f[1], dot2(f[0], f[0], 0.f)))); }
DI float half_add(float v) {
  auto r = __builtin_amdgcn_permlane32_swap(__float_as_uint(v), __float_as_uint(v), false, false);
  return __uint_as_float(r[0]) + __uint_as_float(r[1]);
}
DI float half_max(float v) {
  auto r = __builtin_amdgcn_permlane32_swap(__float_as_uint(v), __float_as_uint(v), false, false);
  return fmaxf(__uint_as_float(r[0]), __uint_as_float(r[1]));
}
template <int CTRL> DI float dpp_max(float v) {
  return fmaxf(v, __uint_as_float(__builtin_amdgcn_update_dpp(0u, __float_as_uint(v), CTRL, 0xF, 0xF, true)));
}
DI float wave_max(float v) {
  v = dpp_max<0xB1>(v); v = dpp_max<0x4E>(v); v = dpp_max<0x141>(v); v = dpp_max<0x140>(v);
  auto r = __builtin_amdgcn_permlane16_swap(__float_as_uint(v), __float_as_uint(v), false, false);
  return half_max(fmaxf(__uint_as_float(r[0]), __uint_as_float(r[1])));
}
DI float max8(const f32x16& p, int o) {
  return fmaxf(fmaxf(fmaxf(p[o], p[o + 1]), fmaxf(p[o + 2], p[o + 3])), fmaxf(fmaxf(p[o + 4], p[o + 5]), fmaxf(p[o + 6], p[o + 7])));
}

DI u32x4 lds_r128(LAS char* l, unsigned off) { return *(LAS const u32x4*)(l + off); }
DI void lds_w128(LAS char* l, unsigned off, u32x4 v) { *(LAS u32x4*)(l + off) = v; }
DI u32x4 lds_tr2(LAS char* l, unsigned off0, unsigned off1) {
  s16x4 a = __builtin_amdgcn_ds_read_tr16_b64_v4i16((LAS s16x4*)(l + off0));
  s16x4 b = __builtin_amdgcn_ds_read_tr16_b64_v4i16((LAS s16x4*)(l + off1));
  return __builtin_bit_cast(u32x4, __builtin_shufflevector(a, b, 0, 1, 2, 3, 4, 5, 6, 7));
}
DI void dma16(rsrc_t r, unsigned voff, unsigned soff, unsigned lds_dst) {
  unsigned keep;
  asm volatile("s_mov_b32 %0, m0\n\ts_mov_b32 m0, %1\n\ts_nop 0\n\tbuffer_load_dwordx4 %2, %3, %4 offen lds\n\ts_mov_b32 m0, %0"
               : "=&s"(keep) : "s"(lds_dst), "v"(voff), "s"(r), "s"(soff) : "memory");
}
DI u32x4 ws_load(rsrc_t r, unsigned off) { return __builtin_amdgcn_raw_buffer_load_b128(r, off, 0, SC1); }
DI void ws_store(rsrc_t r, unsigned off, u32x4 v) { __builtin_amdgcn_raw_buffer_store_b128(v, r, off, 0, SC1); }
DI void drain() { asm volatile("s_waitcnt vmcnt(0)" ::: "memory"); }
DI void set_flag(rsrc_t r, unsigned off, u32x2 ep) { __builtin_amdgcn_raw_buffer_store_b64(ep, r, off, 0, SC1); }
DI void wait_flags(rsrc_t r, unsigned off, u32x2 ep) {
#pragma clang loop unroll(disable)
  for (unsigned spins = 0; spins < SPIN_CAP; ++spins) {
    u32x2 v = __builtin_amdgcn_raw_buffer_load_b64(r, off, 0, SC1);
    if (__all(v[0] == ep[0] && v[1] == ep[1])) break;
    __builtin_amdgcn_s_sleep(2);
    asm volatile("" ::: "memory");
  }
  asm volatile("" ::: "memory");
}
#define WAIT_BAR(N) asm volatile("s_waitcnt vmcnt(" #N ") lgkmcnt(0)\n\ts_barrier" ::: "memory")
#define LDS_BAR() asm volatile("s_waitcnt lgkmcnt(0)\n\ts_barrier" ::: "memory")

DI f32x16 features(u32x4 wp, u32x4 qf) {
  f32x16 p = MFMA(wp, qf, f32x16{});
  const float m0 = half_max(max8(p, 0)), m1 = half_max(max8(p, 8));
#pragma unroll
  for (int i = 0; i < 8; ++i) { p[i] = __builtin_amdgcn_exp2f(p[i] - m0); p[8 + i] = __builtin_amdgcn_exp2f(p[8 + i] - m1); }
  const float s0 = half_add(((p[0] + p[1]) + (p[2] + p[3])) + ((p[4] + p[5]) + (p[6] + p[7])));
  const float s1 = half_add(((p[8] + p[9]) + (p[10] + p[11])) + ((p[12] + p[13]) + (p[14] + p[15])));
  const float r0 = __builtin_amdgcn_rcpf(s0), r1 = __builtin_amdgcn_rcpf(s1);
#pragma unroll
  for (int i = 0; i < 8; ++i) { p[i] *= r0; p[8 + i] *= r1; }
  return p;
}

#define STR_(x) #x
#define STR(x) STR_(x)
#define EX(r) "v_exp_f32 v" #r ", v" #r "\n\t"
#define QK(s0, s1, k0, k1, q0, q1, c0, c1) "v_mfma_f32_32x32x16_bf16 v[" #s0 ":" #s1 "], v[" #k0 ":" #k1 "], v[" #q0 ":" #q1 "], v[" #c0 ":" #c1 "]\n\t"
#define PV(o0, o1, a0, a1, p0, p1) "v_smfmac_f32_32x32x32_bf16 v[" #o0 ":" #o1 "], v[" #a0 ":" #a1 "], v[" #p0 ":" #p1 "], v248\n\t"
#define LOADS(k0, k1, a0, a1, b0, b1)                                                     \
  "s_add_u32 %[t], %[t], 0x800\n\t"                                                       \
  "s_and_b32 %[t], %[t], 0xffff\n\t"                                                      \
  "s_mov_b64 exec, %[msk]\n\t"                                                            \
  "buffer_load_dwordx4 v[" #a0 ":" #a1 "], v250, %[kv], %[t] offen offset:1024 sc1\n\t"   \
  "buffer_load_dwordx4 v[" #b0 ":" #b1 "], v250, %[kv], %[t] offen offset:1536 sc1\n\t"   \
  "s_mov_b64 exec, -1\n\t"                                                                \
  "buffer_load_dwordx4 v[" #k0 ":" #k1 "], v249, %[kv], %[t] offen sc1\n\t"
#define WODMA                                                                             \
  "s_mov_b32 m0, %[wl]\n\t"                                                               \
  "s_add_u32 %[wl], %[wl], 0x400\n\t"                                                     \
  "buffer_load_dwordx4 v251, %[wo], %[wg] offen lds\n\t"                                  \
  "s_add_u32 %[wg], %[wg], 0x2000\n\t"
#define ITER(ck0, ck1, ca0, ca1, cb0, cb1, pk0, pk1, pa0, pa1, pb0, pb1, nk0, nk1, EXTRA) \
  EX(128) EX(129) PV(112, 127, pb0, pb1, 152, 159) EX(130) EX(131)                        \
  LOADS(pk0, pk1, pa0, pa1, pb0, pb1) EXTRA                                               \
  EX(132) EX(133) QK(144, 159, ck0, ck1, 196, 199, 176, 191) EX(134) EX(135) EX(136) EX(137) EX(138) \
  PV(96, 111, ca0, ca1, 128, 135) EX(139) EX(140) EX(141) EX(142) EX(143)                 \
  EX(144) EX(145) PV(96, 111, cb0, cb1, 136, 143) EX(146) EX(147) EX(148) EX(149)         \
  "s_waitcnt vmcnt(7)\n\t"                                                                \
  QK(128, 143, nk0, nk1, 192, 195, 160, 175) EX(150) EX(151) EX(152) EX(153) EX(154)      \
  PV(112, 127, ca0, ca1, 144, 151) EX(155) EX(156) EX(157) EX(158) EX(159)

struct Attn {
  f32x16 o0, o1, sa, sb;
  u32x16 kr, vr0, vr1;
};
DI void attn_segment(Attn& a, const f32x16& c0, const f32x16& c1, const u32x8& q, unsigned idx, unsigned koff, unsigned voff,
                     unsigned woff, rsrc_t kv, rsrc_t wo, unsigned& t, unsigned wl, unsigned wg) {
  unsigned keep, cnt;
  unsigned long long msk = 0x0000FFFF0000FFFFull;
  asm volatile(
      "s_mov_b32 %[keep], m0\n\t"
      "s_mov_b32 %[cnt], 2\n\t"
      "s_nop 15\n\t"
      "1:\n\t"
      ITER(200, 203, 216, 219, 220, 223, 212, 215, 240, 243, 244, 247, 204, 207, WODMA)
      ITER(204, 207, 224, 227, 228, 231, 200, 203, 216, 219, 220, 223, 208, 211, "")
      ITER(208, 211, 232, 235, 236, 239, 204, 207, 224, 227, 228, 231, 212, 215, WODMA)
      ITER(212, 215, 240, 243, 244, 247, 208, 211, 232, 235, 236, 239, 200, 203, "")
      "s_sub_u32 %[cnt], %[cnt], 1\n\t"
      "s_cmp_lg_u32 %[cnt], 0\n\t"
      "s_cbranch_scc1 1b\n\t"
      "s_mov_b32 m0, %[keep]\n\t"
      "s_waitcnt vmcnt(0)\n\t"
      "s_nop 15"
      : "+{v[96:111]}"(a.o0), "+{v[112:127]}"(a.o1), "+{v[128:143]}"(a.sa), "+{v[144:159]}"(a.sb), "+{v[200:215]}"(a.kr),
        "+{v[216:231]}"(a.vr0), "+{v[232:247]}"(a.vr1), [t] "+s"(t), [wl] "+s"(wl), [wg] "+s"(wg), [keep] "=&s"(keep), [cnt] "=&s"(cnt)
      : "{v[160:175]}"(c0), "{v[176:191]}"(c1), "{v[192:199]}"(q), "{v248}"(idx), "{v249}"(koff), "{v250}"(voff), "{v251}"(woff),
        [kv] "s"(kv), [wo] "s"(wo), [msk] "s"(msk)
      : "memory", "scc");
}

#define STAMP(i) do { } while (0)

template <bool B> struct Role { static constexpr bool value = B; };
template <bool HEAVY, bool GATE>
DI void gemm_chunk(LAS char* lds, const unsigned (&xa)[4], unsigned xo, unsigned wa, unsigned gwa, bool gate_hi, f32x16& acc0,
                   f32x16& acc1, float& gacc) {
#pragma unroll
  for (int s = 0; s < 4; ++s) {
    const u32x4 wf = lds_tr2(lds, wa + s * 2048, wa + s * 2048 + 512);
    const u32x4 x0 = lds_r128(lds, xa[s] + xo);
    u32x4 xg = x0;
    if constexpr (HEAVY) {
      const u32x4 x1 = lds_r128(lds, xa[s] + xo + 4096);
      acc0 = MFMA(wf, x0, acc0);
      acc1 = MFMA(wf, x1, acc1);
      if constexpr (GATE) xg = gate_hi ? x1 : x0;
    } else {
      acc0 = MFMA(x0, wf, acc0);
    }
    if constexpr (GATE) {
      const u32x4 gw = lds_r128(lds, gwa + s * 32);
      gacc = dot2(xg[3], gw[3], dot2(xg[2], gw[2], dot2(xg[1], gw[1], dot2(xg[0], gw[0], gacc))));
    }
  }
}

__global__ __launch_bounds__(512, 2) void gated_mix_kernel(const char* __restrict__ x, const char* __restrict__ wqa,
                                                           const char* __restrict__ woa, const char* __restrict__ wqb,
                                                           const char* __restrict__ wphi, const char* __restrict__ wob,
                                                           const char* __restrict__ gatew, char* __restrict__ out,
                                                           char* __restrict__ ws) {
  __shared__ __attribute__((aligned(16))) char lds_raw[L_BYTES];
  LAS char* lds = (LAS char*)lds_raw;
  const unsigned lds0 = (unsigned)(uintptr_t)lds;
  const int tid = threadIdx.x, lane = tid & 63, w = __builtin_amdgcn_readfirstlane(tid >> 6);
  const int g = blockIdx.x & 7, tb = blockIdx.x >> 3;
  const int r32 = lane & 31, h = lane >> 5;
  const bool heavy = w < 4;

  const rsrc_t rx = __builtin_amdgcn_make_buffer_rsrc((void*)x, 0, 2048 * 1024, 0x00020000);
  const rsrc_t rwa = __builtin_amdgcn_make_buffer_rsrc((void*)wqa, 0, 512 * 3072, 0x00020000);
  const rsrc_t rwb = __builtin_amdgcn_make_buffer_rsrc((void*)wqb, 0, 512 * 3072, 0x00020000);
  const rsrc_t rws = __builtin_amdgcn_make_buffer_rsrc((void*)ws, 0, WS_END, 0x00020000);
  STAMP(0);

  u32x2 epv;
  asm volatile("buffer_load_dwordx2 %0, off, %1, 0 sc1" : "=v"(epv) : "s"(rws) : "memory");
  if (w < 2) dma16(__builtin_amdgcn_make_buffer_rsrc((void*)wphi, 0, 32 * 512, 0x00020000), lane * 16, g * 2048 + w * 1024, lds0 + L_WPHI + w * 1024);
  if (w == 2) dma16(__builtin_amdgcn_make_buffer_rsrc((void*)gatew, 0, 1024, 0x00020000), lane * 16, 0, lds0 + L_GW);
  const int drow = 8 * w + (lane >> 3), dcp = lane & 7;
  const unsigned xv = (unsigned)(64 * tb + drow) * 1024 + ((dcp ^ ((drow >> 1) & 7)) * 16);
  const unsigned wv = (unsigned)drow * 3072 + g * 128 + ((dcp ^ ((drow & 2) << 1)) * 16);
  auto dma_x = [&](int c) { dma16(rx, xv, c * 128, lds0 + L_X + c * 8192 + w * 1024); };
  auto dma_w = [&](rsrc_t r, int c, unsigned slot) {
#pragma unroll
    for (int sg = 0; sg < 3; ++sg) dma16(r, wv, c * (64 * 3072) + sg * 1024, lds0 + L_RING + slot + sg * 8192 + w * 1024);
  };
  dma_x(0); dma_w(rwa, 0, 0);
  dma_x(1); dma_w(rwa, 1, SLOT);
  dma_x(2); dma_w(rwa, 2, 2 * SLOT);
#pragma unroll
  for (int c = 3; c < 8; ++c) dma_x(c);

  const int pr = w & 1, seg = heavy ? (w >> 1) : 2, ltt = (w >> 1) & 1;
  const int ttf = heavy ? 1 - ltt : ltt;
  unsigned xa[4];
#pragma unroll
  for (int s = 0; s < 4; ++s) xa[s] = L_X + (heavy ? 0 : ltt * 4096) + r32 * 128 + (((2 * s + h) ^ ((r32 >> 1) & 7)) * 16);
  const int tq = (lane & 15) >> 2, tp = lane & 3, tnb = (lane >> 4) & 1;
  const unsigned wbase = L_RING + seg * 8192 + (8 * h + tq) * 128 + (((2 * pr + tnb) ^ (tq & 2)) * 32) + tp * 8;
  f32x16 acc0 = {}, acc1 = {};
  float gacc = 0.f;
  unsigned slot = 0;
  auto next_slot = [](unsigned s) { return s == 2 * SLOT ? 0u : s + SLOT; };
  auto prev_slot = [](unsigned s) { return s == 0 ? 2 * SLOT : s - SLOT; };

  auto phase_a = [&](auto role) {
#pragma clang loop unroll(disable)
    for (int c = 0; c < 8; ++c) {
      asm volatile("" : "+s"(c));
      if (c >= 3) WAIT_BAR(3);
      else if (c == 2) WAIT_BAR(8);
      else if (c == 1) WAIT_BAR(9);
      else WAIT_BAR(13);
      if (c >= 1) dma_w(c < 6 ? rwa : rwb, c < 6 ? c + 2 : c - 6, prev_slot(slot));
      gemm_chunk<decltype(role)::value, false>(lds, xa, c * 8192, wbase + slot, 0, false, acc0, acc1, gacc);
      slot = next_slot(slot);
    }
  };
  if (heavy) phase_a(Role<true>{}); else phase_a(Role<false>{});
  asm volatile("" : "+v"(epv));
  u32x2 ep;
  {
    const unsigned long long e = (((unsigned long long)__builtin_amdgcn_readfirstlane(epv[1]) << 32) | __builtin_amdgcn_readfirstlane(epv[0])) + 1ull;
    ep = u32x2{(unsigned)e, (unsigned)(e >> 32)};
  }
  STAMP(2);

  const unsigned kvh = WS_KV + (unsigned)(4 * g) * 131072 + (unsigned)(2 * tb) * 2048;
  if (w < 2) {
    const float c = 0.25f * LOG2E;
    const unsigned qb = L_Q + 2 * pr * 2048 + lane * 16;
    lds_w128(lds, qb, pack8s<0>(acc0, c)); lds_w128(lds, qb + 1024, pack8s<0>(acc1, c));
    lds_w128(lds, qb + 2048, pack8s<8>(acc0, c)); lds_w128(lds, qb + 3072, pack8s<8>(acc1, c));
  } else if (w < 4) {
    const int sw = (r32 & 19) | ((r32 & 4) << 1) | ((r32 & 8) >> 1);
    const unsigned kb = kvh + 2 * pr * 131072 + h * 512 + sw * 16;
    const u32x4 k00 = pack8<0>(acc0), k01 = pack8<0>(acc1), k10 = pack8<8>(acc0), k11 = pack8<8>(acc1);
    ws_store(rws, kb, k00); ws_store(rws, kb + 2048, k01);
    ws_store(rws, kb + 131072, k10); ws_store(rws, kb + 131072 + 2048, k11);
    const float n0 = wave_max(fmaxf(half_add(sumsq8(k00)), half_add(sumsq8(k01))));
    const float n1 = wave_max(fmaxf(half_add(sumsq8(k10)), half_add(sumsq8(k11))));
    __builtin_amdgcn_raw_buffer_store_b32(__float_as_uint(h ? n1 : n0), rws,
                                          r32 == 0 ? WS_KN + (unsigned)((4 * g + 2 * pr + h) * 32 + tb) * 4 : OOB, 0, SC1);
  } else {
    const unsigned vb = kvh + (2 * pr + (r32 >> 4)) * 131072 + ltt * 2048 + 1024 + h * 256 + (r32 & 15) * 16;
    ws_store(rws, vb, pack8<0>(acc0)); ws_store(rws, vb + 512, pack8<8>(acc0));
  }
  acc0 = f32x16{}; acc1 = f32x16{};

  auto phase_b = [&](auto role) {
#pragma clang loop unroll(disable)
    for (int c = 0; c < 8; ++c) {
      asm volatile("" : "+s"(c));
      if (c == 7) WAIT_BAR(0);
      else if (c > 0 || w < 2) WAIT_BAR(3);
      else if (decltype(role)::value) WAIT_BAR(8);
      else WAIT_BAR(5);
      if (c < 6) dma_w(rwb, c + 2, prev_slot(slot));
      if (c == 1 && tid == 0) set_flag(rws, WS_FKV + (g * 32 + tb) * 8, ep);
      gemm_chunk<decltype(role)::value, true>(lds, xa, c * 8192, wbase + slot, L_GW + c * 128 + h * 16, ttf == 1, acc0, acc1, gacc);
      slot = next_slot(slot);
    }
  };
  if (heavy) phase_b(Role<true>{}); else phase_b(Role<false>{});
  const float gate = __builtin_amdgcn_rcpf(1.f + __builtin_amdgcn_exp2f(-LOG2E * half_add(gacc)));
  STAMP(3);

  u32x4 wp0, wp1;
  if (heavy) {
    const unsigned wpa = L_WPHI + 2 * pr * 512 + (4 * h + tq) * 32 + tp * 8, sg = tnb ? 0x80008000u : 0u;
    wp0 = lds_tr2(lds, wpa, wpa + 256) ^ sg;
    wp1 = lds_tr2(lds, wpa + 512, wpa + 768) ^ sg;
  } else {
    const unsigned vb = L_VB + ((2 * pr + (r32 >> 4)) * 2 + ltt) * 1024 + h * 256 + (r32 & 15) * 16;
    lds_w128(lds, vb, pack8<0>(acc0)); lds_w128(lds, vb + 512, pack8<8>(acc0));
  }
  LDS_BAR();

  if (heavy) {
    const f32x16 f00 = features(wp0, pack8s<0>(acc0, LOG2E)), f01 = features(wp0, pack8s<0>(acc1, LOG2E));
    const f32x16 f10 = features(wp1, pack8s<8>(acc0, LOG2E)), f11 = features(wp1, pack8s<8>(acc1, LOG2E));
    if (w < 2) {
      const unsigned pb = L_PQ + 2 * pr * 4096 + lane * 16;
      lds_w128(lds, pb, pack8<0>(f00)); lds_w128(lds, pb + 1024, pack8<8>(f00));
      lds_w128(lds, pb + 2048, pack8<0>(f01)); lds_w128(lds, pb + 3072, pack8<8>(f01));
      lds_w128(lds, pb + 4096, pack8<0>(f10)); lds_w128(lds, pb + 5120, pack8<8>(f10));
      lds_w128(lds, pb + 6144, pack8<0>(f11)); lds_w128(lds, pb + 7168, pack8<8>(f11));
      asm volatile("s_waitcnt lgkmcnt(0)" ::: "memory");
      if (lane == 0) set_flag(rws, WS_FPQ + (blockIdx.x * 2 + pr) * 8, ep);
    } else {
      u32x4 id0, id1;
#pragma unroll
      for (int j = 0; j < 4; ++j) {
        const int f = (2 * j & 3) + 8 * (j >> 1) + 4 * h;
        id0[j] = (r32 == f ? 0x3F80u : 0u) | (r32 == f + 1 ? 0x3F800000u : 0u);
        id1[j] = (r32 == f + 16 ? 0x3F80u : 0u) | (r32 == f + 17 ? 0x3F800000u : 0u);
      }
      auto kv_part = [&](const f32x16& fa, const f32x16& fb, int hd) {
        const f32x16 ta = MFMA(pack8<8>(fa), id1, MFMA(pack8<0>(fa), id0, f32x16{}));
        const f32x16 tc = MFMA(pack8<8>(fb), id1, MFMA(pack8<0>(fb), id0, f32x16{}));
        const unsigned vb = L_VB + hd * 2048 + h * 256 + (r32 & 15) * 16;
        u32x4 v[4];
#pragma unroll
        for (int i = 0; i < 4; ++i) { v[i] = lds_r128(lds, vb + i * 512); if (r32 >= 16) v[i] = u32x4{ONES, ONES, ONES, ONES}; }
        f32x16 kp = MFMA(pack8<0>(ta), v[0], f32x16{});
        kp = MFMA(pack8<8>(ta), v[1], kp); kp = MFMA(pack8<0>(tc), v[2], kp); kp = MFMA(pack8<8>(tc), v[3], kp);
        const unsigned pb = WS_KP + (unsigned)(((4 * g + hd) * 8 + h) * 32 + tb) * 512 + r32 * 16;
#pragma unroll
        for (int q4 = 0; q4 < 4; ++q4)
          ws_store(rws, pb + q4 * 32768, __builtin_bit_cast(u32x4, f32x4{kp[4 * q4], kp[4 * q4 + 1], kp[4 * q4 + 2], kp[4 * q4 + 3]}));
      };
      kv_part(f00, f01, 2 * pr);
      kv_part(f10, f11, 2 * pr + 1);
    }
  }
  STAMP(4);

  const int hh = w & 3, kh = w >> 2;
  const u32x4 q0 = lds_r128(lds, L_Q + hh * 2048 + lane * 16), q1 = lds_r128(lds, L_Q + hh * 2048 + 1024 + lane * 16);
  wait_flags(rws, WS_FKV + (g * 32 + r32) * 8, ep);
  if (w == 2 || w == 3) { drain(); if (lane == 0) set_flag(rws, WS_FKP + ((g * 2 + pr) * 32 + tb) * 8, ep); }
  STAMP(5);
  const unsigned kvb = WS_KV + (unsigned)(4 * g + hh) * 131072;
  const unsigned koff = kvb + kh * 65536 + lane * 16, voff = kvb + kh * 65536 + h * 256 + (lane & 15) * 16;
  const float kn = __uint_as_float(__builtin_amdgcn_raw_buffer_load_b32(rws, WS_KN + ((4 * g + hh) * 32 + r32) * 4, 0, SC1));
  Attn at;
  unsigned tcur = (unsigned)tb * 2048;
  {
    u32x4 kf[3], va[3], vb[3];
#pragma unroll
    for (int i = 0; i < 3; ++i) {
      const unsigned to = (tcur + i * 2048) & 0xFFFF;
      kf[i] = ws_load(rws, koff + to); va[i] = ws_load(rws, voff + to + 1024); vb[i] = ws_load(rws, voff + to + 1536);
    }
    const u32x4 one = {ONES, ONES, ONES, ONES};
    if (lane & 16) { va[0] = va[1] = va[2] = vb[0] = vb[1] = vb[2] = one; }
#pragma unroll
    for (int j = 0; j < 4; ++j) {
      at.kr[j] = kf[0][j]; at.kr[4 + j] = kf[1][j]; at.kr[8 + j] = kf[2][j]; at.kr[12 + j] = 0;
      at.vr0[j] = va[0][j]; at.vr0[4 + j] = vb[0][j]; at.vr0[8 + j] = va[1][j]; at.vr0[12 + j] = vb[1][j];
      at.vr1[j] = va[2][j]; at.vr1[4 + j] = vb[2][j]; at.vr1[8 + j] = ONES; at.vr1[12 + j] = ONES;
    }
    tcur = (tcur + 2 * 2048) & 0xFFFF;
  }
  const float kmax2 = wave_max(kn);
  float m0 = __builtin_amdgcn_sqrtf(half_add(sumsq8(q0)) * kmax2), m1 = __builtin_amdgcn_sqrtf(half_add(sumsq8(q1)) * kmax2);
  if (!__all(m0 <= SAFE_SHIFT && m1 <= SAFE_SHIFT)) {
    m0 = m1 = -__builtin_inff();
#pragma clang loop unroll(disable)
    for (int t = 0; t < 64; ++t) {
      const u32x4 kf = ws_load(rws, kvb + t * 2048 + lane * 16);
      const f32x16 s0 = MFMA(kf, q0, f32x16{}), s1 = MFMA(kf, q1, f32x16{});
      m0 = fmaxf(m0, fmaxf(max8(s0, 0), max8(s0, 8))); m1 = fmaxf(m1, fmaxf(max8(s1, 0), max8(s1, 8)));
    }
    m0 = half_max(m0); m1 = half_max(m1);
  }
  f32x16 c0, c1;
#pragma unroll
  for (int i = 0; i < 16; ++i) { c0[i] = -m0; c1[i] = -m1; }
  at.o0 = f32x16{}; at.o1 = f32x16{}; at.sb = f32x16{};
  at.sa = MFMA((u32x4{at.kr[0], at.kr[1], at.kr[2], at.kr[3]}), q0, c0);
  const u32x8 qq = __builtin_shufflevector(q0, q1, 0, 1, 2, 3, 4, 5, 6, 7);
  const rsrc_t roa = __builtin_amdgcn_make_buffer_rsrc((void*)woa, 0, 512 * 1024, 0x00020000);
  const rsrc_t rob = __builtin_amdgcn_make_buffer_rsrc((void*)wob, 0, 512 * 1024, 0x00020000);
  const unsigned woff = (unsigned)(64 * w + (lane >> 3)) * 1024 + g * 128 + ((dcp ^ ((lane >> 2) & 4)) * 16);
  const int hd = 4 * g + hh;
  STAMP(6);

#pragma clang loop unroll(disable)
  for (int sgm = 0; sgm < 4; ++sgm) {
    asm volatile("" : "+s"(sgm));
    attn_segment(at, c0, c1, qq, 0xDDDDDDDDu, koff, voff, woff, rws, sgm < 2 ? rob : roa, tcur,
                 lds0 + (sgm >> 1) * 65536 + w * 8192 + (sgm & 1) * 4096, (sgm & 1) * 32768);
    if (sgm == 0 && w == 3) {
      const int rh = tb & 3, sl = tb >> 2;
      wait_flags(rws, WS_FKP + ((g * 2 + (rh >> 1)) * 32 + r32) * 8, ep);
      const unsigned pb = WS_KP + (unsigned)((4 * g + rh) * 8 + sl) * 16384 + lane * 16;
      f32x4 s = {};
#pragma unroll
      for (int b = 0; b < 2; ++b) {
        u32x4 pv[8];
#pragma unroll
        for (int i = 0; i < 8; ++i) pv[i] = ws_load(rws, pb + (b * 8 + i) * 1024);
#pragma unroll
        for (int i = 0; i < 8; ++i) s += __builtin_bit_cast(f32x4, pv[i]);
      }
      s = f32x4{half_add(s[0]), half_add(s[1]), half_add(s[2]), half_add(s[3])};
      ws_store(rws, h == 0 ? WS_KR + (unsigned)((4 * g + rh) * 8 + sl) * 512 + r32 * 16 : OOB, __builtin_bit_cast(u32x4, s));
      drain();
      if (lane == 0) set_flag(rws, WS_FKR + ((4 * g + rh) * 8 + sl) * 8, ep);
    }
    if (sgm == 1) {
      wait_flags(rws, h ? WS_FPQ + (blockIdx.x * 2 + (hh >> 1)) * 8 : WS_FKR + (hd * 8 + (lane & 7)) * 8, ep);
      f32x16 kv;
#pragma unroll
      for (int q4 = 0; q4 < 4; ++q4) {
        const f32x4 v = __builtin_bit_cast(f32x4, ws_load(rws, WS_KR + (unsigned)(hd * 8 + q4 * 2 + h) * 512 + r32 * 16));
        kv[4 * q4] = v[0]; kv[4 * q4 + 1] = v[1]; kv[4 * q4 + 2] = v[2]; kv[4 * q4 + 3] = v[3];
      }
      const unsigned pb = L_PQ + (hh * 2 + ttf) * 2048 + lane * 16;
      const f32x16 nd = MFMA(pack8<8>(kv), lds_r128(lds, pb + 1024), MFMA(pack8<0>(kv), lds_r128(lds, pb), f32x16{}));
      const float sc = (1.f - gate) * __builtin_amdgcn_rcpf(nd[8] + 1e-6f);
      ws_store(rws, WS_OB + (unsigned)((tb * 2 + ttf) * 32 + hd) * 1024 + lane * 16, pack8s<0>(nd, sc));
      drain();
      if (lane == 0) set_flag(rws, WS_FOB + ((tb * 2 + ttf) * 32 + hd) * 8, ep);
    }
    STAMP(7 + sgm);
  }
  {
    const u32x4 vl = {at.vr1[12], at.vr1[13], at.vr1[14], at.vr1[15]};
    typedef __bf16 bf16x16 __attribute__((ext_vector_type(16)));
    const f32x8 ph = __builtin_shufflevector(at.sb, at.sb, 8, 9, 10, 11, 12, 13, 14, 15);
    at.o1 = __builtin_amdgcn_smfmac_f32_32x32x32_bf16(__builtin_bit_cast(bf16x8, vl), __builtin_bit_cast(bf16x16, ph), at.o1, (int)0xDDDDDDDDu, 0, 0);
  }

  {
    const f32x16 mine = ttf ? at.o1 : at.o0, other = ttf ? at.o0 : at.o1;
    const unsigned so = L_PQ + (hh * 2 + ttf) * 2048 + lane * 16, sl = L_VB + (hh * 2 + ttf) * 1024 + lane * 4;
    lds_w128(lds, so, __builtin_bit_cast(u32x4, f32x4{other[0], other[1], other[2], other[3]}));
    lds_w128(lds, so + 1024, __builtin_bit_cast(u32x4, f32x4{other[4], other[5], other[6], other[7]}));
    *(LAS float*)(lds + sl) = other[8];
    LDS_BAR();
    const unsigned ro = L_PQ + (hh * 2 + 1 - ttf) * 2048 + lane * 16, rl = L_VB + (hh * 2 + 1 - ttf) * 1024 + lane * 4;
    const f32x4 a = __builtin_bit_cast(f32x4, lds_r128(lds, ro)), b = __builtin_bit_cast(f32x4, lds_r128(lds, ro + 1024));
    const float l = mine[8] + *(LAS const float*)(lds + rl);
    f32x16 o = mine;
#pragma unroll
    for (int i = 0; i < 4; ++i) { o[i] += a[i]; o[4 + i] += b[i]; }
    ws_store(rws, WS_OA + (unsigned)((tb * 2 + ttf) * 32 + hd) * 1024 + lane * 16, pack8s<0>(o, gate * __builtin_amdgcn_rcpf(l)));
    drain();
    if (lane == 0) set_flag(rws, WS_FOA + ((tb * 2 + ttf) * 32 + hd) * 8, ep);
  }
  STAMP(11);

  const int ott = w & 1, kq = w >> 1;
  const unsigned wo0 = (unsigned)(kq * 128 + 4 * h + tq) * 128 + ((tnb ^ (tq & 2)) * 32) + tp * 8, wo1 = wo0 ^ 64;
  const unsigned ob = (unsigned)((tb * 2 + ott) * 32 + kq * 8);
  f32x16 d0 = {}, d1 = {};
  {
    wait_flags(rws, WS_FOB + (ob + (lane & 7)) * 8, ep);
    u32x4 of[8];
#pragma unroll
    for (int i = 0; i < 8; ++i) of[i] = ws_load(rws, WS_OB + (ob + i) * 1024 + lane * 16);
    __builtin_amdgcn_sched_barrier(0);
#pragma unroll
    for (int i = 0; i < 8; ++i) {
      d0 = MFMA(lds_tr2(lds, L_WOB + wo0 + i * 2048, L_WOB + wo0 + i * 2048 + 1024), of[i], d0);
      d1 = MFMA(lds_tr2(lds, L_WOB + wo1 + i * 2048, L_WOB + wo1 + i * 2048 + 1024), of[i], d1);
    }
  }
  u32x4 wf0[8], wf1[8];
#pragma unroll
  for (int i = 0; i < 8; ++i) {
    wf0[i] = lds_tr2(lds, L_WOA + wo0 + i * 2048, L_WOA + wo0 + i * 2048 + 1024);
    wf1[i] = lds_tr2(lds, L_WOA + wo1 + i * 2048, L_WOA + wo1 + i * 2048 + 1024);
  }
  LDS_BAR();
  STAMP(12);
  {
    wait_flags(rws, WS_FOA + (ob + (lane & 7)) * 8, ep);
    STAMP(13);
    u32x4 of[8];
#pragma unroll
    for (int i = 0; i < 8; ++i) of[i] = ws_load(rws, WS_OA + (ob + i) * 1024 + lane * 16);
    __builtin_amdgcn_sched_barrier(0);
#pragma unroll
    for (int i = 0; i < 8; ++i) { d0 = MFMA(wf0[i], of[i], d0); d1 = MFMA(wf1[i], of[i], d1); }
  }
  {
    const unsigned tok = ott * 32 + r32, rb = L_RED + kq * 16384 + tok * 256;
#pragma unroll
    for (int q4 = 0; q4 < 4; ++q4) {
      lds_w128(lds, rb + (((2 * q4 + h) ^ (tok & 7)) * 16), __builtin_bit_cast(u32x4, f32x4{d0[4 * q4], d0[4 * q4 + 1], d0[4 * q4 + 2], d0[4 * q4 + 3]}));
      lds_w128(lds, rb + (((8 + 2 * q4 + h) ^ (tok & 7)) * 16), __builtin_bit_cast(u32x4, f32x4{d1[4 * q4], d1[4 * q4 + 1], d1[4 * q4 + 2], d1[4 * q4 + 3]}));
    }
  }
  LDS_BAR();
  STAMP(14);
  {
    const unsigned tok = tid >> 3, c8 = tid & 7;
    const unsigned a0 = L_RED + tok * 256 + (((2 * c8) ^ (tok & 7)) * 16), a1 = a0 ^ 16;
    f32x4 lo = __builtin_bit_cast(f32x4, lds_r128(lds, a0)), hi = __builtin_bit_cast(f32x4, lds_r128(lds, a1));
#pragma unroll
    for (int k = 1; k < 4; ++k) {
      lo += __builtin_bit_cast(f32x4, lds_r128(lds, a0 + k * 16384));
      hi += __builtin_bit_cast(f32x4, lds_r128(lds, a1 + k * 16384));
    }
    *(u32x4*)(out + (size_t)(64 * tb + tok) * 1024 + g * 128 + c8 * 16) = u32x4{pk2(lo[0], lo[1]), pk2(lo[2], lo[3]), pk2(hi[0], hi[1]), pk2(hi[2], hi[3])};
  }
  if (blockIdx.x == 0 && tid == 0) set_flag(rws, WS_EPOCH, ep);
  STAMP(15);
}

}

extern "C" void kernel_launch(void* const* d_in, const int* in_sizes, int n_in, void* d_out, int out_size, void* d_ws,
                              size_t ws_size, hipStream_t stream) {
  gated_mix_kernel<<<256, 512, 0, stream>>>((const char*)d_in[0], (const char*)d_in[1], (const char*)d_in[2], (const char*)d_in[4],
                                            (const char*)d_in[5], (const char*)d_in[6], (const char*)d_in[7], (char*)d_out, (char*)d_ws);
}
```

```cpp
#include <hip/hip_runtime.h>
#include <hip/hip_bf16.h>
#include <stdint.h>

namespace {

typedef __bf16 bf16x8 __attribute__((ext_vector_type(8)));
typedef __bf16 bf16x2 __attribute__((ext_vector_type(2)));
typedef float f32x16 __attribute__((ext_vector_type(16)));
typedef float f32x8 __attribute__((ext_vector_type(8)));
typedef float f32x4 __attribute__((ext_vector_type(4)));
typedef float f32x2 __attribute__((ext_vector_type(2)));
typedef unsigned u32x16 __attribute__((ext_vector_type(16)));
typedef unsigned u32x8 __attribute__((ext_vector_type(8)));
typedef unsigned u32x4 __attribute__((ext_vector_type(4)));
typedef unsigned u32x2 __attribute__((ext_vector_type(2)));
typedef short s16x4 __attribute__((ext_vector_type(4)));
typedef __amdgpu_buffer_rsrc_t rsrc_t;
#define LAS __attribute__((address_space(3)))
#define DI __device__ __forceinline__

constexpr unsigned WS_EPOCH = 0;
constexpr unsigned WS_FKV = 0x1000;
constexpr unsigned WS_FKP = 0x2000;
constexpr unsigned WS_FKR = 0x3000;
constexpr unsigned WS_FOB = 0x4000;
constexpr unsigned WS_KN = 0xC000;
constexpr unsigned WS_KR = 0x10000;
constexpr unsigned WS_KP = 0x100000;
constexpr unsigned WS_OB = 0x500000;
constexpr unsigned WS_OA = 0x700000;
constexpr unsigned WS_ONES = 0x40000;
constexpr unsigned WS_KV = 0x900000;
constexpr unsigned WS_END = 0xD00000;

constexpr unsigned L_X = 0;
constexpr unsigned L_RING = 65536;
constexpr unsigned L_WOB = 0;
constexpr unsigned L_WOA = 65536;
constexpr unsigned L_RED = 0;
constexpr unsigned L_PQ = 131072;
constexpr unsigned L_Q = 147456;
constexpr unsigned L_WPHI = L_Q;
constexpr unsigned L_GW = L_Q + 2048;
constexpr unsigned L_DUMMY = L_Q + 3072;
constexpr unsigned L_GP = L_Q + 4096;
constexpr unsigned L_VB = 155648;
constexpr unsigned L_BYTES = 163840;

constexpr float LOG2E = 1.4426950408889634f;
constexpr float SAFE_SHIFT = 48.0f;
constexpr unsigned ONES = 0x3F803F80u;
constexpr unsigned SPIN_CAP = 1u << 18;
constexpr unsigned SENT = 0xFFFFFFFFu;
constexpr int SC1 = 16;
constexpr unsigned OOB = 0xF0000000u;

#define MFMA(a, b, c) __builtin_amdgcn_mfma_f32_32x32x16_bf16(__builtin_bit_cast(bf16x8, a), __builtin_bit_cast(bf16x8, b), c, 0, 0, 0)

DI unsigned pk2(float a, float b) { f32x2 v = {a, b}; return __builtin_bit_cast(unsigned, __builtin_convertvector(v, bf16x2)); }
template <int O> DI u32x4 pack8(const f32x16& v) {
  return u32x4{pk2(v[O], v[O + 1]), pk2(v[O + 2], v[O + 3]), pk2(v[O + 4], v[O + 5]), pk2(v[O + 6], v[O + 7])};
}
template <int O> DI u32x4 pack8s(const f32x16& v, float s) {
  return u32x4{pk2(v[O] * s, v[O + 1] * s), pk2(v[O + 2] * s, v[O + 3] * s), pk2(v[O + 4] * s, v[O + 5] * s), pk2(v[O + 6] * s, v[O + 7] * s)};
}
DI float dot2(unsigned a, unsigned b, float c) {
  return __builtin_amdgcn_fdot2_f32_bf16(__builtin_bit_cast(bf16x2, a), __builtin_bit_cast(bf16x2, b), c, false);
}
DI float sumsq8(const u32x4& f) { return dot2(f[3], f[3], dot2(f[2], f[2], dot2(f[1], f[1], dot2(f[0], f[0], 0.f)))); }
DI float half_add(float v) {
  auto r = __builtin_amdgcn_permlane32_swap(__float_as_uint(v), __float_as_uint(v), false, false);
  return __uint_as_float(r[0]) + __uint_as_float(r[1]);
}
DI float fmax2(float a, float b) { return __builtin_fmaxf(a, b); }
DI float half_max(float v) {
  auto r = __builtin_amdgcn_permlane32_swap(__float_as_uint(v), __float_as_uint(v), false, false);
  return fmax2(__uint_as_float(r[0]), __uint_as_float(r[1]));
}
template <int CTRL> DI float dpp_max(float v) {
  return fmax2(v, __uint_as_float(__builtin_amdgcn_update_dpp(0u, __float_as_uint(v), CTRL, 0xF, 0xF, true)));
}
DI float wave_max32(float v) {
  v = dpp_max<0xB1>(v); v = dpp_max<0x4E>(v); v = dpp_max<0x141>(v); v = dpp_max<0x140>(v);
  auto r = __builtin_amdgcn_permlane16_swap(__float_as_uint(v), __float_as_uint(v), false, false);
  return fmax2(__uint_as_float(r[0]), __uint_as_float(r[1]));
}
DI float max8(const f32x16& p, int o) {
  return fmax2(fmax2(fmax2(fmax2(p[o], p[o + 1]), p[o + 2]), fmax2(fmax2(p[o + 3], p[o + 4]), p[o + 5])), fmax2(p[o + 6], p[o + 7]));
}

DI u32x4 lds_r128(LAS char* l, unsigned off) { return *(LAS const u32x4*)(l + off); }
DI void lds_w128(LAS char* l, unsigned off, u32x4 v) { *(LAS u32x4*)(l + off) = v; }
DI u32x4 lds_tr2(LAS char* l, unsigned off0, unsigned off1) {
  s16x4 a = __builtin_amdgcn_ds_read_tr16_b64_v4i16((LAS s16x4*)(l + off0));
  s16x4 b = __builtin_amdgcn_ds_read_tr16_b64_v4i16((LAS s16x4*)(l + off1));
  return __builtin_bit_cast(u32x4, __builtin_shufflevector(a, b, 0, 1, 2, 3, 4, 5, 6, 7));
}
DI void dma16(rsrc_t r, unsigned voff, unsigned soff, unsigned lds_dst) {
  asm volatile("s_mov_b32 m0, %0\n\ts_nop 0\n\tbuffer_load_dwordx4 %1, %2, %3 offen lds"
               :: "s"(lds_dst), "v"(voff), "s"(r), "s"(soff) : "memory");
}
DI int lane_id() {
  int l;
  asm volatile("v_mbcnt_lo_u32_b32 %0, -1, 0\n\tv_mbcnt_hi_u32_b32 %0, -1, %0" : "=v"(l));
  return l;
}
DI u32x4 ws_load(rsrc_t r, unsigned off) { return __builtin_amdgcn_raw_buffer_load_b128(r, off, 0, SC1); }
DI void ws_store(rsrc_t r, unsigned off, u32x4 v) { __builtin_amdgcn_raw_buffer_store_b128(v, r, off, 0, SC1); }
DI void drain() { asm volatile("s_waitcnt vmcnt(0)" ::: "memory"); }
DI void set_flag(rsrc_t r, unsigned off, u32x2 ep) { __builtin_amdgcn_raw_buffer_store_b64(ep, r, off, 0, SC1); }
DI void wait_flags(rsrc_t r, unsigned off, u32x2 ep) {
#pragma clang loop unroll(disable)
  for (unsigned spins = 0; spins < SPIN_CAP; ++spins) {
    u32x2 v = __builtin_amdgcn_raw_buffer_load_b64(r, off, 0, SC1);
    if (__all(v[0] == ep[0] && v[1] == ep[1])) break;
    __builtin_amdgcn_s_sleep(2);
    asm volatile("" ::: "memory");
  }
  asm volatile("" ::: "memory");
}
#define SBAR() __builtin_amdgcn_sched_barrier(0)
#define WAIT_BAR(N) do { SBAR(); asm volatile("s_waitcnt vmcnt(" #N ") lgkmcnt(0)\n\ts_barrier" ::: "memory"); SBAR(); } while (0)
#define LDS_BAR() do { SBAR(); asm volatile("s_waitcnt lgkmcnt(0)\n\ts_barrier" ::: "memory"); SBAR(); } while (0)

#define PIN(x) asm volatile("" : "+v"(x))

DI f32x2 feat_max(const f32x16& p) {
  float a0, a1, b0, b1;
  asm("v_max3_f32 %0, %4, %5, %6\n\tv_max3_f32 %1, %12, %13, %14\n\tv_max3_f32 %2, %7, %8, %9\n\tv_max3_f32 %3, %15, %16, %17\n\t"
      "v_max3_f32 %0, %0, %10, %11\n\tv_max3_f32 %1, %1, %18, %19\n\tv_max_f32 %0, %0, %2\n\tv_max_f32 %1, %1, %3"
      : "=&v"(a0), "=&v"(a1), "=&v"(b0), "=&v"(b1)
      : "v"(p[0]), "v"(p[1]), "v"(p[2]), "v"(p[3]), "v"(p[4]), "v"(p[5]), "v"(p[6]), "v"(p[7]), "v"(p[8]), "v"(p[9]), "v"(p[10]),
        "v"(p[11]), "v"(p[12]), "v"(p[13]), "v"(p[14]), "v"(p[15]));
  return f32x2{a0, a1};
}
DI void feat_max_merge(f32x2 (&m)[4]) {
  asm("s_nop 1\n\t"
      "v_permlane32_swap_b32 %0, %1\n\tv_permlane32_swap_b32 %2, %3\n\tv_permlane32_swap_b32 %4, %5\n\tv_permlane32_swap_b32 %6, %7\n\t"
      "v_max_f32 %0, %0, %1\n\tv_max_f32 %2, %2, %3\n\tv_max_f32 %4, %4, %5\n\tv_max_f32 %6, %6, %7\n\t"
      "v_mov_b32 %1, %0\n\tv_mov_b32 %3, %2\n\tv_mov_b32 %5, %4\n\tv_mov_b32 %7, %6\n\t"
      "s_nop 1\n\t"
      "v_permlane32_swap_b32 %0, %1\n\tv_permlane32_swap_b32 %2, %3\n\tv_permlane32_swap_b32 %4, %5\n\tv_permlane32_swap_b32 %6, %7\n\t"
      "s_nop 1"
      : "+v"(m[0][0]), "+v"(m[0][1]), "+v"(m[1][0]), "+v"(m[1][1]), "+v"(m[2][0]), "+v"(m[2][1]), "+v"(m[3][0]), "+v"(m[3][1]));
}
DI void feat_exp(f32x16& p, f32x2 m) {
#pragma unroll
  for (int i = 0; i < 8; ++i) { p[i] = __builtin_amdgcn_exp2f(p[i] - m[0]); p[8 + i] = __builtin_amdgcn_exp2f(p[8 + i] - m[1]); }
  PIN(p);
}
DI u32x8 feat_norm(const f32x16& p) {
  const float s0 = ((p[0] + p[1]) + (p[2] + p[3])) + ((p[4] + p[5]) + (p[6] + p[7]));
  const float s1 = ((p[8] + p[9]) + (p[10] + p[11])) + ((p[12] + p[13]) + (p[14] + p[15]));
  const auto a = __builtin_amdgcn_permlane32_swap(__float_as_uint(s0), __float_as_uint(s1), false, false);
  const unsigned r = __float_as_uint(__builtin_amdgcn_rcpf(__uint_as_float(a[0]) + __uint_as_float(a[1])));
  const auto b = __builtin_amdgcn_permlane32_swap(r, r, false, false);
  u32x8 o = __builtin_shufflevector(pack8s<0>(p, __uint_as_float(b[0])), pack8s<8>(p, __uint_as_float(b[1])), 0, 1, 2, 3, 4, 5, 6, 7);
  PIN(o);
  return o;
}
DI u32x4 lo4(const u32x8& v) { return __builtin_shufflevector(v, v, 0, 1, 2, 3); }
DI u32x4 hi4(const u32x8& v) { return __builtin_shufflevector(v, v, 4, 5, 6, 7); }

#define EX(r) "v_exp_f32 v" #r ", v" #r "\n\t"
#define QK(s0, s1, k0, k1, q0, q1, c0, c1) "v_mfma_f32_32x32x16_bf16 v[" #s0 ":" #s1 "], v[" #k0 ":" #k1 "], v[" #q0 ":" #q1 "], v[" #c0 ":" #c1 "]\n\t"
#define PV(o0, o1, a0, a1, p0, p1) "v_smfmac_f32_32x32x32_bf16 v[" #o0 ":" #o1 "], v[" #a0 ":" #a1 "], v[" #p0 ":" #p1 "], v248\n\t"
#define KVPOL " sc1"
#define LOADS(k0, k1, a0, a1, b0, b1)                                                     \
  "s_add_u32 s76, s76, 0x800\n\t"                                                      \
  "s_and_b32 s76, s76, 0xffff\n\t"                                                     \
  "buffer_load_dwordx4 v[" #k0 ":" #k1 "], v249, s[72:75], s76 offen" KVPOL "\n\t"               \
  "buffer_load_dwordx4 v[" #a0 ":" #a1 "], v250, s[72:75], s76 offen offset:1024" KVPOL "\n\t"   \
  "buffer_load_dwordx4 v[" #b0 ":" #b1 "], v251, s[72:75], s76 offen offset:1536" KVPOL "\n\t"
#define ITER(ck0, ck1, ca0, ca1, cb0, cb1, pk0, pk1, pa0, pa1, pb0, pb1, nk0, nk1)        \
  EX(128) EX(129) PV(176, 191, pb0, pb1, 152, 159) EX(130) EX(131)                        \
  LOADS(pk0, pk1, pa0, pa1, pb0, pb1)                                                     \
  EX(132) EX(133) QK(144, 159, ck0, ck1, 196, 199, 112, 127) EX(134) EX(135) EX(136) EX(137) EX(138) \
  PV(160, 175, ca0, ca1, 128, 135) EX(139) EX(140) EX(141) EX(142) EX(143)                 \
  EX(144) EX(145) PV(160, 175, cb0, cb1, 136, 143) EX(146) EX(147) EX(148) EX(149)         \
  "s_waitcnt vmcnt(6)\n\t"                                                                \
  QK(128, 143, nk0, nk1, 192, 195, 96, 111) EX(150) EX(151) EX(152) EX(153) EX(154)      \
  PV(176, 191, ca0, ca1, 144, 151) EX(155) EX(156) EX(157) EX(158) EX(159)

struct Attn {
  f32x16 o0, o1, sa, sb;
  u32x16 kr, vr0, vr1;
};
DI unsigned long long attn_define() {
  unsigned long long entry;
  asm volatile(
      "s_getpc_b64 %0\n\t"
      "s_branch 9f\n\t"
      "1:\n\t"
      ITER(200, 203, 216, 219, 220, 223, 212, 215, 240, 243, 244, 247, 204, 207)
      ITER(204, 207, 224, 227, 228, 231, 200, 203, 216, 219, 220, 223, 208, 211)
      ITER(208, 211, 232, 235, 236, 239, 204, 207, 224, 227, 228, 231, 212, 215)
      ITER(212, 215, 240, 243, 244, 247, 208, 211, 232, 235, 236, 239, 200, 203)
      "s_sub_u32 s77, s77, 1\n\t"
      "s_cbranch_scc0 1b\n\t"
      "s_waitcnt vmcnt(0)\n\t"
      "s_nop 4\n\t"
      "s_setpc_b64 s[78:79]\n\t"
      "9:"
      : "=s"(entry));
  return entry + 4;
}
struct AttnConst { f32x16 c0, c1; u32x8 q; unsigned idx, koff, voff0, voff1; rsrc_t kv; unsigned long long entry; };
template <int PASSES> DI void attn_run(Attn& a, const AttnConst& k, unsigned& t) {
  asm volatile(
      "s_mov_b32 s77, %[n]\n\t"
      "s_swappc_b64 s[78:79], %[entry]"
      : "+{v[160:175]}"(a.o0), "+{v[176:191]}"(a.o1), "+{v[128:143]}"(a.sa), "+{v[144:159]}"(a.sb), "+{v[200:215]}"(a.kr),
        "+{v[216:231]}"(a.vr0), "+{v[232:247]}"(a.vr1), "+{s76}"(t)
      : "{v[96:111]}"(k.c0), "{v[112:127]}"(k.c1), "{v[192:199]}"(k.q), "{v248}"(k.idx), "{v249}"(k.koff), "{v250}"(k.voff0),
        "{v251}"(k.voff1), "{s[72:75]}"(k.kv), [entry] "s"(k.entry), [n] "i"(PASSES - 1)
      : "memory", "scc", "s77", "s78", "s79");
}

#define STAMP(i) do { } while (0)
#define STAMP_RT(i) do { } while (0)
#define STAMPF(i) do { } while (0)

#define G_RW(W, C, S)                                                                                 \
  "ds_read_b64_tr_b16 v[" #W "+4*" #S ":" #W "+4*" #S "+1], v8 offset:16384*(" #C "&3)+2048*" #S "\n\t"       \
  "ds_read_b64_tr_b16 v[" #W "+4*" #S "+2:" #W "+4*" #S "+3], v8 offset:16384*(" #C "&3)+2048*" #S "+512\n\t"
#define G_RX(X, C, S, T) "ds_read_b128 v[" #X "+4*" #S ":" #X "+4*" #S "+3], v[4+" #S "] offset:8192*" #C "+4096*" #T "\n\t"
#define G_RS(W, X0, X1, C, S) G_RW(W, C, S) G_RX(X0, C, S, 0) G_RX(X1, C, S, 1)
#define G_READ(W, X0, X1, C) G_RS(W, X0, X1, C, 0) G_RS(W, X0, X1, C, 1) G_RS(W, X0, X1, C, 2) G_RS(W, X0, X1, C, 3)
#define G_M(ACC, W, X, S, CIN) \
  "v_mfma_f32_32x32x16_bf16 " ACC ", v[" #W "+4*" #S ":" #W "+4*" #S "+3], v[" #X "+4*" #S ":" #X "+4*" #S "+3], " CIN "\n\t"
#define G_A0 "v[16:31]"
#define G_A1 "v[32:47]"
#define G_MMA2(W, X0, X1, S) G_M(G_A0, W, X0, S, G_A0) G_M(G_A1, W, X1, S, G_A1)
#define G_STEP(RW, RX0, RX1, C, W, X0, X1, C0, C1, D0, D1, D2, D3)                                    \
  G_M(G_A0, W, X0, 0, C0) G_RW(RW, C, 0) G_RX(RX0, C, 0, 0) G_M(G_A1, W, X1, 0, C1) G_RX(RX1, C, 0, 1) G_RW(RW, C, 1) \
  G_M(G_A0, W, X0, 1, G_A0) G_RX(RX0, C, 1, 0) G_RX(RX1, C, 1, 1) D0 G_M(G_A1, W, X1, 1, G_A1) G_RW(RW, C, 2) G_RX(RX0, C, 2, 0) \
  G_M(G_A0, W, X0, 2, G_A0) G_RX(RX1, C, 2, 1) G_RW(RW, C, 3) D1 G_M(G_A1, W, X1, 2, G_A1) G_RX(RX0, C, 3, 0) G_RX(RX1, C, 3, 1) \
  G_M(G_A0, W, X0, 3, G_A0) D2 G_M(G_A1, W, X1, 3, G_A1) D3
#define G_EVEN(C, D0, D1, D2, D3) G_STEP(48, 64, 80, C, 96, 112, 128, G_A0, G_A1, D0, D1, D2, D3)
#define G_ODD(C, D0, D1, D2, D3) G_STEP(96, 112, 128, C, 48, 64, 80, G_A0, G_A1, D0, D1, D2, D3)
#define G_FIRST(D0, D1, D2, D3) G_STEP(96, 112, 128, 1, 48, 64, 80, "0", "0", D0, D1, D2, D3)
#define G_ST ""
#define G_ST0(N) ""
#define G_STOP
#define G_STC
#define G_STP
#define G_STA
#define G_BAR(N) G_ST "s_waitcnt vmcnt(" #N ") lgkmcnt(0)\n\t" G_ST "s_barrier\n\t"
#define G_LBAR G_ST "s_waitcnt lgkmcnt(0)\n\t" G_ST "s_barrier\n\t"
#define G_EPOCH "buffer_load_dwordx2 v[2:3], off, %[ws], 0 sc1\n\t"
#define G_D0(R, N, COL)                                                                               \
  "s_xor_b32 %[tmp], %[rot], " #N "&7\n\t"                                                            \
  "s_add_u32 m0, %[ml], 16384*(" #N "&3)\n\t"                                                         \
  "s_mul_i32 %[tmp], %[tmp], 196608\n\t"                                                              \
  "buffer_load_dwordx4 v[10+2*" #COL "], %[" #R "], %[tmp] offen lds\n\t"
#define G_D1(R, COL) "buffer_load_dwordx4 v[11+2*" #COL "], %[" #R "], %[tmp] offen offset:1024 lds\n\t"
#define G_D2(R, N, COL)                                                                               \
  "s_add_u32 m0, %[ml], 16384*(" #N "&3)+8192\n\t"                                                    \
  "s_nop 0\n\t"                                                                                       \
  "buffer_load_dwordx4 v[10+2*" #COL "], %[" #R "], %[tmp] offen lds\n\t"
#define G_S0(N) G_D0(ra, N, 1), G_D1(ra, 1), G_D2(ra, N, 2), G_D1(ra, 2)
#define G_S1(N) G_D0(rb, N, 0), G_D1(rb, 0), G_D2(rb, N, 1), G_D1(rb, 1)
#define G_S2(N) G_D0(ra, N, 0), G_D1(ra, 0), G_D2(rb, N, 2), G_D1(rb, 2)
#define G_CAT(A, B, C, D) A B C D
#define G_APPLY(M, ...) M(__VA_ARGS__)
#define G_ALL(L) G_APPLY(G_CAT, L)
#define G_DX(C)                                                                                       \
  "s_add_u32 m0, %[mx], 8192*" #C "\n\t"                                                              \
  "s_xor_b32 %[tmp], %[rotx], 128*" #C "\n\t"                                                         \
  "buffer_load_dwordx4 %[xv0], %[rx], %[tmp] offen lds\n\t"                                           \
  "buffer_load_dwordx4 %[xv1], %[rx], %[tmp] offen offset:1024 lds\n\t"
typedef unsigned u32x6 __attribute__((ext_vector_type(6)));
struct Gemm { f32x16 acc0, acc1; u32x16 w, x0, x1; };
struct Src { u32x6 wv; unsigned ml, rot; rsrc_t ra, rb; };
#define G_OUTS "=&{v[16:31]}"(gm.acc0), "=&{v[32:47]}"(gm.acc1), "=&{v[96:111]}"(gm.w), "=&{v[112:127]}"(gm.x0), "=&{v[128:143]}"(gm.x1)
#define G_SRC "{v[10:15]}"(sr.wv), [ra] "s"(sr.ra), [rb] "s"(sr.rb), [ml] "s"(sr.ml), [rot] "s"(sr.rot)
#define G_CLOB "memory", "scc", "v48", "v49", "v50", "v51", "v52", "v53", "v54", "v55", "v56", "v57", "v58", "v59", "v60", "v61", "v62", "v63", \
  "v64", "v65", "v66", "v67", "v68", "v69", "v70", "v71", "v72", "v73", "v74", "v75", "v76", "v77", "v78", "v79",                              \
  "v80", "v81", "v82", "v83", "v84", "v85", "v86", "v87", "v88", "v89", "v90", "v91", "v92", "v93", "v94", "v95"
DI void gemm_stage0(int grp, Gemm& gm, u32x4 xa, unsigned wa, const Src& sr, rsrc_t rx, unsigned xv0, unsigned xv1, unsigned mx,
                    unsigned rotx, rsrc_t ws, u32x2& epv G_STP) {
  unsigned tmp;
  asm volatile(
      "s_cmp_eq_u32 %[grp], 0\n\t"
      "s_cbranch_scc1 3f\n\t"
      G_ST0(0) G_DX(0) G_ALL(G_S0(0)) G_DX(1) G_ALL(G_S0(1))
      G_BAR(6) G_DX(2) G_ALL(G_S0(2)) G_BAR(6) G_DX(3) G_ALL(G_S0(3)) G_BAR(6) G_DX(4) G_ALL(G_S0(4))
      G_BAR(6) G_DX(5) G_ALL(G_S0(5)) G_BAR(6) G_DX(6) G_ALL(G_S0(6)) G_BAR(6) G_DX(7) G_ALL(G_S0(7))
      G_BAR(6) G_ALL(G_S1(8)) G_ALL(G_S1(9)) G_BAR(8) G_ALL(G_S1(10))
      "s_branch 4f\n\t"
      "3:\n\t"
      G_ST0(0) G_EPOCH
      "s_mov_b32 %[tmp], 0\n\t"
      ".irp r,16,18,20,22,24,26,28,30,32,34,36,38,40,42,44,46\n\tv_mov_b64 v[\\r:\\r+1], 0\n\t.endr\n\t"
      G_LBAR G_READ(48, 64, 80, 0)
      "s_branch 2f\n\t"
      "1:\n\t"
      G_BAR(0) G_EVEN(0, "", "", "", "")
      "2:\n\t"
      G_LBAR G_ODD(1, "", "", "", "")
      G_LBAR G_EVEN(2, "", "", "", "")
      G_LBAR G_ODD(3, "", "", "", "")
      ".irp s,0,1,2,3\n\tv_xor_b32 v[4+\\s], 0x8000, v[4+\\s]\n\t.endr\n\t"
      "s_xor_b32 %[tmp], %[tmp], 1\n\t"
      "s_cbranch_scc1 1b\n\t"
      "4:"
      : G_OUTS, "=&{v[2:3]}"(epv), [tmp] "=&s"(tmp) G_STOP
      : "{v[4:7]}"(xa), "{v8}"(wa), G_SRC, [ws] "s"(ws), [grp] "s"(grp), [rx] "s"(rx), [xv0] "v"(xv0), [xv1] "v"(xv1), [mx] "s"(mx),
        [rotx] "s"(rotx)
      : G_CLOB G_STC);
}
DI void gemm_stage1(Gemm& gm, u32x4 xa, unsigned wa, const Src& sr, rsrc_t ws, u32x2& epv G_STP) {
  unsigned tmp;
  asm volatile(
      G_ST0(16)
      G_BAR(8) G_EPOCH G_READ(48, 64, 80, 0) G_ALL(G_S1(11))
      G_BAR(9) G_APPLY(G_FIRST, G_S1(12))
      G_BAR(9) G_APPLY(G_EVEN, 2, G_S1(13)) G_BAR(8) G_APPLY(G_ODD, 3, G_S1(14))
      G_BAR(8) G_APPLY(G_EVEN, 4, G_S1(15)) G_BAR(8) G_APPLY(G_ODD, 5, G_S2(16))
      G_BAR(8) G_APPLY(G_EVEN, 6, G_S2(17)) G_BAR(8) G_APPLY(G_ODD, 7, G_S2(18))
      : G_OUTS, "=&{v[2:3]}"(epv), [tmp] "=&s"(tmp) G_STOP
      : "{v[4:7]}"(xa), "{v8}"(wa), G_SRC, [ws] "s"(ws)
      : G_CLOB G_STC);
}
#define G_KV1(K, V, T)                                                                                \
  "buffer_load_dwordx4 v[" #K ":" #K "+3], v249, %[ws], %[" #T "] offen sc1\n\t"                       \
  "buffer_load_dwordx4 v[" #V ":" #V "+3], v250, %[ws], %[" #T "] offen offset:1024 sc1\n\t"           \
  "buffer_load_dwordx4 v[" #V "+4:" #V "+7], v251, %[ws], %[" #T "] offen offset:1536 sc1\n\t"
#define G_KN "buffer_load_dword v9, v9, %[ws], 0 offen sc1\n\t"
struct Kv0 { unsigned koff, voff0, voff1, t0, t1, t2; rsrc_t ws; };
#define G_KVOUT "=&{v[200:215]}"(at.kr), "=&{v[216:231]}"(at.vr0), "=&{v[232:247]}"(at.vr1), "+{v9}"(kn)
#define G_ZOUT "=&{v[160:175]}"(at.o0), "=&{v[176:191]}"(at.o1), "=&{v[144:159]}"(at.sb)
#define G_KVIN "{v249}"(kv.koff), "{v250}"(kv.voff0), "{v251}"(kv.voff1), [ws] "s"(kv.ws), [t0] "s"(kv.t0), [t1] "s"(kv.t1), [t2] "s"(kv.t2)
#define G_Z(L) ".irp r," L "\n\tv_mov_b64 v[\\r:\\r+1], 0\n\t.endr\n\t"
#define G_Z0 G_Z("152,154,156,158,244,246")
#define G_Z1 G_Z("160,162,164,166,168,170")
#define G_Z2 G_Z("172,174,176,178,180")
#define G_Z3 G_Z("182,184,186,188,190")
#define G_TAIL "s_waitcnt lgkmcnt(0)\n\t" G_MMA2(96, 112, 128, 0) G_MMA2(96, 112, 128, 1) G_MMA2(96, 112, 128, 2) G_MMA2(96, 112, 128, 3)
DI void gemm_stage2(Gemm& gm, u32x4 xa, unsigned wa, const Src& sr, const Kv0& kv, Attn& at, unsigned& kn G_STP) {
  unsigned tmp;
  asm volatile(
      G_ST0(32)
      G_LBAR G_READ(48, 64, 80, 0) G_ALL(G_S2(19))
      G_LBAR G_APPLY(G_FIRST, G_S2(20))
      G_LBAR G_APPLY(G_EVEN, 2, G_S2(21)) G_BAR(8) G_APPLY(G_ODD, 3, G_S2(22))
      G_BAR(8) G_APPLY(G_EVEN, 4, G_S2(23)) G_BAR(8) G_ODD(5, G_KN, G_KV1(200, 216, t0), G_KV1(204, 224, t1), G_KV1(208, 232, t2))
      G_BAR(14) G_EVEN(6, G_Z0, G_Z1, G_Z2, G_Z3) G_BAR(10) G_ODD(7, "", "", "", "")
      G_TAIL
      "s_waitcnt vmcnt(0)\n\t"
      "s_nop 10"
      : G_OUTS, G_KVOUT, G_ZOUT, [tmp] "=&s"(tmp) G_STOP
      : "{v[4:7]}"(xa), "{v8}"(wa), G_SRC, G_KVIN
      : G_CLOB G_STC);
}
DI void kv_issue(const Kv0& kv, Attn& at, unsigned& kn) {
  asm volatile(G_KN G_KV1(200, 216, t0) G_KV1(204, 224, t1) G_KV1(208, 232, t2) : G_KVOUT : G_KVIN : "memory");
}
DI void kv_landed(Attn& at, unsigned& kn) {
  asm volatile(G_Z0 G_Z1 G_Z2 G_Z3 "s_waitcnt vmcnt(0)"
               : "+{v[200:215]}"(at.kr), "+{v[216:231]}"(at.vr0), "+{v[232:247]}"(at.vr1), "+{v9}"(kn), G_ZOUT :: "memory");
}
DI void gemm_tail(Gemm& gm) {
  asm volatile(G_TAIL "s_nop 11"
               : "+{v[16:31]}"(gm.acc0), "+{v[32:47]}"(gm.acc1)
               : "{v[96:111]}"(gm.w), "{v[112:127]}"(gm.x0), "{v[128:143]}"(gm.x1));
}
DI float dot8(const u32x4& a, const u32x4& b, float c) { return dot2(a[3], b[3], dot2(a[2], b[2], dot2(a[1], b[1], dot2(a[0], b[0], c)))); }
DI void make_identity(int r32, int h, u32x4& id0, u32x4& id1) {
  const unsigned val = 0x3F80u << ((r32 & 1) * 16);
  const int hot = (((r32 >> 2) & 1) == h ? 1 : 0) << (((r32 >> 1) & 1) | ((r32 >> 2) & 6));
#pragma unroll
  for (int j = 0; j < 4; ++j) { id0[j] = val & __builtin_amdgcn_sbfe(hot, j, 1); id1[j] = val & __builtin_amdgcn_sbfe(hot, j + 4, 1); }
}
DI f32x16 transpose32(u32x4 lo, u32x4 hi, u32x4 id0, u32x4 id1) { return MFMA(hi, id1, MFMA(lo, id0, f32x16{})); }
DI u32x8 pack16(const f32x16& t) { return __builtin_shufflevector(pack8<0>(t), pack8<8>(t), 0, 1, 2, 3, 4, 5, 6, 7); }
DI float sigmoid_of(float partial) { return __builtin_amdgcn_rcpf(1.f + __builtin_amdgcn_exp2f(-LOG2E * half_add(partial))); }

#define P_TEST(I)                                                                                     \
  "v_max3_u32 v192, v[96+4*" #I "], v[97+4*" #I "], v[98+4*" #I "]\n\t"                                \
  "v_max_u32 v192, v192, v[99+4*" #I "]\n\t"                                                           \
  "v_cmp_eq_u32 vcc, -1, v192\n\t"
#define P_USE(I, N)                                                                                   \
  "s_waitcnt vmcnt(" #N ")\n\t" P_TEST(I)                                                             \
  "s_cbranch_vccnz 7" #I "f\n\t"                                                                     \
  "6" #I ":\n\t"                                                                                     \
  "v_mfma_f32_32x32x16_bf16 v[16:31], v[128+4*" #I ":131+4*" #I "], v[96+4*" #I ":99+4*" #I "], v[16:31]\n\t" \
  "v_mfma_f32_32x32x16_bf16 v[32:47], v[160+4*" #I ":163+4*" #I "], v[96+4*" #I ":99+4*" #I "], v[32:47]\n\t"
#define P_STUB(I) "7" #I ":\n\ts_call_b64 %[ret], 8f\n\ts_branch 6" #I "b\n\t"
#define P_RETRY(I, SO)                                                                                \
  P_TEST(I)                                                                                           \
  "s_cbranch_vccz 1f\n\t"                                                                             \
  "s_mov_b32 %[any], 1\n\t"                                                                           \
  "buffer_load_dwordx4 v[96+4*" #I ":99+4*" #I "], %[vo], %[ws], %[" #SO "] offen offset:1024*(" #I "&3) sc1\n\t" \
  "1:\n\t"
DI void project_a(f32x16& d0, f32x16& d1, const u32x16 (&w0)[2], const u32x16 (&w1)[2], rsrc_t ws, unsigned vo, unsigned so) {
  unsigned long long ret;
  unsigned any, spins = SPIN_CAP, so2 = so + 4096;
  asm volatile(
      "buffer_load_dwordx4 v[96:99], %[vo], %[ws], %[so] offen sc1\n\t"
      "buffer_load_dwordx4 v[100:103], %[vo], %[ws], %[so] offen offset:1024 sc1\n\t"
      "buffer_load_dwordx4 v[104:107], %[vo], %[ws], %[so] offen offset:2048 sc1\n\t"
      "buffer_load_dwordx4 v[108:111], %[vo], %[ws], %[so] offen offset:3072 sc1\n\t"
      "buffer_load_dwordx4 v[112:115], %[vo], %[ws], %[so2] offen sc1\n\t"
      "buffer_load_dwordx4 v[116:119], %[vo], %[ws], %[so2] offen offset:1024 sc1\n\t"
      "buffer_load_dwordx4 v[120:123], %[vo], %[ws], %[so2] offen offset:2048 sc1\n\t"
      "buffer_load_dwordx4 v[124:127], %[vo], %[ws], %[so2] offen offset:3072 sc1\n\t"
      P_USE(0, 7) P_USE(1, 6) P_USE(2, 5) P_USE(3, 4) P_USE(4, 3) P_USE(5, 2) P_USE(6, 1) P_USE(7, 0)
      "s_branch 9f\n\t"
      P_STUB(0) P_STUB(1) P_STUB(2) P_STUB(3) P_STUB(4) P_STUB(5) P_STUB(6) P_STUB(7)
      "8:\n\t"
      "s_waitcnt vmcnt(0)\n\t"
      "s_mov_b32 %[any], 0\n\t"
      P_RETRY(0, so) P_RETRY(1, so) P_RETRY(2, so) P_RETRY(3, so) P_RETRY(4, so2) P_RETRY(5, so2) P_RETRY(6, so2) P_RETRY(7, so2)
      "s_sub_u32 %[spins], %[spins], 1\n\t"
      "s_cselect_b32 %[any], 0, %[any]\n\t"
      "s_cmp_lg_u32 %[any], 0\n\t"
      "s_cbranch_scc1 8b\n\t"
      "s_waitcnt vmcnt(0)\n\t"
      "s_mov_b32 %[spins], 0\n\t"
      "s_setpc_b64 %[ret]\n\t"
      "9:\n\t"
      "s_nop 11"
      : "+{v[16:31]}"(d0), "+{v[32:47]}"(d1), [ret] "=&s"(ret), [any] "=&s"(any), [spins] "+s"(spins)
      : "{v[128:143]}"(w0[0]), "{v[144:159]}"(w0[1]), "{v[160:175]}"(w1[0]), "{v[176:191]}"(w1[1]), [vo] "v"(vo), [ws] "s"(ws),
        [so] "s"(so), [so2] "s"(so2)
      : "memory", "scc", "vcc", "v96", "v97", "v98", "v99", "v100", "v101", "v102", "v103", "v104", "v105", "v106", "v107", "v108", "v109",
        "v110", "v111", "v112", "v113", "v114", "v115", "v116", "v117", "v118", "v119", "v120", "v121", "v122", "v123", "v124", "v125",
        "v126", "v127", "v192");
}

#define KVST(r, o, v) ws_store(r, o, v)
#define HG 2
#define CODE_LINES 160

__global__ __launch_bounds__(512, 2) void gated_mix_kernel(const char* __restrict__ x, const char* __restrict__ wqa,
                                                           const char* __restrict__ woa, const char* __restrict__ wqb,
                                                           const char* __restrict__ wphi, const char* __restrict__ wob,
                                                           const char* __restrict__ gatew, char* __restrict__ out,
                                                           char* __restrict__ ws) {
  __shared__ __attribute__((aligned(16))) char lds_raw[L_BYTES];
  LAS char* lds = (LAS char*)lds_raw;
  const unsigned lds0 = (unsigned)(uintptr_t)lds;
  const int tid = threadIdx.x, lane = tid & 63, w = __builtin_amdgcn_readfirstlane(tid >> 6);
  const int xc = blockIdx.x & 7, in = blockIdx.x >> 3;
  const int g = HG * (xc % (8 / HG)) + in % HG, tb = (32 / HG) * (xc / (8 / HG)) + in / HG;
  const int r32 = lane & 31, h = lane >> 5;
  const int wi = w & 3, grp = w >> 2;

  const rsrc_t rwa = __builtin_amdgcn_make_buffer_rsrc((void*)wqa, 0, 512 * 3072, 0x00020000);
  const rsrc_t rwb = __builtin_amdgcn_make_buffer_rsrc((void*)wqb, 0, 512 * 3072, 0x00020000);
  const rsrc_t rws = __builtin_amdgcn_make_buffer_rsrc((void*)ws, 0, WS_END, 0x00020000);
  STAMP(0); STAMP_RT(62);

  u32x2 epv;
  const int l8 = lane >> 3, dcp = lane & 7;
  Src sr;
  const unsigned wv = (unsigned)(16 * wi + l8) * 3072 + g * 128 + ((dcp ^ ((lane >> 2) & 4)) * 16);
#pragma unroll
  for (int c = 0; c < 3; ++c) { sr.wv[2 * c] = wv + c * 1024; sr.wv[2 * c + 1] = wv + c * 1024 + 8 * 3072 - 1024; }
  sr.ml = lds0 + L_RING + wi * 2048; sr.ra = rwa; sr.rb = rwb;
  sr.rot = (in / HG + (8 / HG) * (in % HG)) & 7;
  const rsrc_t rx = __builtin_amdgcn_make_buffer_rsrc((void*)x, 0, 2048 * 1024, 0x00020000);
  const unsigned xv0 = (unsigned)(64 * tb + 16 * wi + l8) * 1024 + ((dcp ^ (l8 >> 1)) * 16), xv1 = ((xv0 + 8192) ^ 64) - 1024;
  const int pr = wi & 1;
  u32x4 xa;
#pragma unroll
  for (int s = 0; s < 4; ++s) xa[s] = L_X + r32 * 128 + (((2 * s + h) ^ ((r32 >> 1) & 7)) * 16);
  const int tq = (lane & 15) >> 2, tp = lane & 3, tnb = (lane >> 4) & 1;
  unsigned wbase = L_RING + (wi >> 1) * 8192 + (8 * h + tq) * 128 + (((2 * pr + tnb) ^ (tq & 2)) * 32) + tp * 8;
  u32x4 id0, id1;
  if (grp == 0) {
    if (wi < 2) dma16(__builtin_amdgcn_make_buffer_rsrc((void*)wphi, 0, 32 * 512, 0x00020000), lane * 16, g * 2048 + wi * 1024, lds0 + L_WPHI + wi * 1024);
    if (wi == 2) dma16(__builtin_amdgcn_make_buffer_rsrc((void*)gatew, 0, 1024, 0x00020000), lane * 16, 0, lds0 + L_GW);
    if (wi == 3) {
      unsigned long long pc;
      asm volatile("s_getpc_b64 %0" : "=s"(pc));
      const unsigned line = in + 32 * l8;
      const rsrc_t rc = __builtin_amdgcn_make_buffer_rsrc((void*)(pc & ~127ull), 0, CODE_LINES * 128, 0x00020000);
      asm volatile("s_mov_b32 m0, %0\n\ts_nop 0\n\tbuffer_load_dword %1, %2, 0 offen lds" :: "s"(lds0 + L_DUMMY), "v"(line * 128), "s"(rc) : "memory");
    }
  }
  Gemm gm;
  gemm_stage0(grp, gm, xa, wbase, sr, rx, xv0, xv1, lds0 + L_X + wi * 2048, sr.rot * 128, rws, epv G_STA);

  const int hd = 4 * g + wi;
  const unsigned kvb = WS_KV + (unsigned)hd * 131072, kvh = kvb + grp * 65536;
  const unsigned koff = kvh + lane * 16;
  const unsigned vreal = kvh + h * 256 + (lane & 15) * 16, vones = WS_ONES + g * 256;
  const unsigned voff0 = (lane & 16) ? vones - 1024 : vreal, voff1 = (lane & 16) ? vones - 1536 : vreal;
  Attn at;
  unsigned kn = WS_KN + (hd * 32 + r32) * 4;
  Kv0 kv0;
  kv0.koff = koff; kv0.voff0 = voff0; kv0.voff1 = voff1; kv0.ws = rws;
  kv0.t0 = tb * 2048; kv0.t1 = ((tb + 1) & 31) * 2048; kv0.t2 = ((tb + 2) & 31) * 2048;
  auto epoch = [&]() {
    const unsigned long long e = (((unsigned long long)(unsigned)__builtin_amdgcn_readfirstlane(epv[1]) << 32) |
                                  (unsigned)__builtin_amdgcn_readfirstlane(epv[0])) + 1ull;
    return u32x2{(unsigned)e, (unsigned)(e >> 32)};
  };
  auto read_gate = [&](int tt, int lane) {
    const LAS float* gp = (const LAS float*)(lds + L_GP + tt * 256 + lane * 4);
    return sigmoid_of((gp[0] + gp[128]) + (gp[256] + gp[384]));
  };

  u32x2 ep;
  float gate0 = 0.f, gate1;
  if (grp) {
    u32x8 pk[4];
    STAMP(1);
    gemm_stage1(gm, xa, wbase, sr, rws, epv G_STA);
    ep = epoch();
    STAMP(2);
    WAIT_BAR(8); STAMPF(20);
    __builtin_amdgcn_s_setprio(1);
    const int lane = lane_id(), r32 = lane & 31, h = lane >> 5, tq = (lane & 15) >> 2, tp = lane & 3, tnb = (lane >> 4) & 1;
    const unsigned wpa = L_WPHI + 2 * pr * 512 + (4 * h + tq) * 32 + tp * 8, sgn = tnb ? 0x80008000u : 0u;
    const u32x4 wp0 = lds_tr2(lds, wpa, wpa + 256) ^ sgn, wp1 = lds_tr2(lds, wpa + 512, wpa + 768) ^ sgn;
    gemm_tail(gm);
    u32x4 in0 = pack8s<0>(gm.acc0, LOG2E), in1 = pack8s<0>(gm.acc1, LOG2E), in2 = pack8s<8>(gm.acc0, LOG2E), in3 = pack8s<8>(gm.acc1, LOG2E);
    PIN(in0); PIN(in1); PIN(in2); PIN(in3);
    STAMPF(30); WAIT_BAR(4); STAMPF(21);
    f32x16 e0 = MFMA(wp0, in0, f32x16{}), e1 = MFMA(wp0, in1, f32x16{}), e2 = MFMA(wp1, in2, f32x16{}), e3 = MFMA(wp1, in3, f32x16{});
    SBAR();
    gate1 = read_gate(1, lane);
    asm volatile("" : "+{v253}"(gate1));
    asm volatile("s_nop 11" : "+v"(e0), "+v"(e1), "+v"(e2), "+v"(e3));
    f32x2 fm[4] = {feat_max(e0), feat_max(e1), feat_max(e2), feat_max(e3)};
    feat_max_merge(fm);
    PIN(fm[0]); PIN(fm[1]); PIN(fm[2]); PIN(fm[3]);
    STAMPF(31); WAIT_BAR(0); STAMPF(22);
    feat_exp(e0, fm[0]); feat_exp(e1, fm[1]);
    STAMPF(32); LDS_BAR(); STAMPF(23);
    const u32x2 fkv = __builtin_amdgcn_raw_buffer_load_b64(rws, WS_FKV + (g * 32 + r32) * 8, 0, SC1);
    feat_exp(e2, fm[2]); feat_exp(e3, fm[3]);
    STAMPF(33); LDS_BAR(); STAMPF(24);
    pk[0] = feat_norm(e0); pk[1] = feat_norm(e1);
    if (!__all(fkv[0] == ep[0] && fkv[1] == ep[1])) wait_flags(rws, WS_FKV + (g * 32 + r32) * 8, ep);
    STAMPF(34); LDS_BAR(); STAMPF(25);
    pk[2] = feat_norm(e2); pk[3] = feat_norm(e3);
    STAMPF(35); LDS_BAR(); STAMPF(26);
    kv_issue(kv0, at, kn);
    if (wi < 2) {
      const unsigned pb = L_PQ + 2 * pr * 4096 + lane * 16;
#pragma unroll
      for (int j = 0; j < 4; ++j) { lds_w128(lds, pb + j * 2048, lo4(pk[j])); lds_w128(lds, pb + j * 2048 + 1024, hi4(pk[j])); }
    } else {
      make_identity(r32, h, id0, id1);
      e0 = transpose32(lo4(pk[0]), hi4(pk[0]), id0, id1); e1 = transpose32(lo4(pk[1]), hi4(pk[1]), id0, id1);
      e2 = transpose32(lo4(pk[2]), hi4(pk[2]), id0, id1); e3 = transpose32(lo4(pk[3]), hi4(pk[3]), id0, id1);
      PIN(e0); PIN(e1); PIN(e2); PIN(e3);
    }
    STAMPF(36); LDS_BAR(); STAMPF(27);
    if (wi >= 2) { pk[0] = pack16(e0); pk[1] = pack16(e1); pk[2] = pack16(e2); pk[3] = pack16(e3); }
    PIN(pk[0]); PIN(pk[1]); PIN(pk[2]); PIN(pk[3]);
    kv_landed(at, kn);
    STAMPF(28);
    gm.w = __builtin_shufflevector(pk[0], pk[1], 0, 1, 2, 3, 4, 5, 6, 7, 8, 9, 10, 11, 12, 13, 14, 15);
    gm.x0 = __builtin_shufflevector(pk[2], pk[3], 0, 1, 2, 3, 4, 5, 6, 7, 8, 9, 10, 11, 12, 13, 14, 15);
    asm volatile("" : "={v[16:31]}"(gm.acc0), "={v[32:47]}"(gm.acc1), "+{v[96:111]}"(gm.w), "+{v[112:127]}"(gm.x0), "={v[128:143]}"(gm.x1),
                      "={v[2:3]}"(epv), "={v[4:7]}"(xa), "={v8}"(wbase), "={v[10:15]}"(sr.wv));
  }
  int is_a = grp ^ 1;
  asm volatile("" : "+s"(is_a));
  if (is_a) {
    ep = epoch();
    STAMP(1);
    LDS_BAR(); STAMPF(20);
    __builtin_amdgcn_s_setprio(1);
    const int lane = lane_id(), r32 = lane & 31, h = lane >> 5;
    gemm_tail(gm);
    u32x4 p00 = pack8<0>(gm.acc0), p01 = pack8<0>(gm.acc1), p10 = pack8<8>(gm.acc0), p11 = pack8<8>(gm.acc1);
    PIN(p00); PIN(p01); PIN(p10); PIN(p11);
    const unsigned gx = wi == 0 ? xa[0] : wi == 1 ? xa[1] : wi == 2 ? xa[2] : xa[3], gwa = L_GW + wi * 32 + h * 16;
    u32x4 gw[2], gx0[2], gx1[2];
    f32x4 ga = {};
    auto gate_read = [&](int c) {
#pragma unroll
      for (int i = 0; i < 2; ++i) {
        gw[i] = lds_r128(lds, gwa + ((c + i) ^ sr.rot) * 128); gx0[i] = lds_r128(lds, gx + (c + i) * 8192); gx1[i] = lds_r128(lds, gx + (c + i) * 8192 + 4096);
      }
    };
    auto gate_dot = [&]() {
#pragma unroll
      for (int j = 0; j < 4; ++j) {
        ga[0] = dot2(gx0[0][j], gw[0][j], ga[0]); ga[1] = dot2(gx1[0][j], gw[0][j], ga[1]);
        ga[2] = dot2(gx0[1][j], gw[1][j], ga[2]); ga[3] = dot2(gx1[1][j], gw[1][j], ga[3]);
      }
      PIN(ga);
    };
    const unsigned kb = WS_KV + (unsigned)(4 * g + 2 * pr) * 131072 + (unsigned)(2 * tb) * 2048;
    STAMPF(30); LDS_BAR(); STAMPF(21);
    f32x16 t0, t1;
    f32x2 nrm;
    if (wi < 2) {
      const int sw = (r32 & 19) | ((r32 & 4) << 1) | ((r32 & 8) >> 1);
      const unsigned ko = kb + h * 512 + sw * 16;
      KVST(rws, ko, p00); KVST(rws, ko + 2048, p01);
      KVST(rws, ko + 131072, p10); KVST(rws, ko + 131072 + 2048, p11);
      nrm = f32x2{fmax2(half_add(sumsq8(p00)), half_add(sumsq8(p01))), fmax2(half_add(sumsq8(p10)), half_add(sumsq8(p11)))};
      PIN(nrm);
    } else {
      make_identity(r32, h, id0, id1);
      t0 = transpose32(p00, p10, id0, id1); t1 = transpose32(p01, p11, id0, id1);
      PIN(t0); PIN(t1);
    }
    STAMPF(31); LDS_BAR(); STAMPF(22);
    if (wi < 2) {
      const float n = wave_max32(h ? nrm[1] : nrm[0]);
      __builtin_amdgcn_raw_buffer_store_b32(__float_as_uint(n), rws, r32 == 0 ? WS_KN + (unsigned)((4 * g + 2 * pr + h) * 32 + tb) * 4 : OOB, 0, SC1);
    } else {
      const u32x8 v0 = pack16(t0), v1 = pack16(t1);
      const unsigned vo = kb + (r32 >> 4) * 131072 + 1024 + h * 256 + (r32 & 15) * 16;
      KVST(rws, vo, lo4(v0)); KVST(rws, vo + 512, hi4(v0));
      KVST(rws, vo + 2048, lo4(v1)); KVST(rws, vo + 2048 + 512, hi4(v1));
      if (wi == 2) ws_store(rws, lane < 8 ? WS_ONES + tb * 2048 + g * 256 + lane * 16 : OOB, u32x4{ONES, ONES, ONES, ONES});
    }
    gate_read(0);
    STAMPF(32); LDS_BAR(); STAMPF(23);
    gate_dot(); gate_read(2);
    STAMPF(33); LDS_BAR(); STAMPF(24);
    gate_dot(); gate_read(4);
    STAMPF(34); LDS_BAR(); STAMPF(25);
    gate_dot(); gate_read(6);
    STAMPF(35); LDS_BAR(); STAMPF(26);
    gate_dot();
    *(LAS float*)(lds + L_GP + wi * 512 + lane * 4) = ga[0] + ga[2]; *(LAS float*)(lds + L_GP + wi * 512 + 256 + lane * 4) = ga[1] + ga[3];
    STAMPF(36); WAIT_BAR(0); STAMPF(27);
    if (w == 0 && lane == 0) set_flag(rws, WS_FKV + (g * 32 + tb) * 8, ep);
    gate0 = read_gate(0, lane); gate1 = read_gate(1, lane);
    asm volatile("" : "+{v252}"(gate0), "+{v253}"(gate1), "+{v[192:195]}"(id0), "+{v[196:199]}"(id1));
    __builtin_amdgcn_s_setprio(0);
    STAMP(2);
    gemm_stage2(gm, xa, wbase, sr, kv0, at, kn G_STA);
    STAMPF(29);
  }
  if (is_a) {
    const int lane = lane_id(), r32 = lane & 31, h = lane >> 5;
    if (wi < 2) {
      const float c = 0.25f * LOG2E;
      const unsigned qb = L_Q + 2 * pr * 2048 + lane * 16;
      lds_w128(lds, qb, pack8s<0>(gm.acc0, c)); lds_w128(lds, qb + 1024, pack8s<0>(gm.acc1, c));
      lds_w128(lds, qb + 2048, pack8s<8>(gm.acc0, c)); lds_w128(lds, qb + 3072, pack8s<8>(gm.acc1, c));
    } else {
      const u32x8 v0 = pack16(transpose32(pack8<0>(gm.acc0), pack8<8>(gm.acc0), id0, id1));
      const u32x8 v1 = pack16(transpose32(pack8<0>(gm.acc1), pack8<8>(gm.acc1), id0, id1));
      const unsigned vb = L_VB + (2 * pr + (r32 >> 4)) * 2048 + h * 256 + (r32 & 15) * 16;
      lds_w128(lds, vb, lo4(v0)); lds_w128(lds, vb + 512, hi4(v0));
      lds_w128(lds, vb + 1024, lo4(v1)); lds_w128(lds, vb + 1536, hi4(v1));
    }
  }
  float kmax2 = wave_max32(__uint_as_float(kn));
  PIN(kmax2);
  STAMP(3);
  LDS_BAR();

  const int lq = lane_id();
  if (grp == 0) __builtin_amdgcn_s_setprio(2); else __builtin_amdgcn_s_setprio(1);
  const u32x4 q0 = lds_r128(lds, L_Q + wi * 2048 + lq * 16), q1 = lds_r128(lds, L_Q + wi * 2048 + 1024 + lq * 16);
  if (w >= 6) {
#pragma unroll
    for (int j = 0; j < 2; ++j) {
      const unsigned vb = L_VB + (2 * pr + j) * 2048 + (lq >> 5) * 256 + ((lq & 31) & 15) * 16;
      u32x4 v[4];
#pragma unroll
      for (int i = 0; i < 4; ++i) { v[i] = lds_r128(lds, vb + i * 512); if ((lq & 31) >= 16) v[i] = u32x4{ONES, ONES, ONES, ONES}; }
      const u32x16 pt = j ? gm.x0 : gm.w;
      f32x16 kp = MFMA(__builtin_shufflevector(pt, pt, 0, 1, 2, 3), v[0], f32x16{});
      kp = MFMA(__builtin_shufflevector(pt, pt, 4, 5, 6, 7), v[1], kp);
      kp = MFMA(__builtin_shufflevector(pt, pt, 8, 9, 10, 11), v[2], kp);
      kp = MFMA(__builtin_shufflevector(pt, pt, 12, 13, 14, 15), v[3], kp);
      const unsigned pb = WS_KP + (unsigned)(((4 * g + 2 * pr + j) * 8 + (lq >> 5)) * 32 + tb) * 512 + (lq & 31) * 16;
#pragma unroll
      for (int q4 = 0; q4 < 4; ++q4)
        ws_store(rws, pb + q4 * 32768, __builtin_bit_cast(u32x4, f32x4{kp[4 * q4], kp[4 * q4 + 1], kp[4 * q4 + 2], kp[4 * q4 + 3]}));
    }
    drain();
    if (lq == 0) set_flag(rws, WS_FKP + ((g * 2 + pr) * 32 + tb) * 8, ep);
  }
  float m0 = __builtin_amdgcn_sqrtf(half_add(sumsq8(q0)) * kmax2), m1 = __builtin_amdgcn_sqrtf(half_add(sumsq8(q1)) * kmax2);
  if (!__all(m0 <= SAFE_SHIFT && m1 <= SAFE_SHIFT)) {
    m0 = m1 = -3.0e38f;
#pragma clang loop unroll(disable)
    for (int t = 0; t < 64; ++t) {
      const u32x4 kf = ws_load(rws, kvb + t * 2048 + lq * 16);
      const f32x16 s0 = MFMA(kf, q0, f32x16{}), s1 = MFMA(kf, q1, f32x16{});
      m0 = fmax2(m0, fmax2(max8(s0, 0), max8(s0, 8))); m1 = fmax2(m1, fmax2(max8(s1, 0), max8(s1, 8)));
    }
    m0 = half_max(m0); m1 = half_max(m1);
  }
  AttnConst ak;
#pragma unroll
  for (int i = 0; i < 16; ++i) { ak.c0[i] = -m0; ak.c1[i] = -m1; }
  at.sa = MFMA((u32x4{at.kr[0], at.kr[1], at.kr[2], at.kr[3]}), q0, ak.c0);
  asm volatile("s_nop 11" : "+{v[128:143]}"(at.sa));
  ak.q = __builtin_shufflevector(q0, q1, 0, 1, 2, 3, 4, 5, 6, 7);
  ak.idx = 0xDDDDDDDDu; ak.koff = koff; ak.voff0 = voff0; ak.voff1 = voff1; ak.kv = rws; ak.entry = attn_define();
  unsigned tcur = kv0.t2;

  const int otb = 4 * xc + (in & 3), og = in >> 2;
  const rsrc_t rwo = __builtin_amdgcn_make_buffer_rsrc((void*)(grp ? woa : wob), 0, 512 * 1024, 0x00020000);
  auto wo_addr = [&](int l) {
    return (unsigned)(wi * 128 + 4 * (l >> 5) + ((l & 15) >> 2)) * 128 + ((((l >> 4) & 1) ^ ((l >> 2) & 2)) * 32) + (l & 3) * 8;
  };
  auto stage_wo = [&](int half) {
    const int lane = lane_id();
    const unsigned wov = (unsigned)(128 * wi + (lane >> 3)) * 1024 + og * 128 + (((lane & 7) ^ ((lane >> 2) & 4)) * 16);
    unsigned tmp;
    asm volatile(".irp i,0,1,2,3,4,5,6,7\n\ts_add_u32 m0, %1, 1024*\\i\n\ts_add_u32 %0, %2, 8192*\\i\n\t"
                 "buffer_load_dwordx4 %3, %4, %0 offen lds\n\t.endr"
                 : "=&s"(tmp) : "s"(lds0 + grp * 65536 + wi * 16384 + half * 8192), "s"(half * 65536), "v"(wov), "s"(rwo) : "memory", "scc");
  };
  const unsigned ob = (unsigned)((otb * 2 + grp) * 32 + wi * 8) * 1024;
  const unsigned lflag = L_VB + wi * 2048 + 512;
  f32x16 d0, d1;
  u32x4 of[8];
  auto fetch_ob = [&]() {
    const int lane = lane_id();
    wait_flags(rws, WS_FOB + (otb * 32 + wi * 8 + (lane & 7)) * 8, ep);
#pragma unroll
    for (int i = 0; i < 8; ++i) of[i] = __builtin_amdgcn_raw_buffer_load_b128(rws, lane * 16, WS_OB + ob + i * 1024, SC1);
  };
  auto project_b = [&]() {
    const unsigned wo0 = L_WOB + wo_addr(lane_id()), wo1 = wo0 ^ 64;
    d0 = f32x16{}; d1 = f32x16{};
#pragma unroll
    for (int i = 0; i < 8; ++i) {
      d0 = MFMA(lds_tr2(lds, wo0 + i * 2048, wo0 + i * 2048 + 1024), of[i], d0);
      d1 = MFMA(lds_tr2(lds, wo1 + i * 2048, wo1 + i * 2048 + 1024), of[i], d1);
      if (i == 3) SBAR();
    }
    asm volatile("" : "+{v[16:31]}"(d0), "+{v[32:47]}"(d1));
  };
  STAMP(4);

  attn_run<2>(at, ak, tcur);
  STAMP(5);
  stage_wo(0);
  if (w == 0) {
    const int lane = lane_id(), rh = tb & 3, sl = tb >> 2;
    wait_flags(rws, WS_FKP + ((g * 2 + (rh >> 1)) * 32 + (lane & 31)) * 8, ep);
    const unsigned pb = WS_KP + (unsigned)((4 * g + rh) * 8 + sl) * 16384;
    u32x4 pv[16];
#pragma unroll
    for (int i = 0; i < 16; ++i) pv[i] = __builtin_amdgcn_raw_buffer_load_b128(rws, lane * 16, pb + i * 1024, SC1);
    SBAR();
    f32x4 s = __builtin_bit_cast(f32x4, pv[0]);
#pragma unroll
    for (int i = 1; i < 16; ++i) s += __builtin_bit_cast(f32x4, pv[i]);
    s = f32x4{half_add(s[0]), half_add(s[1]), half_add(s[2]), half_add(s[3])};
    ws_store(rws, lane < 32 ? WS_KR + ((4 * g + rh) * 8 + sl) * 512 + lane * 16 : OOB, __builtin_bit_cast(u32x4, s));
    drain();
    if (lane == 0) set_flag(rws, WS_FKR + ((4 * g + rh) * 8 + sl) * 8, ep);
  }
  STAMP(6);
  attn_run<2>(at, ak, tcur);
  STAMP(7);
  stage_wo(1);
  if (grp == 0) {
    const int lane = lane_id();
    wait_flags(rws, WS_FKR + (hd * 8 + (lane & 7)) * 8, ep);
    f32x16 kv;
#pragma unroll
    for (int q4 = 0; q4 < 4; ++q4) {
      const f32x4 v = __builtin_bit_cast(f32x4, __builtin_amdgcn_raw_buffer_load_b128(rws, lane * 16, WS_KR + hd * 4096 + q4 * 1024, SC1));
      kv[4 * q4] = v[0]; kv[4 * q4 + 1] = v[1]; kv[4 * q4 + 2] = v[2]; kv[4 * q4 + 3] = v[3];
    }
    const u32x4 ka = pack8<0>(kv), kb = pack8<8>(kv);
#pragma unroll
    for (int tt = 0; tt < 2; ++tt) {
      const unsigned pb = L_PQ + (wi * 2 + tt) * 2048 + lane * 16, so = (unsigned)((tb * 2 + tt) * 32 + hd) * 1024;
      const f32x16 nd = MFMA(kb, lds_r128(lds, pb + 1024), MFMA(ka, lds_r128(lds, pb), f32x16{}));
      const float sc = (1.f - (tt ? gate1 : gate0)) * __builtin_amdgcn_rcpf(nd[8] + 1e-6f);
      ws_store(rws, WS_OB + so + lane * 16, pack8s<0>(nd, sc));
      ws_store(rws, WS_OA + so + lane * 16, u32x4{SENT, SENT, SENT, SENT});
    }
  }
  STAMP(8);
  attn_run<1>(at, ak, tcur);
  if (grp == 0 && lane_id() == 0) {
    set_flag(rws, WS_FOB + (tb * 32 + hd) * 8, ep);
    *(LAS volatile u32x2*)(lds + lflag) = ep;
  }
  STAMP(9);
  attn_run<1>(at, ak, tcur);
  if (grp == 0) __builtin_amdgcn_s_setprio(0);
  fetch_ob();
  STAMP(10);
  attn_run<1>(at, ak, tcur);
  if (grp) {
#pragma clang loop unroll(disable)
    for (unsigned spins = 0; spins < SPIN_CAP; ++spins) {
      const u32x2 v = *(LAS volatile u32x2*)(lds + lflag);
      if (__all(v[0] == ep[0] && v[1] == ep[1])) break;
      __builtin_amdgcn_s_sleep(2);
    }
    asm volatile("" ::: "memory");
  }
  project_b();
  STAMP(11);
  attn_run<1>(at, ak, tcur);
  STAMP(12);
  {
    const u32x4 vl = {at.vr1[12], at.vr1[13], at.vr1[14], at.vr1[15]};
    typedef __bf16 bf16x16 __attribute__((ext_vector_type(16)));
    const f32x8 ph = __builtin_shufflevector(at.sb, at.sb, 8, 9, 10, 11, 12, 13, 14, 15);
    at.o1 = __builtin_amdgcn_smfmac_f32_32x32x32_bf16(__builtin_bit_cast(bf16x8, vl), __builtin_bit_cast(bf16x16, ph), at.o1, (int)0xDDDDDDDDu, 0, 0);
  }

  const int ln = lane_id();
  {
    const int lane = ln;
    const f32x16 mine = grp ? at.o1 : at.o0, other = grp ? at.o0 : at.o1;
    const unsigned so = L_PQ + (wi * 2 + grp) * 2048 + lane * 16, sl = L_VB + (wi * 2 + grp) * 1024 + lane * 4;
    lds_w128(lds, so, __builtin_bit_cast(u32x4, f32x4{other[0], other[1], other[2], other[3]}));
    lds_w128(lds, so + 1024, __builtin_bit_cast(u32x4, f32x4{other[4], other[5], other[6], other[7]}));
    *(LAS float*)(lds + sl) = other[8];
    LDS_BAR();
    const unsigned ro = so ^ 2048, rl = sl ^ 1024;
    const f32x4 a = __builtin_bit_cast(f32x4, lds_r128(lds, ro)), b = __builtin_bit_cast(f32x4, lds_r128(lds, ro + 1024));
    const float l = mine[8] + *(LAS const float*)(lds + rl);
    f32x16 o = mine;
#pragma unroll
    for (int i = 0; i < 4; ++i) { o[i] += a[i]; o[4 + i] += b[i]; }
    const u32x4 ov = __builtin_elementwise_min(pack8s<0>(o, (grp ? gate1 : gate0) * __builtin_amdgcn_rcpf(l)), u32x4{SENT - 1, SENT - 1, SENT - 1, SENT - 1});
    ws_store(rws, WS_OA + (unsigned)((tb * 2 + grp) * 32 + hd) * 1024 + lane * 16, ov);
    SBAR();
  }
  STAMP(13);

  u32x16 wf0[2], wf1[2];
  const unsigned wo0 = L_WOA + wo_addr(ln), wo1 = wo0 ^ 64;
#pragma unroll
  for (int i = 0; i < 8; ++i) {
    const u32x4 a = lds_tr2(lds, wo0 + i * 2048, wo0 + i * 2048 + 1024), b = lds_tr2(lds, wo1 + i * 2048, wo1 + i * 2048 + 1024);
#pragma unroll
    for (int j = 0; j < 4; ++j) { wf0[i >> 2][(i & 3) * 4 + j] = a[j]; wf1[i >> 2][(i & 3) * 4 + j] = b[j]; }
  }
  STAMP(14);
  project_a(d0, d1, wf0, wf1, rws, ln * 16, WS_OA + ob);
  {
    const unsigned h = ln >> 5, tok = grp * 32 + (ln & 31), rb = L_RED + wi * 16384 + tok * 256;
#pragma unroll
    for (int q4 = 0; q4 < 4; ++q4) {
      lds_w128(lds, rb + (((2 * q4 + h) ^ (tok & 7)) * 16), __builtin_bit_cast(u32x4, f32x4{d0[4 * q4], d0[4 * q4 + 1], d0[4 * q4 + 2], d0[4 * q4 + 3]}));
      lds_w128(lds, rb + (((8 + 2 * q4 + h) ^ (tok & 7)) * 16), __builtin_bit_cast(u32x4, f32x4{d1[4 * q4], d1[4 * q4 + 1], d1[4 * q4 + 2], d1[4 * q4 + 3]}));
    }
  }
  STAMP(15);
  LDS_BAR();
  {
    const unsigned tok = w * 8 + (ln >> 3), c8 = ln & 7;
    const unsigned a0 = L_RED + tok * 256 + (((2 * c8) ^ (tok & 7)) * 16), a1 = a0 ^ 16;
    f32x4 lo = __builtin_bit_cast(f32x4, lds_r128(lds, a0)), hi = __builtin_bit_cast(f32x4, lds_r128(lds, a1));
#pragma unroll
    for (int k = 1; k < 4; ++k) {
      lo += __builtin_bit_cast(f32x4, lds_r128(lds, a0 + k * 16384));
      hi += __builtin_bit_cast(f32x4, lds_r128(lds, a1 + k * 16384));
    }
    *(u32x4*)(out + (size_t)(64 * otb + tok) * 1024 + og * 128 + c8 * 16) = u32x4{pk2(lo[0], lo[1]), pk2(lo[2], lo[3]), pk2(hi[0], hi[1]), pk2(hi[2], hi[3])};
  }
  if (blockIdx.x == 0 && w == 0 && ln == 0) set_flag(rws, WS_EPOCH, ep);
  STAMP(16); STAMP_RT(63);
}

}

extern "C" void kernel_launch(void* const* d_in, const int* in_sizes, int n_in, void* d_out, int out_size, void* d_ws,
                              size_t ws_size, hipStream_t stream) {
  gated_mix_kernel<<<256, 512, 0, stream>>>((const char*)d_in[0], (const char*)d_in[1], (const char*)d_in[2], (const char*)d_in[4],
                                            (const char*)d_in[5], (const char*)d_in[6], (const char*)d_in[7], (char*)d_out, (char*)d_ws);
}
```

```cpp
#include <hip/hip_runtime.h>
#include <hip/hip_bf16.h>
#include <stdint.h>

namespace {

typedef __bf16 bf16x8 __attribute__((ext_vector_type(8)));
typedef __bf16 bf16x2 __attribute__((ext_vector_type(2)));
typedef float f32x16 __attribute__((ext_vector_type(16)));
typedef float f32x8 __attribute__((ext_vector_type(8)));
typedef float f32x4 __attribute__((ext_vector_type(4)));
typedef float f32x2 __attribute__((ext_vector_type(2)));
typedef unsigned u32x16 __attribute__((ext_vector_type(16)));
typedef unsigned u32x8 __attribute__((ext_vector_type(8)));
typedef unsigned u32x4 __attribute__((ext_vector_type(4)));
typedef unsigned u32x2 __attribute__((ext_vector_type(2)));
typedef short s16x4 __attribute__((ext_vector_type(4)));
typedef __amdgpu_buffer_rsrc_t rsrc_t;
#define LAS __attribute__((address_space(3)))
#define DI __device__ __forceinline__

constexpr unsigned WS_EPOCH = 0;
constexpr unsigned FL = 128;
constexpr unsigned WS_FKV = 0x60000;
constexpr unsigned WS_FKP = 0x70000;
constexpr unsigned WS_FOB = 0x80000;
constexpr unsigned WS_KN = 0xC000;
constexpr unsigned WS_KR = 0x10000;
constexpr unsigned WS_KP = 0x100000;
constexpr unsigned WS_OB = 0x500000;
constexpr unsigned WS_OA = 0x700000;
constexpr unsigned WS_ONES = 0x40000;
constexpr unsigned WS_KV = 0x900000;
constexpr unsigned WS_END = 0xD00000;

constexpr unsigned L_X = 0;
constexpr unsigned L_RING = 65536;
constexpr unsigned L_WO = 0;
constexpr unsigned L_RED = 0;
constexpr unsigned L_PQ = 131072;
constexpr unsigned L_Q = 147456;
constexpr unsigned L_WPHI = L_Q;
constexpr unsigned L_GW = L_Q + 2048;
constexpr unsigned L_DUMMY = L_Q + 3072;
constexpr unsigned L_GP = L_Q + 4096;
constexpr unsigned L_VB = 155648;
constexpr unsigned L_BYTES = 163840;

constexpr float LOG2E = 1.4426950408889634f;
constexpr float SAFE_SHIFT = 48.0f;
constexpr unsigned ONES = 0x3F803F80u;
constexpr unsigned SPIN_CAP = 1u << 18;
constexpr unsigned SENT = 0xFFFFFFFFu;
constexpr int SC1 = 16;
constexpr unsigned OOB = 0xF0000000u;

#define MFMA(a, b, c) __builtin_amdgcn_mfma_f32_32x32x16_bf16(__builtin_bit_cast(bf16x8, a), __builtin_bit_cast(bf16x8, b), c, 0, 0, 0)

DI unsigned pk2(float a, float b) { f32x2 v = {a, b}; return __builtin_bit_cast(unsigned, __builtin_convertvector(v, bf16x2)); }
template <int O> DI u32x4 pack8(const f32x16& v) {
  return u32x4{pk2(v[O], v[O + 1]), pk2(v[O + 2], v[O + 3]), pk2(v[O + 4], v[O + 5]), pk2(v[O + 6], v[O + 7])};
}
template <int O> DI u32x4 pack8s(const f32x16& v, float s) {
  u32x4 o;
#pragma unroll
  for (int i = 0; i < 4; ++i)
    o[i] = __builtin_bit_cast(unsigned, __builtin_convertvector(f32x2{v[O + 2 * i], v[O + 2 * i + 1]} * f32x2{s, s}, bf16x2));
  return o;
}
DI float dot2(unsigned a, unsigned b, float c) {
  return __builtin_amdgcn_fdot2_f32_bf16(__builtin_bit_cast(bf16x2, a), __builtin_bit_cast(bf16x2, b), c, false);
}
DI float sumsq8(const u32x4& f) { return dot2(f[3], f[3], dot2(f[2], f[2], dot2(f[1], f[1], dot2(f[0], f[0], 0.f)))); }
DI float half_add(float v) {
  auto r = __builtin_amdgcn_permlane32_swap(__float_as_uint(v), __float_as_uint(v), false, false);
  return __uint_as_float(r[0]) + __uint_as_float(r[1]);
}
DI float fmax2(float a, float b) { return __builtin_fmaxf(a, b); }
DI float half_max(float v) {
  auto r = __builtin_amdgcn_permlane32_swap(__float_as_uint(v), __float_as_uint(v), false, false);
  return fmax2(__uint_as_float(r[0]), __uint_as_float(r[1]));
}
template <int CTRL> DI float dpp_max(float v) {
  return fmax2(v, __uint_as_float(__builtin_amdgcn_update_dpp(0u, __float_as_uint(v), CTRL, 0xF, 0xF, true)));
}
DI float wave_max32(float v) {
  v = dpp_max<0xB1>(v); v = dpp_max<0x4E>(v); v = dpp_max<0x141>(v); v = dpp_max<0x140>(v);
  auto r = __builtin_amdgcn_permlane16_swap(__float_as_uint(v), __float_as_uint(v), false, false);
  return fmax2(__uint_as_float(r[0]), __uint_as_float(r[1]));
}
DI float max8(const f32x16& p, int o) {
  return fmax2(fmax2(fmax2(fmax2(p[o], p[o + 1]), p[o + 2]), fmax2(fmax2(p[o + 3], p[o + 4]), p[o + 5])), fmax2(p[o + 6], p[o + 7]));
}

DI u32x4 lds_r128(LAS char* l, unsigned off) { return *(LAS const u32x4*)(l + off); }
DI void lds_w128(LAS char* l, unsigned off, u32x4 v) { *(LAS u32x4*)(l + off) = v; }
DI u32x4 lds_tr2(LAS char* l, unsigned off0, unsigned off1) {
  s16x4 a = __builtin_amdgcn_ds_read_tr16_b64_v4i16((LAS s16x4*)(l + off0));
  s16x4 b = __builtin_amdgcn_ds_read_tr16_b64_v4i16((LAS s16x4*)(l + off1));
  return __builtin_bit_cast(u32x4, __builtin_shufflevector(a, b, 0, 1, 2, 3, 4, 5, 6, 7));
}
DI void dma16(rsrc_t r, unsigned voff, unsigned soff, unsigned lds_dst) {
  asm volatile("s_mov_b32 m0, %0\n\ts_nop 0\n\tbuffer_load_dwordx4 %1, %2, %3 offen lds"
               :: "s"(lds_dst), "v"(voff), "s"(r), "s"(soff) : "memory");
}
DI int lane_id() {
  int l;
  asm volatile("v_mbcnt_lo_u32_b32 %0, -1, 0\n\tv_mbcnt_hi_u32_b32 %0, -1, %0" : "=v"(l));
  return l;
}
DI u32x4 ws_load(rsrc_t r, unsigned off) { return __builtin_amdgcn_raw_buffer_load_b128(r, off, 0, SC1); }
DI void ws_store(rsrc_t r, unsigned off, u32x4 v) { __builtin_amdgcn_raw_buffer_store_b128(v, r, off, 0, SC1); }
DI void drain() { asm volatile("s_waitcnt vmcnt(0)" ::: "memory"); }
DI void set_flag(rsrc_t r, unsigned off, u32x2 ep) { __builtin_amdgcn_raw_buffer_store_b64(ep, r, off, 0, SC1); }
DI void wait_flags(rsrc_t r, unsigned off, u32x2 ep) {
#pragma clang loop unroll(disable)
  for (unsigned spins = 0; spins < SPIN_CAP; ++spins) {
    u32x2 v = __builtin_amdgcn_raw_buffer_load_b64(r, off, 0, SC1);
    if (__all(v[0] == ep[0] && v[1] == ep[1])) break;
    __builtin_amdgcn_s_sleep(2);
    asm volatile("" ::: "memory");
  }
  asm volatile("" ::: "memory");
}
#define SBAR() __builtin_amdgcn_sched_barrier(0)
#define WAIT_BAR(N) do { SBAR(); asm volatile("s_waitcnt vmcnt(" #N ") lgkmcnt(0)\n\ts_barrier" ::: "memory"); SBAR(); } while (0)
#define LDS_BAR() do { SBAR(); asm volatile("s_waitcnt lgkmcnt(0)\n\ts_barrier" ::: "memory"); SBAR(); } while (0)

#define PIN(x) asm volatile("" : "+v"(x))

DI u32x4 lo4(const u32x8& v) { return __builtin_shufflevector(v, v, 0, 1, 2, 3); }
DI u32x4 hi4(const u32x8& v) { return __builtin_shufflevector(v, v, 4, 5, 6, 7); }

#define EX(r) "v_exp_f32 v" #r ", v" #r "\n\t"
#define QK(s0, s1, k0, k1, q0, q1, c0, c1) "v_mfma_f32_32x32x16_bf16 v[" #s0 ":" #s1 "], v[" #k0 ":" #k1 "], v[" #q0 ":" #q1 "], v[" #c0 ":" #c1 "]\n\t"
#define PV(o0, o1, a0, a1, p0, p1) "v_smfmac_f32_32x32x32_bf16 v[" #o0 ":" #o1 "], v[" #a0 ":" #a1 "], v[" #p0 ":" #p1 "], v248\n\t"
#define KVPOL " sc1"
#define LOADS(k0, k1, a0, a1, b0, b1)                                                     \
  "s_add_u32 s76, s76, 0x800\n\t"                                                      \
  "s_and_b32 s76, s76, 0xffff\n\t"                                                     \
  "buffer_load_dwordx4 v[" #k0 ":" #k1 "], v249, s[72:75], s76 offen" KVPOL "\n\t"               \
  "buffer_load_dwordx4 v[" #a0 ":" #a1 "], v250, s[72:75], s76 offen offset:1024" KVPOL "\n\t"   \
  "buffer_load_dwordx4 v[" #b0 ":" #b1 "], v251, s[72:75], s76 offen offset:1536" KVPOL "\n\t"
#define WODMA                                                                             \
  "s_mov_b32 m0, s84\n\t"                                                                 \
  "s_add_u32 s84, s84, 0x400\n\t"                                                         \
  "buffer_load_dwordx4 v254, s[80:83], s85 offen lds\n\t"                                 \
  "s_add_u32 s85, s85, 0x2000\n\t"
#define ITER(ck0, ck1, ca0, ca1, cb0, cb1, pk0, pk1, pa0, pa1, pb0, pb1, nk0, nk1, EXTRA, WAIT) \
  EX(128) EX(129) PV(176, 191, pb0, pb1, 152, 159) EX(130) EX(131)                        \
  LOADS(pk0, pk1, pa0, pa1, pb0, pb1)                                                     \
  EX(132) EX(133) QK(144, 159, ck0, ck1, 196, 199, 112, 127) EX(134) EX(135) EXTRA EX(136) EX(137) EX(138) \
  PV(160, 175, ca0, ca1, 128, 135) EX(139) EX(140) EX(141) EX(142) EX(143)                 \
  EX(144) EX(145) PV(160, 175, cb0, cb1, 136, 143) EX(146) EX(147) EX(148) EX(149)         \
  WAIT                                                                                    \
  QK(128, 143, nk0, nk1, 192, 195, 96, 111) EX(150) EX(151) EX(152) EX(153) EX(154)      \
  PV(176, 191, ca0, ca1, 144, 151) EX(155) EX(156) EX(157) EX(158) EX(159)

struct Attn {
  f32x16 o0, o1, sa, sb;
  u32x16 kr, vr0, vr1;
  unsigned t, wl, wg;
};
DI unsigned long long attn_define() {
  unsigned long long entry;
  asm volatile(
      "s_getpc_b64 %0\n\t"
      "s_branch 9f\n\t"
      "1:\n\t"
      ITER(200, 203, 216, 219, 220, 223, 212, 215, 240, 243, 244, 247, 204, 207, WODMA, "")
      ITER(204, 207, 224, 227, 228, 231, 200, 203, 216, 219, 220, 223, 208, 211, "", "")
      ITER(208, 211, 232, 235, 236, 239, 204, 207, 224, 227, 228, 231, 212, 215, WODMA, "s_waitcnt vmcnt(8)\n\t")
      ITER(212, 215, 240, 243, 244, 247, 208, 211, 232, 235, 236, 239, 200, 203, "", "s_waitcnt vmcnt(7)\n\t")
      "s_sub_u32 s77, s77, 1\n\t"
      "s_waitcnt vmcnt(0)\n\t"
      "s_cbranch_scc0 1b\n\t"
      "s_nop 4\n\t"
      "s_setpc_b64 s[78:79]\n\t"
      "9:"
      : "=s"(entry));
  return entry + 4;
}
struct AttnConst { f32x16 c0, c1; u32x8 q; unsigned idx, koff, voff0, voff1, wov; rsrc_t kv, wo; unsigned long long entry; };
DI void attn_run(Attn& a, const AttnConst& k, unsigned passes_less_one) {
  asm volatile(
      "s_swappc_b64 s[78:79], %[entry]"
      : "+{v[160:175]}"(a.o0), "+{v[176:191]}"(a.o1), "+{v[128:143]}"(a.sa), "+{v[144:159]}"(a.sb), "+{v[200:215]}"(a.kr),
        "+{v[216:231]}"(a.vr0), "+{v[232:247]}"(a.vr1), "+{s76}"(a.t), "+{s84}"(a.wl), "+{s85}"(a.wg), "+{s77}"(passes_less_one)
      : "{v[96:111]}"(k.c0), "{v[112:127]}"(k.c1), "{v[192:199]}"(k.q), "{v248}"(k.idx), "{v249}"(k.koff), "{v250}"(k.voff0),
        "{v251}"(k.voff1), "{v254}"(k.wov), "{s[72:75]}"(k.kv), "{s[80:83]}"(k.wo), [entry] "s"(k.entry)
      : "memory", "scc", "s78", "s79");
}

#define STAMP(i) do { } while (0)
#define STAMP_RT(i) do { } while (0)
#define STAMPF(i) do { } while (0)

#define G_RW(W, C, S)                                                                                 \
  "ds_read_b64_tr_b16 v[" #W "+4*" #S ":" #W "+4*" #S "+1], v8 offset:16384*(" #C "&3)+2048*" #S "\n\t"       \
  "ds_read_b64_tr_b16 v[" #W "+4*" #S "+2:" #W "+4*" #S "+3], v8 offset:16384*(" #C "&3)+2048*" #S "+512\n\t"
#define G_RX(X, C, S, T) "ds_read_b128 v[" #X "+4*" #S ":" #X "+4*" #S "+3], v[4+" #S "] offset:8192*" #C "+4096*" #T "\n\t"
#define G_RS(W, X0, X1, C, S) G_RW(W, C, S) G_RX(X0, C, S, 0) G_RX(X1, C, S, 1)
#define G_READ(W, X0, X1, C) G_RS(W, X0, X1, C, 0) G_RS(W, X0, X1, C, 1) G_RS(W, X0, X1, C, 2) G_RS(W, X0, X1, C, 3)
#define G_M(ACC, W, X, S, CIN) \
  "v_mfma_f32_32x32x16_bf16 " ACC ", v[" #W "+4*" #S ":" #W "+4*" #S "+3], v[" #X "+4*" #S ":" #X "+4*" #S "+3], " CIN "\n\t"
#define G_A0 "v[16:31]"
#define G_A1 "v[32:47]"
#define G_MMA2(W, X0, X1, S) G_M(G_A0, W, X0, S, G_A0) G_M(G_A1, W, X1, S, G_A1)
#define G_STEP(RW, RX0, RX1, C, W, X0, X1, C0, C1, D0, D1, D2, D3)                                    \
  G_M(G_A0, W, X0, 0, C0) G_RW(RW, C, 0) G_RX(RX0, C, 0, 0) G_M(G_A1, W, X1, 0, C1) G_RX(RX1, C, 0, 1) G_RW(RW, C, 1) \
  G_M(G_A0, W, X0, 1, G_A0) G_RX(RX0, C, 1, 0) G_RX(RX1, C, 1, 1) D0 G_M(G_A1, W, X1, 1, G_A1) G_RW(RW, C, 2) G_RX(RX0, C, 2, 0) \
  G_M(G_A0, W, X0, 2, G_A0) G_RX(RX1, C, 2, 1) G_RW(RW, C, 3) D1 G_M(G_A1, W, X1, 2, G_A1) G_RX(RX0, C, 3, 0) G_RX(RX1, C, 3, 1) \
  G_M(G_A0, W, X0, 3, G_A0) D2 G_M(G_A1, W, X1, 3, G_A1) D3
#define G_EVEN(C, D0, D1, D2, D3) G_STEP(48, 64, 80, C, 96, 112, 128, G_A0, G_A1, D0, D1, D2, D3)
#define G_ODD(C, D0, D1, D2, D3) G_STEP(96, 112, 128, C, 48, 64, 80, G_A0, G_A1, D0, D1, D2, D3)
#define G_FIRST(D0, D1, D2, D3) G_STEP(96, 112, 128, 1, 48, 64, 80, "0", "0", D0, D1, D2, D3)
#define G_ST ""
#define G_ST0(N) ""
#define G_STOP
#define G_STC
#define G_STP
#define G_STA
#define G_BAR(N) G_ST "s_waitcnt vmcnt(" #N ") lgkmcnt(0)\n\t" G_ST "s_barrier\n\t"
#define G_LBAR G_ST "s_waitcnt lgkmcnt(0)\n\t" G_ST "s_barrier\n\t"
#define G_EPOCH "buffer_load_dwordx2 v[2:3], off, %[ws], 0 sc1\n\t"
#define G_D0(R, N, COL)                                                                               \
  "s_xor_b32 %[tmp], %[rot], " #N "&7\n\t"                                                            \
  "s_add_u32 m0, %[ml], 16384*(" #N "&3)\n\t"                                                         \
  "s_mul_i32 %[tmp], %[tmp], 196608\n\t"                                                              \
  "buffer_load_dwordx4 v[10+2*" #COL "], %[" #R "], %[tmp] offen lds\n\t"
#define G_D1(R, COL) "buffer_load_dwordx4 v[11+2*" #COL "], %[" #R "], %[tmp] offen offset:1024 lds\n\t"
#define G_D2(R, N, COL)                                                                               \
  "s_add_u32 m0, %[ml], 16384*(" #N "&3)+8192\n\t"                                                    \
  "s_nop 0\n\t"                                                                                       \
  "buffer_load_dwordx4 v[10+2*" #COL "], %[" #R "], %[tmp] offen lds\n\t"
#define G_S0(N) G_D0(ra, N, 1), G_D1(ra, 1), G_D2(ra, N, 2), G_D1(ra, 2)
#define G_S1(N) G_D0(rb, N, 0), G_D1(rb, 0), G_D2(rb, N, 1), G_D1(rb, 1)
#define G_S2(N) G_D0(ra, N, 0), G_D1(ra, 0), G_D2(rb, N, 2), G_D1(rb, 2)
#define G_CAT(A, B, C, D) A B C D
#define G_APPLY(M, ...) M(__VA_ARGS__)
#define G_ALL(L) G_APPLY(G_CAT, L)
#define G_DX(C)                                                                                       \
  "s_add_u32 m0, %[mx], 8192*" #C "\n\t"                                                              \
  "s_xor_b32 %[tmp], %[rotx], 128*" #C "\n\t"                                                         \
  "buffer_load_dwordx4 %[xv0], %[rx], %[tmp] offen lds\n\t"                                           \
  "buffer_load_dwordx4 %[xv1], %[rx], %[tmp] offen offset:1024 lds\n\t"
typedef unsigned u32x6 __attribute__((ext_vector_type(6)));
struct Gemm { f32x16 acc0, acc1; u32x16 w, x0, x1; };
struct Src { u32x6 wv; unsigned ml, rot; rsrc_t ra, rb; };
#define G_OUTS "=&{v[16:31]}"(gm.acc0), "=&{v[32:47]}"(gm.acc1), "=&{v[96:111]}"(gm.w), "=&{v[112:127]}"(gm.x0), "=&{v[128:143]}"(gm.x1)
#define G_SRC "{v[10:15]}"(sr.wv), [ra] "s"(sr.ra), [rb] "s"(sr.rb), [ml] "s"(sr.ml), [rot] "s"(sr.rot)
#define G_CLOB2 "memory", "scc", "v80", "v81", "v82", "v83", "v84", "v85", "v86", "v87", "v88", "v89", "v90", "v91", "v92", "v93", "v94", "v95"
#define G_CLOB "memory", "scc", "v48", "v49", "v50", "v51", "v52", "v53", "v54", "v55", "v56", "v57", "v58", "v59", "v60", "v61", "v62", "v63", \
  "v64", "v65", "v66", "v67", "v68", "v69", "v70", "v71", "v72", "v73", "v74", "v75", "v76", "v77", "v78", "v79",                              \
  "v80", "v81", "v82", "v83", "v84", "v85", "v86", "v87", "v88", "v89", "v90", "v91", "v92", "v93", "v94", "v95"
DI void first_chunks(const Src& sr, rsrc_t rx, unsigned xv0, unsigned xv1, unsigned mx, unsigned rotx) {
  unsigned tmp;
  asm volatile(G_DX(0) G_ALL(G_S0(0)) G_DX(1) G_ALL(G_S0(1))
               : [tmp] "=&s"(tmp)
               : G_SRC, [rx] "s"(rx), [xv0] "v"(xv0), [xv1] "v"(xv1), [mx] "s"(mx), [rotx] "s"(rotx)
               : "memory", "scc");
}
DI void gemm_stage0(int grp, Gemm& gm, u32x4 xa, unsigned wa, const Src& sr, rsrc_t rx, unsigned xv0, unsigned xv1, unsigned mx,
                    unsigned rotx, rsrc_t ws, u32x2& epv G_STP) {
  unsigned tmp;
  asm volatile(
      "s_cmp_eq_u32 %[grp], 0\n\t"
      "s_cbranch_scc1 3f\n\t"
      G_ST0(0)
      G_BAR(6) G_DX(2) G_ALL(G_S0(2)) G_BAR(6) G_DX(3) G_ALL(G_S0(3)) G_BAR(6) G_DX(4) G_ALL(G_S0(4))
      G_BAR(6) G_DX(5) G_ALL(G_S0(5)) G_BAR(6) G_DX(6) G_ALL(G_S0(6)) G_BAR(6) G_DX(7) G_ALL(G_S0(7))
      G_BAR(6) G_ALL(G_S1(8)) G_ALL(G_S1(9)) G_BAR(8) G_ALL(G_S1(10))
      "s_branch 4f\n\t"
      "3:\n\t"
      G_ST0(0) G_EPOCH
      "s_mov_b32 %[tmp], 0\n\t"
      ".irp r,16,18,20,22,24,26,28,30,32,34,36,38,40,42,44,46\n\tv_mov_b64 v[\\r:\\r+1], 0\n\t.endr\n\t"
      G_LBAR G_READ(48, 64, 80, 0)
      "s_branch 2f\n\t"
      "1:\n\t"
      G_BAR(0) G_EVEN(0, "", "", "", "")
      "2:\n\t"
      G_LBAR G_ODD(1, "", "", "", "")
      G_LBAR G_EVEN(2, "", "", "", "")
      G_LBAR G_ODD(3, "", "", "", "")
      ".irp s,0,1,2,3\n\tv_xor_b32 v[4+\\s], 0x8000, v[4+\\s]\n\t.endr\n\t"
      "s_xor_b32 %[tmp], %[tmp], 1\n\t"
      "s_cbranch_scc1 1b\n\t"
      "4:"
      : G_OUTS, "=&{v[2:3]}"(epv), [tmp] "=&s"(tmp) G_STOP
      : "{v[4:7]}"(xa), "{v8}"(wa), G_SRC, [ws] "s"(ws), [grp] "s"(grp), [rx] "s"(rx), [xv0] "v"(xv0), [xv1] "v"(xv1), [mx] "s"(mx),
        [rotx] "s"(rotx)
      : G_CLOB G_STC);
}
DI void gemm_stage1(Gemm& gm, u32x4 xa, unsigned wa, const Src& sr, rsrc_t ws, u32x2& epv G_STP) {
  unsigned tmp;
  asm volatile(
      G_ST0(16)
      G_BAR(8) "s_setprio 1\n\t" G_EPOCH G_READ(48, 64, 80, 0) G_ALL(G_S1(11))
      G_BAR(9) G_APPLY(G_FIRST, G_S1(12))
      G_BAR(9) G_APPLY(G_EVEN, 2, G_S1(13)) G_BAR(8) G_APPLY(G_ODD, 3, G_S1(14))
      G_BAR(8) G_APPLY(G_EVEN, 4, G_S1(15)) G_BAR(8) G_APPLY(G_ODD, 5, G_S2(16))
      G_BAR(8) G_APPLY(G_EVEN, 6, G_S2(17)) G_BAR(8) G_APPLY(G_ODD, 7, G_S2(18))
      : G_OUTS, "=&{v[2:3]}"(epv), [tmp] "=&s"(tmp) G_STOP
      : "{v[4:7]}"(xa), "{v8}"(wa), G_SRC, [ws] "s"(ws)
      : G_CLOB G_STC);
}
#define G_KV1(K, V, T)                                                                                \
  "buffer_load_dwordx4 v[" #K ":" #K "+3], v249, %[ws], %[" #T "] offen sc1\n\t"                       \
  "buffer_load_dwordx4 v[" #V ":" #V "+3], v250, %[ws], %[" #T "] offen offset:1024 sc1\n\t"           \
  "buffer_load_dwordx4 v[" #V "+4:" #V "+7], v251, %[ws], %[" #T "] offen offset:1536 sc1\n\t"
#define G_KN "buffer_load_dword v9, v9, %[ws], 0 offen sc1\n\t"
struct Kv0 { unsigned koff, voff0, voff1, t0, t1, t2; rsrc_t ws; };
#define G_KVOUT "=&{v[200:215]}"(at.kr), "=&{v[216:231]}"(at.vr0), "=&{v[232:247]}"(at.vr1), "+{v9}"(kn)
#define G_ZOUT "=&{v[160:175]}"(at.o0), "=&{v[176:191]}"(at.o1), "=&{v[144:159]}"(at.sb)
#define G_KVIN "{v249}"(kv.koff), "{v250}"(kv.voff0), "{v251}"(kv.voff1), [ws] "s"(kv.ws), [t0] "s"(kv.t0), [t1] "s"(kv.t1), [t2] "s"(kv.t2)
#define G_Z(L) ".irp r," L "\n\tv_mov_b64 v[\\r:\\r+1], 0\n\t.endr\n\t"
#define G_Z0 G_Z("152,154,156,158,244,246")
#define G_Z1 G_Z("160,162,164,166,168,170")
#define G_Z2 G_Z("172,174,176,178,180")
#define G_Z3 G_Z("182,184,186,188,190")
#define G_TAIL "s_waitcnt lgkmcnt(0)\n\t" G_MMA2(96, 112, 128, 0) G_MMA2(96, 112, 128, 1) G_MMA2(96, 112, 128, 2) G_MMA2(96, 112, 128, 3)
#define A_PACK                                                                                        \
  ".irp i,0,1,2,3\n\tv_cvt_pk_bf16_f32 v[48+\\i], v[16+2*\\i], v[17+2*\\i]\n\tv_cvt_pk_bf16_f32 v[56+\\i], v[24+2*\\i], v[25+2*\\i]\n\t.endr\n\t" \
  ".irp i,0,1,2,3\n\tv_cvt_pk_bf16_f32 v[52+\\i], v[32+2*\\i], v[33+2*\\i]\n\tv_cvt_pk_bf16_f32 v[60+\\i], v[40+2*\\i], v[41+2*\\i]\n\t.endr\n\t"
DI void gemm_stage2(Gemm& gm, u32x4 xa, unsigned wa, const Src& sr, const Kv0& kv, Attn& at, unsigned& kn,
                    u32x16& pa, u32x16& pb, unsigned qa G_STP) {
  unsigned tmp;
  asm volatile(
      G_ST0(32)
      G_LBAR "s_setprio 1\n\t" G_READ(48, 64, 80, 0) G_ALL(G_S2(19))
      G_LBAR G_APPLY(G_FIRST, G_S2(20))
      G_LBAR G_APPLY(G_EVEN, 2, G_S2(21)) G_BAR(8) G_APPLY(G_ODD, 3, G_S2(22))
      G_BAR(8) G_APPLY(G_EVEN, 4, G_S2(23)) G_BAR(8) G_ODD(5, G_KN, G_KV1(200, 216, t0), G_KV1(204, 224, t1), G_KV1(208, 232, t2))
      G_BAR(14) G_EVEN(6, G_Z0, G_Z1, G_Z2, G_Z3) G_BAR(10) G_ODD(7, "", "", "", "")
      G_TAIL
      "s_waitcnt vmcnt(0)\n\t"
      "s_nop 9\n\t"
      A_PACK
      ".irp i,0,1,2,3\n\tds_write_b128 %[qa], v[48+4*\\i:51+4*\\i] offset:1024*\\i\n\t.endr"
      : G_OUTS, G_KVOUT, G_ZOUT, "=&{v[48:63]}"(pa), "=&{v[64:79]}"(pb), [tmp] "=&s"(tmp) G_STOP
      : "{v[4:7]}"(xa), "{v8}"(wa), G_SRC, G_KVIN, [qa] "v"(qa)
      : G_CLOB2 G_STC);
}
#define F_JOBS(M) M(16, 80, 48) M(32, 84, 56) M(96, 88, 64) M(112, 92, 72)
#define F_MAX1(E, T, P)                                                                               \
  "v_max3_f32 v[" #T "], v[" #E "], v[" #E "+1], v[" #E "+2]\n\t"                                      \
  "v_max3_f32 v[" #T "+2], v[" #E "+8], v[" #E "+9], v[" #E "+10]\n\t"                                 \
  "v_max3_f32 v[" #T "+1], v[" #E "+3], v[" #E "+4], v[" #E "+5]\n\t"                                  \
  "v_max3_f32 v[" #T "+3], v[" #E "+11], v[" #E "+12], v[" #E "+13]\n\t"                               \
  "v_max3_f32 v[" #T "], v[" #T "], v[" #E "+6], v[" #E "+7]\n\t"                                      \
  "v_max3_f32 v[" #T "+2], v[" #T "+2], v[" #E "+14], v[" #E "+15]\n\t"                                \
  "v_max_f32 v[" #T "], v[" #T "], v[" #T "+1]\n\t"                                                    \
  "v_max_f32 v[" #T "+2], v[" #T "+2], v[" #T "+3]\n\t"
#define F_SWAP(E, T, P) "v_permlane32_swap_b32 v[" #T "], v[" #T "+2]\n\t"
#define F_MAX2(E, T, P) "v_max_f32 v[" #T "], v[" #T "], v[" #T "+2]\n\t"
#define F_NEG(E, T, P) "v_mul_f32 v[" #T "], 0xbfb8aa3b, v[" #T "]\n\t"
#define F_DUP(E, T, P) "v_mov_b32 v[" #T "+2], v[" #T "]\n\t"
#define F_FMA(E, T)                                                                                   \
  ".irp i,0,1,2,3,4,5,6,7\n\t"                                                                        \
  "v_fma_f32 v[" #E "+\\i], v[" #E "+\\i], %[l2e], v[" #T "]\n\t"                                      \
  "v_fma_f32 v[" #E "+8+\\i], v[" #E "+8+\\i], %[l2e], v[" #T "+2]\n\t"                                \
  ".endr\n\t"
#define F_EX(E, L) ".irp i," L "\n\tv_exp_f32 v[" #E "+\\i], v[" #E "+\\i]\n\t.endr\n\t"
#define F_EXP(E, T) F_FMA(E, T) F_EX(E, "0,8,1,9,2,10,3,11,4,12,5,13,6,14,7,15")
#define F_SUM1(E, T, P) ".irp i,0,1,2,3,4,5,6,7\n\tv_add_f32 v[" #P "+\\i], v[" #E "+2*\\i], v[" #E "+2*\\i+1]\n\t.endr\n\t"
#define F_SUM2(E, T, P) ".irp i,0,2,4,6\n\tv_add_f32 v[" #P "+\\i], v[" #P "+\\i], v[" #P "+\\i+1]\n\t.endr\n\t"
#define F_SUM3(E, T, P) "v_add_f32 v[" #T "], v[" #P "], v[" #P "+2]\n\tv_add_f32 v[" #T "+2], v[" #P "+4], v[" #P "+6]\n\t"
#define F_ADD(E, T, P) "v_add_f32 v[" #T "], v[" #T "], v[" #T "+2]\n\t"
#define F_RCP(E, T, P) "v_rcp_f32 v[" #T "], v[" #T "]\n\t"
#define F_PACK(E, T, P) ".irp i,0,1,2,3,4,5,6,7\n\tv_cvt_pk_bf16_f32 v[" #P "+\\i], v[" #E "+2*\\i], v[" #E "+2*\\i+1]\n\t.endr\n\t"
#define F_SCALE(E, T, P)                                                                              \
  ".irp i,0,1,2,3,4,5,6,7\n\t"                                                                        \
  "v_mul_f32 v[" #E "+\\i], v[" #E "+\\i], v[" #T "]\n\t"                                              \
  "v_mul_f32 v[" #E "+8+\\i], v[" #E "+8+\\i], v[" #T "+2]\n\t"                                        \
  ".endr\n\t" F_PACK(E, T, P)
#define F_TR0(E, T, P) "v_mfma_f32_32x32x16_bf16 v[" #E ":" #E "+15], v[" #P ":" #P "+3], v[128:131], 0\n\t"
#define F_TR1(E, T, P) "v_mfma_f32_32x32x16_bf16 v[" #E ":" #E "+15], v[" #P "+4:" #P "+7], v[132:135], v[" #E ":" #E "+15]\n\t"
#define F_FLAGS "buffer_load_dwordx2 v[136:137], %[fo], %[ws], 0 offen sc1\n\t"
struct Side { unsigned wpa, sgn, gpa, fo, pqa, val, hot; };
DI void side_stage2(Gemm& gm, const Side& sd, int is_k, u32x2 ep, const Kv0& kv, Attn& at, unsigned& kn, float& gate, u32x16& pa,
                    u32x16& pb G_STP) {
  unsigned long long cc;
  unsigned l2e = 0x3FB8AA3Bu;
  unsigned spins = SPIN_CAP;
  asm volatile(
      G_ST0(32) G_BAR(8)
      "s_setprio 0\n\t"
      "ds_read_b64_tr_b16 v[64:65], %[wpa]\n\tds_read_b64_tr_b16 v[66:67], %[wpa] offset:256\n\t"
      "ds_read_b64_tr_b16 v[68:69], %[wpa] offset:512\n\tds_read_b64_tr_b16 v[70:71], %[wpa] offset:768\n\t"
      "ds_read2st64_b32 v[72:73], %[gpa] offset1:2\n\tds_read2st64_b32 v[74:75], %[gpa] offset0:4 offset1:6\n\t"
      G_MMA2(96, 112, 128, 0) G_MMA2(96, 112, 128, 1)
      "s_waitcnt lgkmcnt(0)\n\t"
      "v_add_f32 v72, v72, v74\n\tv_add_f32 v73, v73, v75\n\t"
      ".irp r,64,65,66,67\n\tv_xor_b32 v\\r, %[sgn], v\\r\n\t.endr\n\t"
      "v_add_f32 v72, v72, v73\n\t"
      G_MMA2(96, 112, 128, 2)
      "v_mov_b32 v73, v72\n\t"
      ".irp r,68,69,70,71\n\tv_xor_b32 v\\r, %[sgn], v\\r\n\t.endr\n\t"
      "v_permlane32_swap_b32 v72, v73\n\t"
      G_MMA2(96, 112, 128, 3)
      "v_add_f32 v72, v72, v73\n\t"
      "v_mul_f32 v72, 0xbfb8aa3b, v72\n\t"
      "v_exp_f32 v72, v72\n\t"
      "s_nop 0\n\t"
      "v_add_f32 v72, 1.0, v72\n\t"
      "v_rcp_f32 v253, v72\n\t"
      "s_nop 5\n\t"
      A_PACK
      G_BAR(4)
      "s_setprio 2\n\t"
      "v_mfma_f32_32x32x16_bf16 v[16:31], v[64:67], v[48:51], 0\n\t"
      "v_mfma_f32_32x32x16_bf16 v[32:47], v[64:67], v[52:55], 0\n\t"
      ".irp j,0,1,2,3\n\tv_bfe_i32 v[128+\\j], %[hot], \\j, 1\n\t.endr\n\t"
      "v_mfma_f32_32x32x16_bf16 v[96:111], v[68:71], v[56:59], 0\n\t"
      ".irp j,4,5,6,7\n\tv_bfe_i32 v[128+\\j], %[hot], \\j, 1\n\t.endr\n\t"
      "v_mfma_f32_32x32x16_bf16 v[112:127], v[68:71], v[60:63], 0\n\t"
      "s_setprio 0\n\t"
      ".irp j,0,1,2,3,4,5,6,7\n\tv_and_b32 v[128+\\j], %[val], v[128+\\j]\n\t.endr\n\t"
      F_JOBS(F_MAX1) "s_nop 0\n\t" F_JOBS(F_SWAP) F_JOBS(F_MAX2) F_JOBS(F_NEG) F_JOBS(F_DUP) F_JOBS(F_SWAP)
      G_BAR(0)
      F_EXP(16, 80) F_FMA(32, 84) F_EX(32, "0,8,1,9")
      G_LBAR
      F_FLAGS F_EX(32, "2,10,3,11,4,12,5,13,6,14,7,15") F_EXP(96, 88)
      G_LBAR
      F_EXP(112, 92) F_SUM1(16, 80, 48) F_SUM1(32, 84, 56)
      "1:\n\t"
      "s_waitcnt vmcnt(0)\n\t"
      "v_cmp_ne_u32 vcc, %[e0], v136\n\t"
      "v_cmp_ne_u32 %[cc], %[e1], v137\n\t"
      "s_or_b64 vcc, vcc, %[cc]\n\t"
      "s_cbranch_vccz 2f\n\t"
      "s_sleep 2\n\t"
      F_FLAGS
      "s_sub_u32 %[spins], %[spins], 1\n\t"
      "s_cbranch_scc0 1b\n\t"
      "s_waitcnt vmcnt(0)\n\t"
      "2:\n\t"
      G_LBAR
      F_SUM1(96, 88, 64) F_SUM1(112, 92, 72) F_JOBS(F_SUM2) F_JOBS(F_SUM3) F_JOBS(F_SWAP) F_JOBS(F_ADD) F_JOBS(F_RCP) F_JOBS(F_DUP)
      F_JOBS(F_SWAP)
      G_LBAR
      G_KN G_KV1(200, 216, t0) F_SCALE(16, 80, 48) G_KV1(204, 224, t1) F_SCALE(32, 84, 56) G_KV1(208, 232, t2) F_SCALE(96, 88, 64)
      G_LBAR
      F_SCALE(112, 92, 72)
      "s_cmp_lg_u32 %[isk], 0\n\t"
      "s_cbranch_scc1 3f\n\t"
      ".irp i,0,1,2,3,4,5,6,7\n\tds_write_b128 %[pqa], v[48+4*\\i:51+4*\\i] offset:1024*\\i\n\t.endr\n\t"
      G_Z0 G_Z1 G_Z2 G_Z3
      "s_branch 4f\n\t"
      "3:\n\t"
      F_JOBS(F_TR0) F_JOBS(F_TR1) G_Z0 G_Z1 G_Z2 G_Z3 F_JOBS(F_PACK)
      "4:\n\t"
      "s_waitcnt vmcnt(0)"
      : "+{v[16:31]}"(gm.acc0), "+{v[32:47]}"(gm.acc1), "+{v[96:111]}"(gm.w), "+{v[112:127]}"(gm.x0), "+{v[128:143]}"(gm.x1),
        "=&{v[48:63]}"(pa), "=&{v[64:79]}"(pb), "=&{v253}"(gate), G_KVOUT, G_ZOUT, [cc] "=&s"(cc), [spins] "+s"(spins) G_STOP
      : [wpa] "v"(sd.wpa), [sgn] "v"(sd.sgn), [gpa] "v"(sd.gpa), [fo] "v"(sd.fo), [pqa] "v"(sd.pqa), [val] "v"(sd.val), [hot] "v"(sd.hot),
        [isk] "s"(is_k), [e0] "s"(ep[0]), [e1] "s"(ep[1]), [l2e] "s"(l2e), G_KVIN
      : "memory", "scc", "vcc", "v80", "v81", "v82", "v83", "v84", "v85", "v86", "v87", "v88", "v89", "v90", "v91", "v92", "v93", "v94",
        "v95" G_STC);
}
#define A_GREAD(C)                                                                                    \
  "v_xor_b32 v92, 128*" #C ", %[gwa]\n\tv_xor_b32 v93, 128*(" #C "+1), %[gwa]\n\t"                     \
  "ds_read_b128 v[72:75], %[gx] offset:8192*" #C "\n\tds_read_b128 v[80:83], %[gx] offset:8192*" #C "+4096\n\t" \
  "ds_read_b128 v[76:79], %[gx] offset:8192*(" #C "+1)\n\tds_read_b128 v[84:87], %[gx] offset:8192*(" #C "+1)+4096\n\t" \
  "ds_read_b128 v[64:67], v92\n\tds_read_b128 v[68:71], v93\n\t"
#define A_GDOT                                                                                        \
  ".irp j,0,1,2,3\n\t"                                                                                \
  "v_dot2c_f32_bf16 v88, v[72+\\j], v[64+\\j]\n\tv_dot2c_f32_bf16 v89, v[80+\\j], v[64+\\j]\n\t"       \
  "v_dot2c_f32_bf16 v90, v[76+\\j], v[68+\\j]\n\tv_dot2c_f32_bf16 v91, v[84+\\j], v[68+\\j]\n\t"       \
  ".endr\n\t"
#define A_DPPMAX(CTRL) "s_nop 1\n\tv_max_f32_dpp v96, v96, v96 " CTRL " row_mask:0xf bank_mask:0xf\n\t"
#define A_ST(R, O) "buffer_store_dwordx4 v[" #R ":" #R "+3], " O " sc1\n\t"
struct SideA { unsigned so, so2, so3, gx, gwa, gpa, fo; };
DI void side_stage1(Gemm& gm, const SideA& sa, unsigned val, unsigned hot, int is_v, rsrc_t ws, u32x2 ep, float& gate0, float& gate1 G_STP) {
  unsigned ks = 0x3EB8AA3Bu, far = 131072;
  asm volatile(
      G_ST0(16) G_LBAR
      G_MMA2(96, 112, 128, 0) G_MMA2(96, 112, 128, 1) G_MMA2(96, 112, 128, 2) G_MMA2(96, 112, 128, 3)
      "s_cmp_lg_u32 %[isv], 0\n\t"
      ".irp r,88,90,92,94\n\tv_mov_b64 v[\\r:\\r+1], 0\n\t.endr\n\t"
      "s_cbranch_scc1 1f\n\t"
      "s_nop 5\n\t"
      ".irp r,16,17,18,19,20,21,22,23,24,25,26,27,28,29,30,31\n\tv_mul_f32 v\\r, %[ks], v\\r\n\t.endr\n\t"
      G_LBAR
      ".irp r,32,33,34,35,36,37,38,39,40,41,42,43,44,45,46,47\n\tv_mul_f32 v\\r, %[ks], v\\r\n\t.endr\n\t"
      A_PACK
      A_ST(48, "%[so], %[ws], 0 offen") A_ST(52, "%[so], %[ws], 0 offen offset:2048")
      ".irp j,0,1,2,3\n\t"
      "v_dot2c_f32_bf16 v92, v[48+\\j], v[48+\\j]\n\tv_dot2c_f32_bf16 v93, v[52+\\j], v[52+\\j]\n\t"
      "v_dot2c_f32_bf16 v94, v[56+\\j], v[56+\\j]\n\tv_dot2c_f32_bf16 v95, v[60+\\j], v[60+\\j]\n\t"
      ".endr\n\t"
      G_LBAR
      A_ST(56, "%[so], %[ws], %[far] offen") A_ST(60, "%[so], %[ws], %[far] offen offset:2048")
      "v_permlane32_swap_b32 v92, v94\n\tv_permlane32_swap_b32 v93, v95\n\t"
      "v_add_f32 v96, v92, v94\n\tv_add_f32 v97, v93, v95\n\t"
      A_GREAD(0)
      "v_max_f32 v96, v96, v97\n\t"
      A_DPPMAX("quad_perm:[1,0,3,2]") A_DPPMAX("quad_perm:[2,3,0,1]") A_DPPMAX("row_half_mirror") A_DPPMAX("row_mirror")
      "v_mov_b32 v97, v96\n\ts_nop 1\n\tv_permlane16_swap_b32 v96, v97\n\tv_max_f32 v96, v96, v97\n\t"
      "buffer_store_dword v96, %[so2], %[ws], 0 offen sc1\n\t"
      "v_mov_b32 v100, -1\n\t"
      "buffer_store_dword v100, %[so3], %[ws], 0 offen sc1\n\t"
      "s_branch 2f\n\t"
      "1:\n\t"
      ".irp j,0,1,2,3,4,5,6,7\n\tv_bfe_i32 v[96+\\j], %[hot], \\j, 1\n\t.endr\n\t"
      A_PACK
      ".irp j,0,1,2,3,4,5,6,7\n\tv_and_b32 v[96+\\j], %[val], v[96+\\j]\n\t.endr\n\t"
      G_LBAR
      "s_setprio 2\n\t"
      "v_mfma_f32_32x32x16_bf16 v[16:31], v[48:51], v[96:99], 0\n\t"
      "v_mfma_f32_32x32x16_bf16 v[32:47], v[52:55], v[96:99], 0\n\t"
      "v_mfma_f32_32x32x16_bf16 v[16:31], v[56:59], v[100:103], v[16:31]\n\t"
      "v_mfma_f32_32x32x16_bf16 v[32:47], v[60:63], v[100:103], v[32:47]\n\t"
      "s_setprio 0\n\t"
      ".irp r,104,105,106,107\n\tv_mov_b32 v\\r, 0x3f803f80\n\t.endr\n\t"
      A_ST(104, "%[so2], %[ws], 0 offen")
      "s_nop 5\n\t"
      F_PACK(16, 0, 48)
      A_ST(48, "%[so], %[ws], 0 offen") A_ST(52, "%[so], %[ws], 0 offen offset:512")
      G_LBAR
      F_PACK(32, 0, 56)
      A_ST(56, "%[so], %[ws], 0 offen offset:2048") A_ST(60, "%[so], %[ws], 0 offen offset:2560")
      A_GREAD(0)
      "2:\n\t"
      G_LBAR A_GDOT A_GREAD(2)
      G_LBAR A_GDOT A_GREAD(4)
      G_LBAR A_GDOT A_GREAD(6)
      G_LBAR A_GDOT
      "s_nop 2\n\t"
      "v_add_f32 v88, v88, v90\n\tv_add_f32 v89, v89, v91\n\t"
      "ds_write2st64_b32 %[gpa], v88, v89 offset1:1\n\t"
      "v_and_b32 v92, 0xfffff0ff, %[gpa]\n\t"
      "v_mov_b32 v94, %[e0]\n\tv_mov_b32 v95, %[e1]\n\t"
      G_BAR(0)
      "ds_read2st64_b32 v[64:65], v92 offset1:1\n\tds_read2st64_b32 v[66:67], v92 offset0:2 offset1:3\n\t"
      "ds_read2st64_b32 v[68:69], v92 offset0:4 offset1:5\n\tds_read2st64_b32 v[70:71], v92 offset0:6 offset1:7\n\t"
      "buffer_store_dwordx2 v[94:95], %[fo], %[ws], 0 offen sc1\n\t"
      "s_waitcnt lgkmcnt(0)\n\t"
      "v_add_f32 v64, v64, v66\n\tv_add_f32 v65, v65, v67\n\tv_add_f32 v68, v68, v70\n\tv_add_f32 v69, v69, v71\n\t"
      "v_add_f32 v64, v64, v68\n\tv_add_f32 v65, v65, v69\n\t"
      "s_nop 1\n\tv_permlane32_swap_b32 v64, v65\n\t"
      "v_add_f32 v64, v64, v65\n\tv_mul_f32 v64, 0xbfb8aa3b, v64\n\tv_exp_f32 v64, v64\n\ts_nop 0\n\t"
      "v_add_f32 v64, 1.0, v64\n\tv_rcp_f32 v252, v64\n\ts_nop 0\n\tv_mov_b32 v253, v252\n\t"
      "s_nop 1\n\tv_permlane32_swap_b32 v252, v253"
      : "+{v[16:31]}"(gm.acc0), "+{v[32:47]}"(gm.acc1), "+{v[96:111]}"(gm.w), "+{v[112:127]}"(gm.x0), "+{v[128:143]}"(gm.x1),
        "=&{v252}"(gate0), "=&{v253}"(gate1) G_STOP
      : [so] "v"(sa.so), [so2] "v"(sa.so2), [so3] "v"(sa.so3), [gx] "v"(sa.gx), [gwa] "v"(sa.gwa), [gpa] "v"(sa.gpa), [fo] "v"(sa.fo), [val] "v"(val),
        [hot] "v"(hot), [isv] "s"(is_v), [ws] "s"(ws), [e0] "s"(ep[0]), [e1] "s"(ep[1]), [ks] "s"(ks), [far] "s"(far)
      : G_CLOB G_STC);
}
DI void identity_seed(int r32, int h, unsigned& val, unsigned& hot) {
  val = 0x3F80u << ((r32 & 1) * 16);
  hot = (((r32 >> 2) & 1) == h ? 1u : 0u) << (((r32 >> 1) & 1) | ((r32 >> 2) & 6));
}
DI u32x8 pack16(const f32x16& t) { return __builtin_shufflevector(pack8<0>(t), pack8<8>(t), 0, 1, 2, 3, 4, 5, 6, 7); }

#define P_TEST(F, I)                                                                                  \
  "v_max3_u32 v80, v[" #F "+4*" #I "], v[" #F "+1+4*" #I "], v[" #F "+2+4*" #I "]\n\t"                 \
  "v_max_u32 v80, v80, v[" #F "+3+4*" #I "]\n\t"                                                       \
  "v_cmp_eq_u32 vcc, -1, v80\n\t"
#define P_LOAD(F, I, SO) "buffer_load_dwordx4 v[" #F "+4*" #I ":" #F "+3+4*" #I "], %[vo], %[ws], %[" #SO "] offen offset:1024*(" #I "&3) sc1\n\t"
#define P_RD(R, I, OFF)                                                                               \
  "ds_read_b64_tr_b16 v[" R ":" R "+1], %[w0] offset:" #OFF "+2048*" #I "\n\t"                         \
  "ds_read_b64_tr_b16 v[" R "+2:" R "+3], %[w0] offset:" #OFF "+2048*" #I "+1024\n\t"                  \
  "ds_read_b64_tr_b16 v[" R "+4:" R "+5], %[w1] offset:" #OFF "+2048*" #I "\n\t"                       \
  "ds_read_b64_tr_b16 v[" R "+6:" R "+7], %[w1] offset:" #OFF "+2048*" #I "+1024\n\t"
#define P_RB(I) P_RD("192+8*(" #I "&3)", I, 0)
#define P_RA(I) P_RD("128+8*" #I, I, 16384)
#define P_MM(W, F, I, C0, C1)                                                                         \
  "v_mfma_f32_32x32x16_bf16 v[16:31], v[" W ":" W "+3], v[" #F "+4*" #I ":" #F "+3+4*" #I "], " C0 "\n\t"   \
  "v_mfma_f32_32x32x16_bf16 v[32:47], v[" W "+4:" W "+7], v[" #F "+4*" #I ":" #F "+3+4*" #I "], " C1 "\n\t"
#define P_USEB(I, N, C0, C1, MORE)                                                                    \
  "s_waitcnt lgkmcnt(" #N ")\n\t" P_MM("192+8*(" #I "&3)", 48, I, C0, C1) MORE
#define P_USEA(I, N) "s_waitcnt vmcnt(" #N ")\n\t" P_TEST(96, I) "s_cbranch_vccnz 7" #I "f\n\t6" #I ":\n\t" P_MM("128+8*" #I, 96, I, G_A0, G_A1)
#define P_STUB(A, B, I, R) #A #I ":\n\ts_call_b64 %[ret], " #R "f\n\ts_branch " #B #I "b\n\t"
#define P_STUBS(A, B, R) P_STUB(A, B, 0, R) P_STUB(A, B, 1, R) P_STUB(A, B, 2, R) P_STUB(A, B, 3, R) P_STUB(A, B, 4, R) P_STUB(A, B, 5, R) P_STUB(A, B, 6, R) P_STUB(A, B, 7, R)
#define P_RETRY(F, I, SO) P_TEST(F, I) "s_cbranch_vccz 1f\n\ts_mov_b32 %[any], 1\n\t" P_LOAD(F, I, SO) "1:\n\t"
#define P_SLOW(R, F, S0, S1)                                                                          \
  #R ":\n\ts_waitcnt vmcnt(0)\n\ts_mov_b32 %[any], 0\n\t"                                             \
  P_RETRY(F, 0, S0) P_RETRY(F, 1, S0) P_RETRY(F, 2, S0) P_RETRY(F, 3, S0) P_RETRY(F, 4, S1) P_RETRY(F, 5, S1) P_RETRY(F, 6, S1) P_RETRY(F, 7, S1) \
  "s_sub_u32 %[spins], %[spins], 1\n\ts_cselect_b32 %[any], 0, %[any]\n\ts_cselect_b32 %[spins], 0, %[spins]\n\t" \
  "s_cmp_lg_u32 %[any], 0\n\ts_cbranch_scc1 " #R "b\n\ts_waitcnt vmcnt(0)\n\ts_setpc_b64 %[ret]\n\t"
#define P_LOADA P_LOAD(96, 0, a0) P_LOAD(96, 1, a0) P_LOAD(96, 2, a0) P_LOAD(96, 3, a0) P_LOAD(96, 4, a1) P_LOAD(96, 5, a1) P_LOAD(96, 6, a1) P_LOAD(96, 7, a1)
#define A_AT 7
#define P_AT(I) ".if " STR(A_AT) " == " #I "\n\t" P_LOADA ".endif\n\t"
#define STR_(x) #x
#define STR(x) STR_(x)
DI void project(f32x16& d0, f32x16& d1, u32x16 fb0, u32x16 fb1, unsigned w0, rsrc_t ws, unsigned vo, unsigned ob) {
  unsigned long long ret;
  unsigned any, spins = SPIN_CAP, w1 = w0 ^ 64, a0 = WS_OA + ob, a1 = a0 + 4096;
  asm volatile(
      P_RB(0) P_RB(1)
      P_USEB(0, 4, "0", "0", P_RB(2) P_RA(0) P_AT(0)) P_USEB(1, 8, G_A0, G_A1, P_RB(3) P_RA(1) P_AT(1))
      P_USEB(2, 12, G_A0, G_A1, P_RB(4) P_RA(2) P_AT(2)) P_USEB(3, 12, G_A0, G_A1, P_RB(5) P_RA(3) P_AT(3))
      P_USEB(4, 12, G_A0, G_A1, P_RB(6) P_RA(4) P_AT(4)) P_USEB(5, 12, G_A0, G_A1, P_RB(7) P_RA(5) P_AT(5))
      P_USEB(6, 12, G_A0, G_A1, P_RA(6) P_AT(6)) P_USEB(7, 8, G_A0, G_A1, P_RA(7) P_AT(7))
      "s_waitcnt lgkmcnt(0)\n\t"
      "s_barrier\n\t"
      P_USEA(0, 7) P_USEA(1, 6) P_USEA(2, 5) P_USEA(3, 4) P_USEA(4, 3) P_USEA(5, 2) P_USEA(6, 1) P_USEA(7, 0)
      "s_branch 9f\n\t"
      P_STUBS(7, 6, 3) P_SLOW(3, 96, a0, a1)
      "9:\n\t"
      "s_nop 11"
      : "=&{v[16:31]}"(d0), "=&{v[32:47]}"(d1), [ret] "=&s"(ret), [any] "=&s"(any), [spins] "+s"(spins)
      : "{v[48:63]}"(fb0), "{v[64:79]}"(fb1), [w0] "v"(w0), [w1] "v"(w1), [vo] "v"(vo), [ws] "s"(ws), [a0] "s"(a0), [a1] "s"(a1)
      : "memory", "scc", "vcc", "v80",
#define R8(a, b, c, d, e, f, g, h) "v" #a, "v" #b, "v" #c, "v" #d, "v" #e, "v" #f, "v" #g, "v" #h
        R8(96, 97, 98, 99, 100, 101, 102, 103), R8(104, 105, 106, 107, 108, 109, 110, 111), R8(112, 113, 114, 115, 116, 117, 118, 119),
        R8(120, 121, 122, 123, 124, 125, 126, 127), R8(128, 129, 130, 131, 132, 133, 134, 135), R8(136, 137, 138, 139, 140, 141, 142, 143),
        R8(144, 145, 146, 147, 148, 149, 150, 151), R8(152, 153, 154, 155, 156, 157, 158, 159), R8(160, 161, 162, 163, 164, 165, 166, 167),
        R8(168, 169, 170, 171, 172, 173, 174, 175), R8(176, 177, 178, 179, 180, 181, 182, 183), R8(184, 185, 186, 187, 188, 189, 190, 191),
        R8(192, 193, 194, 195, 196, 197, 198, 199), R8(200, 201, 202, 203, 204, 205, 206, 207), R8(208, 209, 210, 211, 212, 213, 214, 215),
        R8(216, 217, 218, 219, 220, 221, 222, 223));
}
DI bool marked(const u32x4& v) { return max(max(v[0], v[1]), max(v[2], v[3])) == SENT; }
DI u32x4 unmarked(const u32x4& v) { return __builtin_elementwise_min(v, u32x4{SENT - 1, SENT - 1, SENT - 1, SENT - 1}); }

#define KVST(r, o, v) ws_store(r, o, v)
#define HG 2
#define KP_GAP 0
#define ALLONES 1
#define CODE_LINES 160

__global__ __launch_bounds__(512, 2) void gated_mix_kernel(const char* __restrict__ x, const char* __restrict__ wqa,
                                                           const char* __restrict__ woa, const char* __restrict__ wqb,
                                                           const char* __restrict__ wphi, const char* __restrict__ wob,
                                                           const char* __restrict__ gatew, char* __restrict__ out,
                                                           char* __restrict__ ws) {
  __shared__ __attribute__((aligned(16))) char lds_raw[L_BYTES];
  LAS char* lds = (LAS char*)lds_raw;
  const unsigned lds0 = (unsigned)(uintptr_t)lds;
  const int tid = threadIdx.x, lane = tid & 63, w = __builtin_amdgcn_readfirstlane(tid >> 6);
  const int xc = blockIdx.x & 7, in = blockIdx.x >> 3;
  const int g = HG * (xc % (8 / HG)) + in % HG, tb = (32 / HG) * (xc / (8 / HG)) + in / HG;
  const int r32 = lane & 31, h = lane >> 5;
  const int wi = w & 3, grp = w >> 2;

  const rsrc_t rwa = __builtin_amdgcn_make_buffer_rsrc((void*)wqa, 0, 512 * 3072, 0x00020000);
  const rsrc_t rwb = __builtin_amdgcn_make_buffer_rsrc((void*)wqb, 0, 512 * 3072, 0x00020000);
  const rsrc_t rws = __builtin_amdgcn_make_buffer_rsrc((void*)ws, 0, WS_END, 0x00020000);
  STAMP(0); STAMP_RT(62);

  u32x2 epv;
  const int l8 = lane >> 3, dcp = lane & 7;
  Src sr;
  const unsigned wv = (unsigned)(16 * wi + l8) * 3072 + g * 128 + ((dcp ^ ((lane >> 2) & 4)) * 16);
#pragma unroll
  for (int c = 0; c < 3; ++c) { sr.wv[2 * c] = wv + c * 1024; sr.wv[2 * c + 1] = wv + c * 1024 + 8 * 3072 - 1024; }
  sr.ml = lds0 + L_RING + wi * 2048; sr.ra = rwa; sr.rb = rwb;
  sr.rot = (in / HG + (8 / HG) * (in % HG)) & 7;
  const rsrc_t rx = __builtin_amdgcn_make_buffer_rsrc((void*)x, 0, 2048 * 1024, 0x00020000);
  const unsigned xv0 = (unsigned)(64 * tb + 16 * wi + l8) * 1024 + ((dcp ^ (l8 >> 1)) * 16), xv1 = ((xv0 + 8192) ^ 64) - 1024;
  if (grp) first_chunks(sr, rx, xv0, xv1, lds0 + L_X + wi * 2048, sr.rot * 128);
  SBAR();
  const int pr = wi & 1;
  u32x4 xa;
#pragma unroll
  for (int s = 0; s < 4; ++s) xa[s] = L_X + r32 * 128 + (((2 * s + h) ^ ((r32 >> 1) & 7)) * 16);
  const int tq = (lane & 15) >> 2, tp = lane & 3, tnb = (lane >> 4) & 1;
  unsigned wbase = L_RING + (wi >> 1) * 8192 + (8 * h + tq) * 128 + (((2 * pr + tnb) ^ (tq & 2)) * 32) + tp * 8;
  if (grp == 0) {
    if (wi < 2) dma16(__builtin_amdgcn_make_buffer_rsrc((void*)wphi, 0, 32 * 512, 0x00020000), lane * 16, g * 2048 + wi * 1024, lds0 + L_WPHI + wi * 1024);
    if (wi == 2) dma16(__builtin_amdgcn_make_buffer_rsrc((void*)gatew, 0, 1024, 0x00020000), lane * 16, 0, lds0 + L_GW);
    if (wi == 3) {
      unsigned long long pc;
      unsigned len;
      asm volatile("s_getpc_b64 %0\n.Lgm_code_from:\n\ts_mov_b32 %1, .Lgm_code_to-.Lgm_code_from" : "=s"(pc), "=s"(len));
      const unsigned line = in + 32 * l8;
      const rsrc_t rc = __builtin_amdgcn_make_buffer_rsrc((void*)pc, 0, len, 0x00020000);
      asm volatile("s_mov_b32 m0, %0\n\ts_nop 0\n\tbuffer_load_dword %1, %2, 0 offen lds" :: "s"(lds0 + L_DUMMY), "v"(line * 128), "s"(rc) : "memory");
    }
  }
  const int hd = 4 * g + wi;
  const unsigned kvb = WS_KV + (unsigned)hd * 131072, kvh = kvb + grp * 65536;
  const unsigned koff = kvh + lane * 16;
  const unsigned vreal = kvh + h * 256 + (lane & 15) * 16, vones = WS_ONES + g * 256;
  const bool pad = lane & 16, one = (lane & 11) == 0;
  const unsigned voff0 = pad ? (one || ALLONES ? vones - 1024 : OOB) : vreal, voff1 = pad ? (one || ALLONES ? vones - 1536 : OOB) : vreal;
  Attn at;
  unsigned kn = WS_KN + (hd * 32 + r32) * 4;
  Kv0 kv0;
  kv0.koff = koff; kv0.voff0 = voff0; kv0.voff1 = voff1; kv0.ws = rws;
  kv0.t0 = tb * 2048; kv0.t1 = ((tb + 1) & 31) * 2048; kv0.t2 = ((tb + 2) & 31) * 2048;
  Side sd;
  sd.wpa = L_WPHI + 2 * pr * 512 + (4 * h + tq) * 32 + tp * 8; sd.sgn = tnb ? 0x80008000u : 0u;
  sd.gpa = L_GP + 256 + lane * 4; sd.fo = WS_FKV + (g * 32 + r32) * FL; sd.pqa = L_PQ + 2 * pr * 4096 + lane * 16;
  identity_seed(r32, h, sd.val, sd.hot);
  SideA sa;
  {
    const unsigned kb = WS_KV + (unsigned)(4 * g + 2 * pr) * 131072 + (unsigned)(2 * tb) * 2048;
    const int sw = (r32 & 19) | ((r32 & 4) << 1) | ((r32 & 8) >> 1);
    sa.so = wi < 2 ? kb + h * 512 + sw * 16 : kb + (r32 >> 4) * 131072 + 1024 + h * 256 + (r32 & 15) * 16;
    sa.so2 = wi < 2 ? (r32 == 0 ? WS_KN + (unsigned)((4 * g + 2 * pr + h) * 32 + tb) * 4 : OOB)
                    : (wi == 2 && lane < 8 ? WS_ONES + tb * 2048 + g * 256 + lane * 16 : OOB);
    sa.so3 = w == 0 ? WS_KR + ((4 * g + (tb & 3)) * 8 + (tb >> 2)) * 256 + lane * 4 : OOB;
    sa.gx = L_X + r32 * 128 + (((2 * wi + h) ^ ((r32 >> 1) & 7)) * 16);
    sa.gwa = L_GW + sr.rot * 128 + wi * 32 + h * 16;
    sa.gpa = L_GP + wi * 512 + lane * 4;
    sa.fo = tid == 0 ? WS_FKV + (g * 32 + tb) * FL : OOB;
  }
  unsigned qa = (wi < 2 ? L_Q : L_VB) + pr * 4096 + lane * 16;
  asm volatile("" : "+v"(sa.so), "+v"(sa.so2), "+v"(sa.so3), "+v"(sa.gx), "+v"(sa.gwa), "+v"(sa.gpa), "+v"(sa.fo), "+v"(qa));
  asm volatile("" : "+v"(sd.wpa), "+v"(sd.sgn), "+v"(sd.gpa), "+v"(sd.fo), "+v"(sd.pqa), "+v"(sd.val), "+v"(sd.hot), "+{v9}"(kn),
                    "+{v249}"(kv0.koff), "+{v250}"(kv0.voff0), "+{v251}"(kv0.voff1));
  Gemm gm;
  gemm_stage0(grp, gm, xa, wbase, sr, rx, xv0, xv1, lds0 + L_X + wi * 2048, sr.rot * 128, rws, epv G_STA);

  auto epoch = [&]() {
    const unsigned long long e = (((unsigned long long)(unsigned)__builtin_amdgcn_readfirstlane(epv[1]) << 32) |
                                  (unsigned)__builtin_amdgcn_readfirstlane(epv[0])) + 1ull;
    return u32x2{(unsigned)e, (unsigned)(e >> 32)};
  };
  u32x2 ep;
  float gate0 = 0.f, gate1;
  u32x4 idb;
  u32x16 pa, pb;
  if (grp) {
    STAMP(1);
    gemm_stage1(gm, xa, wbase, sr, rws, epv G_STA);
    ep = epoch();
    STAMP(2);
    side_stage2(gm, sd, wi >> 1, ep, kv0, at, kn, gate1, pa, pb G_STA);
    idb = __builtin_shufflevector(gm.x1, gm.x1, 0, 1, 2, 3);
    asm volatile("" : "={v[2:3]}"(epv), "={v[4:7]}"(xa), "={v8}"(wbase), "={v[10:15]}"(sr.wv), "=v"(sa.so), "=v"(sa.so2), "=v"(sa.so3), "=v"(sa.gx),
                      "=v"(sa.gwa), "=v"(sa.gpa), "=v"(sa.fo), "=v"(qa), "=v"(sd.val), "=v"(sd.hot));
  }
  int is_a = grp ^ 1;
  asm volatile("" : "+s"(is_a));
  if (is_a) {
    ep = epoch();
    STAMP(1);
    side_stage1(gm, sa, sd.val, sd.hot, wi >> 1, rws, ep, gate0, gate1 G_STA);
    STAMP(2);
    gemm_stage2(gm, xa, wbase, sr, kv0, at, kn, pa, pb, qa G_STA);
    idb = __builtin_shufflevector(gm.x1, gm.x1, 0, 1, 2, 3);
  }
  float kmax2 = wave_max32(__uint_as_float(kn));
  PIN(kmax2);
  STAMP(3);
  LDS_BAR();

  const int lq = lane_id();
  if (grp == 0) __builtin_amdgcn_s_setprio(2); else __builtin_amdgcn_s_setprio(1);
  const u32x4 q0 = lds_r128(lds, L_Q + wi * 2048 + lq * 16), q1 = lds_r128(lds, L_Q + wi * 2048 + 1024 + lq * 16);
  if (w >= 6) {
    __builtin_amdgcn_s_setprio(3);
    f32x16 padc;
#pragma unroll
    for (int i = 0; i < 16; ++i) padc[i] = (lq & 16) ? 1.f : 0.f;
#pragma unroll
    for (int j = 0; j < 2; ++j) {
      const unsigned vb = L_VB + (2 * pr + j) * 2048 + lq * 16;
      const u32x8 v0 = pack16(MFMA(lds_r128(lds, vb), idb, padc)), v1 = pack16(MFMA(lds_r128(lds, vb + 1024), idb, padc));
      const u32x4 v[4] = {lo4(v0), hi4(v0), lo4(v1), hi4(v1)};
      const u32x16 pt = j ? pb : pa;
      f32x16 kp = MFMA(__builtin_shufflevector(pt, pt, 0, 1, 2, 3), v[0], f32x16{});
      kp = MFMA(__builtin_shufflevector(pt, pt, 4, 5, 6, 7), v[1], kp);
      kp = MFMA(__builtin_shufflevector(pt, pt, 8, 9, 10, 11), v[2], kp);
      kp = MFMA(__builtin_shufflevector(pt, pt, 12, 13, 14, 15), v[3], kp);
      const unsigned pb = WS_KP + (unsigned)(((4 * g + 2 * pr + j) * 8 + (lq >> 4)) * 32 + tb) * 256 + (lq & 15) * 16;
      ws_store(rws, pb, pack8<0>(kp));
      __builtin_amdgcn_s_sleep(KP_GAP);
      ws_store(rws, pb + 32768, pack8<8>(kp));
      __builtin_amdgcn_s_sleep(KP_GAP);
    }
    __builtin_amdgcn_s_setprio(1);
    drain();
    if (lq == 0) set_flag(rws, WS_FKP + ((g * 2 + pr) * 32 + tb) * FL, ep);
  }
  float m0 = __builtin_amdgcn_sqrtf(half_add(sumsq8(q0)) * kmax2), m1 = __builtin_amdgcn_sqrtf(half_add(sumsq8(q1)) * kmax2);
  if (!__all(m0 <= SAFE_SHIFT && m1 <= SAFE_SHIFT)) {
    m0 = m1 = -3.0e38f;
#pragma clang loop unroll(disable)
    for (int t = 0; t < 64; ++t) {
      const u32x4 kf = ws_load(rws, kvb + t * 2048 + lq * 16);
      const f32x16 s0 = MFMA(kf, q0, f32x16{}), s1 = MFMA(kf, q1, f32x16{});
      m0 = fmax2(m0, fmax2(max8(s0, 0), max8(s0, 8))); m1 = fmax2(m1, fmax2(max8(s1, 0), max8(s1, 8)));
    }
    m0 = half_max(m0); m1 = half_max(m1);
  }
  AttnConst ak;
#pragma unroll
  for (int i = 0; i < 16; ++i) { ak.c0[i] = -m0; ak.c1[i] = -m1; }
  at.sa = MFMA((u32x4{at.kr[0], at.kr[1], at.kr[2], at.kr[3]}), q0, ak.c0);
  asm volatile("s_nop 11" : "+{v[128:143]}"(at.sa));
  ak.q = __builtin_shufflevector(q0, q1, 0, 1, 2, 3, 4, 5, 6, 7);
  const int otb = 4 * xc + (in & 3), og = in >> 2;
  ak.idx = 0xDDDDDDDDu; ak.koff = kv0.koff; ak.voff0 = kv0.voff0; ak.voff1 = kv0.voff1; ak.kv = rws; ak.entry = attn_define();
  ak.wo = __builtin_amdgcn_make_buffer_rsrc((void*)(grp ? woa : wob), 0, 512 * 1024, 0x00020000);
  ak.wov = (unsigned)(128 * wi + (lq >> 3)) * 1024 + og * 128 + (((lq & 7) ^ ((lq >> 2) & 4)) * 16);
  at.t = kv0.t2; at.wl = lds0 + L_WO + (2 * wi + grp) * 16384; at.wg = 0;
  const unsigned ob = (unsigned)((otb * 2 + grp) * 32 + wi * 8) * 1024;
  const unsigned lflag = L_VB + wi * 2048 + 512;
  const unsigned own = (unsigned)(tb * 64 + hd) * 1024;
  STAMP(4);
  attn_run(at, ak, grp ? 5 : 0);
  STAMP(5);
  if (grp == 0) {
    const int rh = tb & 3, sl = tb >> 2;
    const u32x4 mark = {SENT, SENT, SENT, SENT};
    u32x4 pv[8];
    ws_store(rws, WS_OA + own + lane_id() * 16, mark);
    attn_run(at, ak, 0);
    if (w == 0) {
      const int lane = lane_id();
      wait_flags(rws, WS_FKP + ((g * 2 + (rh >> 1)) * 32 + (lane & 31)) * FL, ep);
      const unsigned pb = WS_KP + (unsigned)((4 * g + rh) * 8 + sl) * 8192;
#pragma unroll
      for (int i = 0; i < 8; ++i) pv[i] = __builtin_amdgcn_raw_buffer_load_b128(rws, lane * 16, pb + i * 1024, SC1);
    }
    attn_run(at, ak, 0);
    STAMP(6);
    if (w == 0) {
      const int lane = lane_id();
      unsigned sel_lo = 0x00003F80u, sel_hi = 0x3F800000u;
      PIN(sel_lo); PIN(sel_hi);
      float s[8] = {};
#pragma unroll
      for (int i = 0; i < 8; ++i)
#pragma unroll
        for (int j = 0; j < 4; ++j) { s[2 * j] = dot2(pv[i][j], sel_lo, s[2 * j]); s[2 * j + 1] = dot2(pv[i][j], sel_hi, s[2 * j + 1]); }
      float t[4], u[2];
#pragma unroll
      for (int e = 0; e < 4; ++e) {
        const auto r = __builtin_amdgcn_permlane16_swap(__float_as_uint(s[e]), __float_as_uint(s[4 + e]), false, false);
        t[e] = __uint_as_float(r[0]) + __uint_as_float(r[1]);
      }
#pragma unroll
      for (int e = 0; e < 2; ++e) {
        const auto r = __builtin_amdgcn_permlane32_swap(__float_as_uint(t[e]), __float_as_uint(t[2 + e]), false, false);
        u[e] = __uint_as_float(r[0]) + __uint_as_float(r[1]);
      }
      __builtin_amdgcn_raw_buffer_store_b32(min(pk2(u[0], u[1]), SENT - 1), rws,
                                            WS_KR + ((4 * g + rh) * 8 + sl) * 256 + (lane & 15) * 16 + ((lane >> 4) & 1) * 8 + (lane >> 5) * 4, 0, SC1);
    }
    ws_store(rws, WS_OA + own + 32768 + lane_id() * 16, mark);
    attn_run(at, ak, 0);
    STAMP(7);
    u32x4 ka, kb;
    auto load_kv = [&]() {
      const int lane = lane_id();
      ka = __builtin_amdgcn_raw_buffer_load_b128(rws, lane * 16, WS_KR + hd * 2048, SC1);
      kb = __builtin_amdgcn_raw_buffer_load_b128(rws, lane * 16, WS_KR + hd * 2048 + 1024, SC1);
    };
    load_kv();
    attn_run(at, ak, 0);
    STAMP(8);
#pragma clang loop unroll(disable)
    for (unsigned spins = 0; spins < SPIN_CAP && __any(marked(ka) || marked(kb)); ++spins) {
      asm volatile("" ::: "memory");
      load_kv();
    }
    {
      const int lane = lane_id();
#pragma unroll
      for (int tt = 0; tt < 2; ++tt) {
        const unsigned pb = L_PQ + (wi * 2 + tt) * 2048 + lane * 16;
        const f32x16 nd = MFMA(kb, lds_r128(lds, pb + 1024), MFMA(ka, lds_r128(lds, pb), f32x16{}));
        const float sc = (1.f - (tt ? gate1 : gate0)) * __builtin_amdgcn_rcpf(nd[8] + 1e-6f);
        ws_store(rws, WS_OB + own + tt * 32768 + lane * 16, pack8s<0>(nd, sc));
      }
      asm volatile("" ::: "memory");
      if (lane == 0) *(LAS volatile u32x2*)(lds + lflag) = ep;
    }
    attn_run(at, ak, 0);
    if (lane_id() == 0) set_flag(rws, WS_FOB + (tb * 32 + hd) * FL, ep);
    __builtin_amdgcn_s_setprio(0);
  }
  STAMP(9);
  const unsigned fo = WS_FOB + (otb * 32 + wi * 8 + (lane_id() & 7)) * FL;
  const u32x2 fv = __builtin_amdgcn_raw_buffer_load_b64(rws, fo, 0, SC1);
  attn_run(at, ak, 0);
  STAMP(10);
  if (grp) {
#pragma clang loop unroll(disable)
    for (unsigned spins = 0; spins < SPIN_CAP; ++spins) {
      const u32x2 v = *(LAS volatile u32x2*)(lds + lflag);
      if (__all(v[0] == ep[0] && v[1] == ep[1])) break;
      __builtin_amdgcn_s_sleep(2);
    }
    asm volatile("" ::: "memory");
  }
  if (!__all(fv[0] == ep[0] && fv[1] == ep[1])) wait_flags(rws, fo, ep);
  u32x16 fb0, fb1;
  {
    const int lane = lane_id();
#pragma unroll
    for (int i = 0; i < 8; ++i) {
      const u32x4 v = __builtin_amdgcn_raw_buffer_load_b128(rws, lane * 16, WS_OB + ob + i * 1024, SC1);
#pragma unroll
      for (int j = 0; j < 4; ++j) { if (i < 4) fb0[4 * i + j] = v[j]; else fb1[4 * (i - 4) + j] = v[j]; }
    }
  }
  attn_run(at, ak, 0);
  STAMP(12);
  {
    const u32x4 vl = {at.vr1[12], at.vr1[13], at.vr1[14], at.vr1[15]};
    typedef __bf16 bf16x16 __attribute__((ext_vector_type(16)));
    const f32x8 ph = __builtin_shufflevector(at.sb, at.sb, 8, 9, 10, 11, 12, 13, 14, 15);
    at.o1 = __builtin_amdgcn_smfmac_f32_32x32x32_bf16(__builtin_bit_cast(bf16x8, vl), __builtin_bit_cast(bf16x16, ph), at.o1, (int)0xDDDDDDDDu, 0, 0);
  }

  const int ln = lane_id();
  {
    const f32x16 mine = grp ? at.o1 : at.o0, other = grp ? at.o0 : at.o1;
    const unsigned so = L_PQ + (wi * 2 + grp) * 2048 + ln * 16, sl = L_VB + (wi * 2 + grp) * 1024 + ln * 4;
    lds_w128(lds, so, __builtin_bit_cast(u32x4, f32x4{other[0], other[1], other[2], other[3]}));
    lds_w128(lds, so + 1024, __builtin_bit_cast(u32x4, f32x4{other[4], other[5], other[6], other[7]}));
    *(LAS float*)(lds + sl) = other[8];
    LDS_BAR();
    const unsigned ro = so ^ 2048, rl = sl ^ 1024;
    const f32x4 a = __builtin_bit_cast(f32x4, lds_r128(lds, ro)), b = __builtin_bit_cast(f32x4, lds_r128(lds, ro + 1024));
    const float l = mine[8] + *(LAS const float*)(lds + rl);
    f32x16 o = mine;
#pragma unroll
    for (int i = 0; i < 4; ++i) { o[i] += a[i]; o[4 + i] += b[i]; }
    ws_store(rws, WS_OA + own + grp * 32768 + ln * 16, unmarked(pack8s<0>(o, (grp ? gate1 : gate0) * __builtin_amdgcn_rcpf(l))));
    SBAR();
  }
  STAMP(13);
  f32x16 d0, d1;
  project(d0, d1, fb0, fb1,
          L_WO + wi * 32768 + (unsigned)(4 * (ln >> 5) + ((ln & 15) >> 2)) * 128 + ((((ln >> 4) & 1) ^ ((ln >> 2) & 2)) * 32) + (ln & 3) * 8,
          rws, ln * 16, ob);
  {
    const unsigned h = ln >> 5, tok = grp * 32 + (ln & 31), rb = L_RED + wi * 16384 + tok * 256;
#pragma unroll
    for (int q4 = 0; q4 < 4; ++q4) {
      lds_w128(lds, rb + (((2 * q4 + h) ^ (tok & 7)) * 16), __builtin_bit_cast(u32x4, f32x4{d0[4 * q4], d0[4 * q4 + 1], d0[4 * q4 + 2], d0[4 * q4 + 3]}));
      lds_w128(lds, rb + (((8 + 2 * q4 + h) ^ (tok & 7)) * 16), __builtin_bit_cast(u32x4, f32x4{d1[4 * q4], d1[4 * q4 + 1], d1[4 * q4 + 2], d1[4 * q4 + 3]}));
    }
  }
  STAMP(15);
  LDS_BAR();
  {
    const unsigned tok = w * 8 + (ln >> 3), c8 = ln & 7;
    const unsigned a0 = L_RED + tok * 256 + (((2 * c8) ^ (tok & 7)) * 16), a1 = a0 ^ 16;
    f32x4 lo = __builtin_bit_cast(f32x4, lds_r128(lds, a0)), hi = __builtin_bit_cast(f32x4, lds_r128(lds, a1));
#pragma unroll
    for (int k = 1; k < 4; ++k) {
      lo += __builtin_bit_cast(f32x4, lds_r128(lds, a0 + k * 16384));
      hi += __builtin_bit_cast(f32x4, lds_r128(lds, a1 + k * 16384));
    }
    *(u32x4*)(out + (size_t)(64 * otb + tok) * 1024 + og * 128 + c8 * 16) = u32x4{pk2(lo[0], lo[1]), pk2(lo[2], lo[3]), pk2(hi[0], hi[1]), pk2(hi[2], hi[3])};
  }
  if (blockIdx.x == 0 && w == 0 && ln == 0) set_flag(rws, WS_EPOCH, ep);
  asm volatile(".Lgm_code_to:");
  STAMP(16); STAMP_RT(63);
}

}

extern "C" void kernel_launch(void* const* d_in, const int* in_sizes, int n_in, void* d_out, int out_size, void* d_ws,
                              size_t ws_size, hipStream_t stream) {
  gated_mix_kernel<<<256, 512, 0, stream>>>((const char*)d_in[0], (const char*)d_in[1], (const char*)d_in[2], (const char*)d_in[4],
                                            (const char*)d_in[5], (const char*)d_in[6], (const char*)d_in[7], (char*)d_out, (char*)d_ws);
}
```

```cpp
#include <hip/hip_runtime.h>
#include <hip/hip_bf16.h>
#include <stdint.h>

namespace {

typedef __bf16 bf16x8 __attribute__((ext_vector_type(8)));
typedef __bf16 bf16x2 __attribute__((ext_vector_type(2)));
typedef float f32x16 __attribute__((ext_vector_type(16)));
typedef float f32x8 __attribute__((ext_vector_type(8)));
typedef float f32x4 __attribute__((ext_vector_type(4)));
typedef float f32x2 __attribute__((ext_vector_type(2)));
typedef unsigned u32x16 __attribute__((ext_vector_type(16)));
typedef unsigned u32x8 __attribute__((ext_vector_type(8)));
typedef unsigned u32x4 __attribute__((ext_vector_type(4)));
typedef unsigned u32x2 __attribute__((ext_vector_type(2)));
typedef short s16x4 __attribute__((ext_vector_type(4)));
typedef __amdgpu_buffer_rsrc_t rsrc_t;
#define LAS __attribute__((address_space(3)))
#define DI __device__ __forceinline__

constexpr unsigned WS_EPOCH = 0;
constexpr unsigned FL = 128;
constexpr unsigned WS_KR = 0x10000;
constexpr unsigned WS_ONES = 0x40000;
constexpr unsigned WS_FKV = 0x60000;
constexpr unsigned WS_FKP = 0x70000;
constexpr unsigned WS_FOB = 0x80000;
constexpr unsigned WS_KP = 0x100000;
constexpr unsigned WS_OB = 0x500000;
constexpr unsigned WS_OA = 0x700000;
constexpr unsigned WS_KV = 0x900000;
constexpr unsigned WS_END = 0xD00000;

constexpr unsigned L_X = 0;
constexpr unsigned L_RING = 65536;
constexpr unsigned L_WO = 0;
constexpr unsigned L_RED = 0;
constexpr unsigned L_PQ = 131072;
constexpr unsigned L_Q = 147456;
constexpr unsigned L_WPHI = L_Q;
constexpr unsigned L_GW = L_Q + 2048;
constexpr unsigned L_DUMMY = L_Q + 3072;
constexpr unsigned L_GP = L_Q + 4096;
constexpr unsigned L_VB = 155648;
constexpr unsigned L_BYTES = 163840;

constexpr int SC1 = 16;
constexpr unsigned OOB = 0xF0000000u;

DI void dma16(rsrc_t r, unsigned voff, unsigned soff, unsigned lds_dst) {
  asm volatile("s_mov_b32 m0, %0\n\ts_nop 0\n\tbuffer_load_dwordx4 %1, %2, %3 offen lds"
               :: "s"(lds_dst), "v"(voff), "s"(r), "s"(soff) : "memory");
}
#define SBAR() __builtin_amdgcn_sched_barrier(0)

#define EX(r) "v_exp_f32 v" #r ", v" #r "\n\t"
#define QK(s0, s1, k0, k1, q0, q1, c0, c1) "v_mfma_f32_32x32x16_bf16 v[" #s0 ":" #s1 "], v[" #k0 ":" #k1 "], v[" #q0 ":" #q1 "], v[" #c0 ":" #c1 "]\n\t"
#define PV(o0, o1, a0, a1, p0, p1) "v_smfmac_f32_32x32x32_bf16 v[" #o0 ":" #o1 "], v[" #a0 ":" #a1 "], v[" #p0 ":" #p1 "], v5\n\t"
#define LOADS(k0, k1, a0, a1, b0, b1)                                                     \
  "s_add_u32 s76, s76, 0x800\n\t"                                                         \
  "s_and_b32 s76, s76, 0xffff\n\t"                                                        \
  "buffer_load_dwordx4 v[" #k0 ":" #k1 "], v249, s[72:75], s76 offen sc1\n\t"             \
  "buffer_load_dwordx4 v[" #a0 ":" #a1 "], v250, s[72:75], s76 offen offset:1024 sc1\n\t" \
  "buffer_load_dwordx4 v[" #b0 ":" #b1 "], v251, s[72:75], s76 offen offset:1536 sc1\n\t"
#define WODMA                                                                             \
  "s_mov_b32 m0, s84\n\t"                                                                 \
  "s_add_u32 s84, s84, 0x400\n\t"                                                         \
  "buffer_load_dwordx4 v254, s[80:83], s85 offen lds\n\t"                                 \
  "s_add_u32 s85, s85, 0x2000\n\t"
#define ITER(ck0, ck1, ca0, ca1, cb0, cb1, pk0, pk1, pa0, pa1, pb0, pb1, nk0, nk1, EXTRA, WAIT) \
  EX(128) EX(129) PV(176, 191, pb0, pb1, 152, 159) EX(130) EX(131)                        \
  LOADS(pk0, pk1, pa0, pa1, pb0, pb1)                                                     \
  EX(132) EX(133) QK(144, 159, ck0, ck1, 196, 199, 112, 127) EX(134) EX(135) EXTRA EX(136) EX(137) EX(138) \
  PV(160, 175, ca0, ca1, 128, 135) EX(139) EX(140) EX(141) EX(142) EX(143)                 \
  EX(144) EX(145) PV(160, 175, cb0, cb1, 136, 143) EX(146) EX(147) EX(148) EX(149)         \
  WAIT                                                                                    \
  QK(128, 143, nk0, nk1, 192, 195, 96, 111) EX(150) EX(151) EX(152) EX(153) EX(154)      \
  PV(176, 191, ca0, ca1, 144, 151) EX(155) EX(156) EX(157) EX(158) EX(159)

#define ATTN_ROUTINE                                                                                  \
  "400:\n\t"                                                                                          \
  ITER(200, 203, 216, 219, 220, 223, 212, 215, 240, 243, 244, 247, 204, 207, WODMA, "")               \
  ITER(204, 207, 224, 227, 228, 231, 200, 203, 216, 219, 220, 223, 208, 211, "", "")                  \
  "s_cbranch_vccnz 402f\n\t401:\n\t"                                                                 \
  ITER(208, 211, 232, 235, 236, 239, 204, 207, 224, 227, 228, 231, 212, 215, WODMA, "s_waitcnt vmcnt(8)\n\t") \
  ITER(212, 215, 240, 243, 244, 247, 208, 211, 232, 235, 236, 239, 200, 203, "", "s_waitcnt vmcnt(7)\n\t")    \
  "s_sub_u32 s77, s77, 1\n\ts_waitcnt vmcnt(0)\n\ts_cbranch_scc0 400b\n\ts_setpc_b64 s[78:79]\n\t"  \
  "402:\n\ts_setprio 0\n\ts_branch 401b\n\t"
#define RUNV(N, V) "s_mov_b64 vcc, " V "\n\ts_mov_b32 s77, " N "\n\ts_swappc_b64 s[78:79], s[92:93]\n\t"
#define RUN(N) RUNV(N, "0")

#define STAMP(i) do { } while (0)
#define STAMP_RT(i) do { } while (0)
#define ST(i) ""
#define ST_END ""
#define ST_OP
#define ST_CL
#define ST_PAR
#define ST_ARG

#define G_BAR(N) "s_waitcnt vmcnt(" #N ") lgkmcnt(0)\n\ts_barrier\n\t"
#define G_LBAR "s_waitcnt lgkmcnt(0)\n\ts_barrier\n\t"
#define G_EPOCH "buffer_load_dwordx2 v[2:3], off, s[72:75], 0 sc1\n\t"
#define G_EPOCH_USE "v_readfirstlane_b32 s86, v2\n\tv_readfirstlane_b32 s87, v3\n\ts_add_u32 s86, s86, 1\n\ts_addc_u32 s87, s87, 0\n\t"
#define D_ROW(KC) "s_xor_b32 s52, s59, " KC "\n\ts_mul_i32 s52, s52, 196608\n\t"
#define D_A(R, COL, LOFF) "s_add_u32 m0, s57, " LOFF "\n\ts_nop 0\n\tbuffer_load_dwordx4 v[10+2*" #COL "], " R ", s52 offen lds\n\t"
#define D_B(R, COL) "buffer_load_dwordx4 v[11+2*" #COL "], " R ", s52 offen offset:1024 lds\n\t"
#define RA "s[40:43]"
#define RB "s[44:47]"
#define D_X(C)                                                                                        \
  "s_add_u32 m0, s58, 8192*" #C "\n\ts_xor_b32 s52, s60, 128*" #C "\n\t"                              \
  "buffer_load_dwordx4 v144, s[48:51], s52 offen lds\n\tbuffer_load_dwordx4 v145, s[48:51], s52 offen offset:1024 lds\n\t"
#define D_W(N, R0, C0, R1, C1) D_ROW(#N "&7") D_A(R0, C0, "16384*(" #N "&3)"), D_B(R0, C0), D_A(R1, C1, "16384*(" #N "&3)+8192"), D_B(R1, C1)
#define D_S0(N) D_W(N, RA, 1, RA, 2)
#define D_S1(N) D_W(N, RB, 0, RB, 1)
#define D_S2(N) D_W(N, RA, 0, RB, 2)
#define G_CAT(A, B, C, D) A B C D
#define G_APPLY(M, ...) M(__VA_ARGS__)
#define G_ALL(L) G_APPLY(G_CAT, L)

#define G_RW(W, C, S)                                                                                 \
  "ds_read_b64_tr_b16 v[" #W "+4*" #S ":" #W "+4*" #S "+1], v8 offset:16384*(" #C "&3)+2048*" #S "\n\t"       \
  "ds_read_b64_tr_b16 v[" #W "+4*" #S "+2:" #W "+4*" #S "+3], v8 offset:16384*(" #C "&3)+2048*" #S "+512\n\t"
#define G_RX(X, C, S, T) "ds_read_b128 v[" #X "+4*" #S ":" #X "+4*" #S "+3], v[4+" #S "] offset:8192*" #C "+4096*" #T "\n\t"
#define G_RS(W, X0, X1, C, S) G_RW(W, C, S) G_RX(X0, C, S, 0) G_RX(X1, C, S, 1)
#define G_READ(W, X0, X1, C) G_RS(W, X0, X1, C, 0) G_RS(W, X0, X1, C, 1) G_RS(W, X0, X1, C, 2) G_RS(W, X0, X1, C, 3)
#define G_M(ACC, W, X, S, CIN) \
  "v_mfma_f32_32x32x16_bf16 " ACC ", v[" #W "+4*" #S ":" #W "+4*" #S "+3], v[" #X "+4*" #S ":" #X "+4*" #S "+3], " CIN "\n\t"
#define G_A0 "v[16:31]"
#define G_A1 "v[32:47]"
#define G_MMA2(W, X0, X1, S) G_M(G_A0, W, X0, S, G_A0) G_M(G_A1, W, X1, S, G_A1)
#define G_STEP(RW, RX0, RX1, C, W, X0, X1, C0, C1, D0, D1, D2, D3)                                    \
  G_M(G_A0, W, X0, 0, C0) G_RW(RW, C, 0) G_RX(RX0, C, 0, 0) G_M(G_A1, W, X1, 0, C1) G_RX(RX1, C, 0, 1) G_RW(RW, C, 1) \
  G_M(G_A0, W, X0, 1, G_A0) G_RX(RX0, C, 1, 0) G_RX(RX1, C, 1, 1) D0 G_M(G_A1, W, X1, 1, G_A1) G_RW(RW, C, 2) G_RX(RX0, C, 2, 0) \
  G_M(G_A0, W, X0, 2, G_A0) G_RX(RX1, C, 2, 1) G_RW(RW, C, 3) D1 G_M(G_A1, W, X1, 2, G_A1) G_RX(RX0, C, 3, 0) G_RX(RX1, C, 3, 1) \
  G_M(G_A0, W, X0, 3, G_A0) D2 G_M(G_A1, W, X1, 3, G_A1) D3
#define G_EVEN(C, D0, D1, D2, D3) G_STEP(48, 64, 80, C, 96, 112, 128, G_A0, G_A1, D0, D1, D2, D3)
#define G_ODD(C, D0, D1, D2, D3) G_STEP(96, 112, 128, C, 48, 64, 80, G_A0, G_A1, D0, D1, D2, D3)
#define G_FIRST(D0, D1, D2, D3) G_STEP(96, 112, 128, 1, 48, 64, 80, "0", "0", D0, D1, D2, D3)
#define A_PACK                                                                                        \
  ".irp i,0,1,2,3\n\tv_cvt_pk_bf16_f32 v[48+\\i], v[16+2*\\i], v[17+2*\\i]\n\tv_cvt_pk_bf16_f32 v[56+\\i], v[24+2*\\i], v[25+2*\\i]\n\t.endr\n\t" \
  ".irp i,0,1,2,3\n\tv_cvt_pk_bf16_f32 v[52+\\i], v[32+2*\\i], v[33+2*\\i]\n\tv_cvt_pk_bf16_f32 v[60+\\i], v[40+2*\\i], v[41+2*\\i]\n\t.endr\n\t"
#define G_Z(L) ".irp r," L "\n\tv_mov_b64 v[\\r:\\r+1], 0\n\t.endr\n\t"
#define G_ZO G_Z("160,162,164,166,168,170,172,174,176,178,180,182,184,186,188,190,244,246")
#define G_QPRE "v_add_u32 v6, s66, v159\n\tv_mov_b32 v4, v159\n\tv_xor_b32 v7, 0x400, v6\n\tv_mov_b32 v5, 0xdddddddd\n\t"
#define G_QRD "ds_read_b128 v[192:195], v7\n\tds_read_b128 v[196:199], v6\n\t"
#define G_C(L) ".irp r," L "\n\tv_mov_b32 v\\r, 0xc2400000\n\t.endr\n\t"
#define G_C0 G_C("96,97,98,99,100,101,102,103,104,105,106,107,108,109,110,111")
#define G_C1 G_C("112,113,114,115,116,117,118,119,120,121,122,123,124,125,126,127")
#define G_ST4(R, V, O) "buffer_store_dwordx4 v[" #R ":" #R "+3], v" #V ", s[72:75], " O " sc1\n\t"
#define G_POLL(R) "buffer_load_dwordx3 v[" #R ":" #R "+2], v158, s[72:75], 0 offen sc1\n\t"
#define G_SPIN(L, AGAIN) "s_cbranch_vccz " #L "1f\n\t" AGAIN "s_sub_u32 s53, s53, 1\n\ts_cbranch_scc0 " #L "0b\n\ts_mov_b32 s53, 0\n\t" #L "1:\n\t"
#define G_POLLWAIT(L, R)                                                                              \
  #L "0:\n\ts_waitcnt vmcnt(0)\n\tv_cmp_ne_u64 vcc, s[86:87], v[" #R ":" #R "+1]\n\t"                  \
  G_SPIN(L, "s_sleep 4\n\t" G_POLL(R)) "s_waitcnt vmcnt(0)\n\tv_mov_b32 v9, v[" #R "+2]\n\t"
#define G_KV1(K, V, T)                                                                                \
  "buffer_load_dwordx4 v[" #K ":" #K "+3], v249, s[72:75], s" #T " offen sc1\n\t"                      \
  "buffer_load_dwordx4 v[" #V ":" #V "+3], v250, s[72:75], s" #T " offen offset:1024 sc1\n\t"          \
  "buffer_load_dwordx4 v[" #V "+4:" #V "+7], v251, s[72:75], s" #T " offen offset:1536 sc1\n\t"
#define G_KV G_KV1(200, 216, 62) G_KV1(204, 224, 63) G_KV1(208, 232, 64)
#define DPPMAX(R, CTRL) "v_max_f32_dpp v" #R ", v" #R ", v" #R " " CTRL " row_mask:0xf bank_mask:0xf\n\t"
#define SIGMOID(R, OUT) \
  "v_mul_f32 v" #R ", 0xbfb8aa3b, v" #R "\n\tv_exp_f32 v" #R ", v" #R "\n\ts_nop 0\n\tv_add_f32 v" #R ", 1.0, v" #R "\n\tv_rcp_f32 v" #OUT ", v" #R "\n\t"
#define A_GREAD(C)                                                                                    \
  "v_xor_b32 v92, 128*" #C ", v153\n\tv_xor_b32 v93, 128*(" #C "+1), v153\n\t"                     \
  "ds_read_b128 v[72:75], v152 offset:8192*" #C "\n\tds_read_b128 v[80:83], v152 offset:8192*" #C "+4096\n\t" \
  "ds_read_b128 v[76:79], v152 offset:8192*(" #C "+1)\n\tds_read_b128 v[84:87], v152 offset:8192*(" #C "+1)+4096\n\t" \
  "ds_read_b128 v[64:67], v92\n\tds_read_b128 v[68:71], v93\n\t"
#define A_GDOT                                                                                        \
  ".irp j,0,1,2,3\n\t"                                                                                \
  "v_dot2c_f32_bf16 v88, v[72+\\j], v[64+\\j]\n\tv_dot2c_f32_bf16 v89, v[80+\\j], v[64+\\j]\n\t"       \
  "v_dot2c_f32_bf16 v90, v[76+\\j], v[68+\\j]\n\tv_dot2c_f32_bf16 v91, v[84+\\j], v[68+\\j]\n\t"       \
  ".endr\n\t"
#define A_DPPMAX(CTRL) "s_nop 1\n\tv_max_f32_dpp v96, v96, v96 " CTRL " row_mask:0xf bank_mask:0xf\n\t"
#define F_JOBS(M) M(16, 80, 48) M(32, 84, 56) M(96, 88, 64) M(112, 92, 72)
#define F_MAX1(E, T, P)                                                                               \
  "v_max3_f32 v[" #T "], v[" #E "], v[" #E "+1], v[" #E "+2]\n\t"                                      \
  "v_max3_f32 v[" #T "+2], v[" #E "+8], v[" #E "+9], v[" #E "+10]\n\t"                                 \
  "v_max3_f32 v[" #T "+1], v[" #E "+3], v[" #E "+4], v[" #E "+5]\n\t"                                  \
  "v_max3_f32 v[" #T "+3], v[" #E "+11], v[" #E "+12], v[" #E "+13]\n\t"                               \
  "v_max3_f32 v[" #T "], v[" #T "], v[" #E "+6], v[" #E "+7]\n\t"                                      \
  "v_max3_f32 v[" #T "+2], v[" #T "+2], v[" #E "+14], v[" #E "+15]\n\t"                                \
  "v_max_f32 v[" #T "], v[" #T "], v[" #T "+1]\n\t"                                                    \
  "v_max_f32 v[" #T "+2], v[" #T "+2], v[" #T "+3]\n\t"
#define F_SWAP(E, T, P) "v_permlane32_swap_b32 v[" #T "], v[" #T "+2]\n\t"
#define F_MAX2(E, T, P) "v_max_f32 v[" #T "], v[" #T "], v[" #T "+2]\n\t"
#define F_NEG(E, T, P) "v_mul_f32 v[" #T "], 0xbfb8aa3b, v[" #T "]\n\t"
#define F_DUP(E, T, P) "v_mov_b32 v[" #T "+2], v[" #T "]\n\t"
#define F_FMA(E, T)                                                                                   \
  ".irp i,0,1,2,3,4,5,6,7\n\t"                                                                        \
  "v_fma_f32 v[" #E "+\\i], v[" #E "+\\i], s71, v[" #T "]\n\t"                                      \
  "v_fma_f32 v[" #E "+8+\\i], v[" #E "+8+\\i], s71, v[" #T "+2]\n\t"                                \
  ".endr\n\t"
#define F_EX(E, L) ".irp i," L "\n\tv_exp_f32 v[" #E "+\\i], v[" #E "+\\i]\n\t.endr\n\t"
#define F_EXP(E, T) F_FMA(E, T) F_EX(E, "0,8,1,9,2,10,3,11,4,12,5,13,6,14,7,15")
#define F_SUM1(E, T, P) ".irp i,0,1,2,3,4,5,6,7\n\tv_add_f32 v[" #P "+\\i], v[" #E "+2*\\i], v[" #E "+2*\\i+1]\n\t.endr\n\t"
#define F_SUM2(E, T, P) ".irp i,0,2,4,6\n\tv_add_f32 v[" #P "+\\i], v[" #P "+\\i], v[" #P "+\\i+1]\n\t.endr\n\t"
#define F_SUM3(E, T, P) "v_add_f32 v[" #T "], v[" #P "], v[" #P "+2]\n\tv_add_f32 v[" #T "+2], v[" #P "+4], v[" #P "+6]\n\t"
#define F_ADD(E, T, P) "v_add_f32 v[" #T "], v[" #T "], v[" #T "+2]\n\t"
#define F_RCP(E, T, P) "v_rcp_f32 v[" #T "], v[" #T "]\n\t"
#define F_PACK(E, T, P) ".irp i,0,1,2,3,4,5,6,7\n\tv_cvt_pk_bf16_f32 v[" #P "+\\i], v[" #E "+2*\\i], v[" #E "+2*\\i+1]\n\t.endr\n\t"
#define F_SCALE(E, T, P)                                                                              \
  ".irp i,0,1,2,3,4,5,6,7\n\t"                                                                        \
  "v_mul_f32 v[" #E "+\\i], v[" #E "+\\i], v[" #T "]\n\t"                                              \
  "v_mul_f32 v[" #E "+8+\\i], v[" #E "+8+\\i], v[" #T "+2]\n\t"                                        \
  ".endr\n\t" F_PACK(E, T, P)
#define F_TR0(E, T, P) "v_mfma_f32_32x32x16_bf16 v[" #E ":" #E "+15], v[" #P ":" #P "+3], v[128:131], 0\n\t"
#define F_TR1(E, T, P) "v_mfma_f32_32x32x16_bf16 v[" #E ":" #E "+15], v[" #P "+4:" #P "+7], v[132:135], v[" #E ":" #E "+15]\n\t"
#define P_TEST(F, I)                                                                                  \
  "v_max3_u32 v80, v[" #F "+4*" #I "], v[" #F "+1+4*" #I "], v[" #F "+2+4*" #I "]\n\t"                 \
  "v_max_u32 v80, v80, v[" #F "+3+4*" #I "]\n\t"                                                       \
  "v_cmp_eq_u32 vcc, -1, v80\n\t"
#define P_LOAD(F, I, SO) "buffer_load_dwordx4 v[" #F "+4*" #I ":" #F "+3+4*" #I "], v4, s[72:75], s" #SO " offen offset:1024*(" #I "&3) sc1\n\t"
#define P_RD(R, I, OFF)                                                                               \
  "ds_read_b64_tr_b16 v[" R ":" R "+1], v0 offset:" #OFF "+2048*" #I "\n\t"                         \
  "ds_read_b64_tr_b16 v[" R "+2:" R "+3], v0 offset:" #OFF "+2048*" #I "+1024\n\t"                  \
  "ds_read_b64_tr_b16 v[" R "+4:" R "+5], v1 offset:" #OFF "+2048*" #I "\n\t"                       \
  "ds_read_b64_tr_b16 v[" R "+6:" R "+7], v1 offset:" #OFF "+2048*" #I "+1024\n\t"
#define P_RB(I) P_RD("192+8*(" #I "&3)", I, 0)
#define P_RA(I) P_RD("128+8*" #I, I, 16384)
#define P_MM(W, F, I, C0, C1)                                                                         \
  "v_mfma_f32_32x32x16_bf16 v[16:31], v[" W ":" W "+3], v[" #F "+4*" #I ":" #F "+3+4*" #I "], " C0 "\n\t"   \
  "v_mfma_f32_32x32x16_bf16 v[32:47], v[" W "+4:" W "+7], v[" #F "+4*" #I ":" #F "+3+4*" #I "], " C1 "\n\t"
#define P_USEB(I, N, C0, C1, MORE)                                                                    \
  "s_waitcnt lgkmcnt(" #N ")\n\t" P_MM("192+8*(" #I "&3)", 48, I, C0, C1) MORE
#define P_USEA(I, N) "s_waitcnt vmcnt(" #N ")\n\t" P_TEST(96, I) "s_cbranch_vccnz 57" #I "f\n\t56" #I ":\n\t" P_MM("128+8*" #I, 96, I, G_A0, G_A1)
#define P_STUB(A, B, I, R) #A #I ":\n\ts_call_b64 s[78:79], " #R "f\n\ts_branch " #B #I "b\n\t"
#define P_STUBS(A, B, R) P_STUB(A, B, 0, R) P_STUB(A, B, 1, R) P_STUB(A, B, 2, R) P_STUB(A, B, 3, R) P_STUB(A, B, 4, R) P_STUB(A, B, 5, R) P_STUB(A, B, 6, R) P_STUB(A, B, 7, R)
#define P_RETRY(F, I, SO) P_TEST(F, I) "s_cbranch_vccz 1f\n\ts_mov_b32 s52, 1\n\t" P_LOAD(F, I, SO) "1:\n\t"
#define P_SLOW(R, F, S0, S1)                                                                          \
  #R ":\n\ts_waitcnt vmcnt(0)\n\ts_mov_b32 s52, 0\n\t"                                             \
  P_RETRY(F, 0, S0) P_RETRY(F, 1, S0) P_RETRY(F, 2, S0) P_RETRY(F, 3, S0) P_RETRY(F, 4, S1) P_RETRY(F, 5, S1) P_RETRY(F, 6, S1) P_RETRY(F, 7, S1) \
  "s_sub_u32 s53, s53, 1\n\ts_cselect_b32 s52, 0, s52\n\ts_cselect_b32 s53, 0, s53\n\t" \
  "s_cmp_lg_u32 s52, 0\n\ts_cbranch_scc1 " #R "b\n\ts_waitcnt vmcnt(0)\n\ts_setpc_b64 s[78:79]\n\t"
#define P_LOAD8(F, S0, S1) P_LOAD(F, 0, S0) P_LOAD(F, 1, S0) P_LOAD(F, 2, S0) P_LOAD(F, 3, S0) P_LOAD(F, 4, S1) P_LOAD(F, 5, S1) P_LOAD(F, 6, S1) P_LOAD(F, 7, S1)
#define P_LOADA P_LOAD8(96, 54, 55)
#define OUT_POL " sc1"
#define HALF_SWITCH 0
#define A_AT 7
#define P_AT(I) ".if " STR(A_AT) " == " #I "\n\t" P_LOADA ".endif\n\t"
#define STR_(x) #x
#define STR(x) STR_(x)
typedef unsigned u32x6 __attribute__((ext_vector_type(6)));
struct Src { u32x6 wv; rsrc_t ra, rb, rx; };
#define M_PAR0                                                                                        \
  "s_add_u32 s34, s26, 0x500000\n\ts_lshl_b32 s33, s56, 15\n\ts_sub_u32 s33, s26, s33\n\ts_lshr_b32 s33, s33, 3\n\ts_add_u32 s33, s33, 0x80000\n\t" \
  "s_lshl_b32 s29, s25, 13\n\ts_add_u32 s29, s29, 0x100000\n\ts_lshl_b32 s30, s25, 8\n\ts_add_u32 s30, s30, 0x10000\n\t" \
  "s_lshr_b32 s28, s25, 4\n\ts_lshl_b32 s28, s28, 12\n\ts_add_u32 s28, s28, 0x70000\n\t"
#define M_PAR1                                                                                        \
  "s_lshl_b32 s31, s24, 1\n\ts_add_u32 s31, s31, 0x10000\n\ts_lshl_b32 s39, s24, 7\n\ts_add_u32 s39, s39, 0x900000\n\t" \
  "s_lshl_b32 s32, s62, 5\n\ts_add_u32 s32, s32, s24\n\ts_add_u32 s24, s32, 0x700000\n\ts_add_u32 s25, s32, 0x708000\n\t" \
  "s_add_u32 s26, s32, 0x500000\n\ts_add_u32 s27, s32, 0x508000\n\ts_lshr_b32 s32, s32, 3\n\ts_add_u32 s32, s32, 0x80000\n\t"
#define M_PAR2                                                                                        \
  "s_lshl_b32 s38, s56, 15\n\ts_add_u32 s38, s38, s24\n\ts_lshl_b32 s35, s58, 1\n\ts_add_u32 s35, s35, 0x20000\n\t" \
  "s_add_u32 s36, s58, 0x26200\n\ts_lshl_b32 s88, s56, 11\n\ts_add_u32 s88, s88, s35\n\ts_lshl_b32 s89, s56, 10\n\t" \
  "s_add_u32 s89, s89, s58\n\ts_add_u32 s89, s89, 0x26000\n\ts_lshl_b32 s90, s37, 11\n\t"              \
  "s_mov_b32 s76, s64\n\ts_lshl_b32 s84, s58, 4\n\ts_lshl_b32 s85, s56, 14\n\ts_add_u32 s84, s84, s85\n\ts_mov_b32 s85, 0\n\t"
static_assert(WS_OB == 0x500000 && WS_OA == 0x700000 && WS_FOB == 0x80000 && WS_KP == 0x100000 && WS_KR == 0x10000 && WS_FKP == 0x70000 &&
              WS_KV == 0x900000 && L_PQ == 0x20000 && L_VB == 0x26000 && L_X == 0 && L_WO == 0 && L_RED == 0 && FL == 128, "literals above");
DI void first_chunks(const Src& sr, unsigned xv0, unsigned xv1, unsigned ml, unsigned mx, unsigned rot, unsigned rotx) {
  asm volatile(D_X(0) G_ALL(D_S0(0)) D_X(1) G_ALL(D_S0(1))
               :: "{v[10:15]}"(sr.wv), "{v144}"(xv0), "{v145}"(xv1), "{s[40:43]}"(sr.ra), "{s[48:51]}"(sr.rx), "{s57}"(ml), "{s58}"(mx),
                  "{s59}"(rot), "{s60}"(rotx)
               : "memory", "scc", "s52");
}
struct Prog { unsigned wo0, red, koff, voff0, voff1, wov; rsrc_t out, ws, wo; };
DI void program(Src sr, u32x4 xa, unsigned wa, u32x16 vp, u32x16 sp, u32x4 base, unsigned w, unsigned outoff, const Prog& pc ST_PAR) {
  asm volatile(
      "s_getpc_b64 s[92:93]\n\t"
      "99:\n\t"
      "s_add_u32 s92, s92, 400f-99b\n\ts_addc_u32 s93, s93, 0\n\t"
      "s_mov_b32 s53, 0x40000\n\t"
      "s_cmp_eq_u32 s56, 0\n\t"
      "s_cbranch_scc1 100f\n\t"
      G_BAR(6) D_X(2) G_ALL(D_S0(2)) G_BAR(6) D_X(3) G_ALL(D_S0(3)) G_BAR(6) D_X(4) G_ALL(D_S0(4)) M_PAR0
      G_BAR(6) D_X(5) G_ALL(D_S0(5)) M_PAR1 G_BAR(6) D_X(6) G_ALL(D_S0(6)) M_PAR2 G_BAR(6) D_X(7) G_ALL(D_S0(7))
      G_BAR(6) G_ALL(D_S1(8)) G_ALL(D_S1(9)) G_BAR(8) G_ALL(D_S1(10))
      "s_branch 200f\n\t"
      "100:\n\t"
      G_EPOCH
      G_Z("16,18,20,22,24,26,28,30,32,34,36,38,40,42,44,46")
      "s_mov_b32 s52, 0\n\t"
      G_LBAR G_READ(48, 64, 80, 0)
      "s_branch 102f\n\t"
      "101:\n\t"
      G_BAR(0) G_EVEN(0, "", "", "", "")
      "102:\n\t"
      G_LBAR G_ODD(1, "", "", "", "") G_LBAR G_EVEN(2, "", "", "", "") G_LBAR G_ODD(3, "", "", "", "")
      ".irp r,4,5,6,7\n\tv_xor_b32 v\\r, 0x8000, v\\r\n\t.endr\n\t"
      "s_xor_b32 s52, s52, 1\n\t"
      "s_cbranch_scc1 101b\n\t"
      ST(1)
      G_LBAR
      G_MMA2(96, 112, 128, 0) G_MMA2(96, 112, 128, 1) G_MMA2(96, 112, 128, 2) G_MMA2(96, 112, 128, 3)
      G_EPOCH_USE G_Z("88,90,92,94")
      "s_cmp_lg_u32 s61, 0\n\t"
      "s_cbranch_scc1 110f\n\t"
      "s_nop 0\n\t"
      ".irp r,16,17,18,19,20,21,22,23,24,25,26,27,28,29,30,31\n\tv_mul_f32 v\\r, 0x3eb8aa3b, v\\r\n\t.endr\n\t"
      G_LBAR
      ".irp r,32,33,34,35,36,37,38,39,40,41,42,43,44,45,46,47\n\tv_mul_f32 v\\r, 0x3eb8aa3b, v\\r\n\t.endr\n\t"
      A_PACK
      G_ST4(48, 149, "0 offen") G_ST4(52, 149, "0 offen offset:2048")
      ".irp j,0,1,2,3\n\t"
      "v_dot2c_f32_bf16 v92, v[48+\\j], v[48+\\j]\n\tv_dot2c_f32_bf16 v93, v[52+\\j], v[52+\\j]\n\t"
      "v_dot2c_f32_bf16 v94, v[56+\\j], v[56+\\j]\n\tv_dot2c_f32_bf16 v95, v[60+\\j], v[60+\\j]\n\t"
      ".endr\n\t"
      "s_mov_b32 s54, 131072\n\t"
      G_LBAR
      G_ST4(56, 149, "s54 offen") G_ST4(60, 149, "s54 offen offset:2048")
      "v_permlane32_swap_b32 v92, v94\n\tv_permlane32_swap_b32 v93, v95\n\t"
      "v_add_f32 v96, v92, v94\n\tv_add_f32 v97, v93, v95\n\t"
      A_GREAD(0)
      "v_max_f32 v96, v96, v97\n\t"
      A_DPPMAX("quad_perm:[1,0,3,2]") A_DPPMAX("quad_perm:[2,3,0,1]") A_DPPMAX("row_half_mirror") A_DPPMAX("row_mirror")
      "v_mov_b32 v97, v96\n\tv_mov_b32 v100, -1\n\ts_nop 0\n\tv_permlane16_swap_b32 v96, v97\n\tv_max_f32 v96, v96, v97\n\t"
      "buffer_store_dword v96, v150, s[72:75], 0 offen sc1\n\t"
      "buffer_store_dword v100, v151, s[72:75], 0 offen sc1\n\t"
      "s_branch 111f\n\t"
      "110:\n\t"
      ".irp j,0,1,2,3,4,5,6,7\n\tv_bfe_i32 v[96+\\j], v157, \\j, 1\n\t.endr\n\t"
      A_PACK
      ".irp j,0,1,2,3,4,5,6,7\n\tv_and_b32 v[96+\\j], v156, v[96+\\j]\n\t.endr\n\t"
      G_LBAR
      "s_setprio 2\n\t"
      "v_mfma_f32_32x32x16_bf16 v[16:31], v[48:51], v[96:99], 0\n\t"
      "v_mfma_f32_32x32x16_bf16 v[32:47], v[52:55], v[96:99], 0\n\t"
      "v_mfma_f32_32x32x16_bf16 v[16:31], v[56:59], v[100:103], v[16:31]\n\t"
      "v_mfma_f32_32x32x16_bf16 v[32:47], v[60:63], v[100:103], v[32:47]\n\t"
      "s_setprio 0\n\t"
      ".irp r,104,105,106,107\n\tv_mov_b32 v\\r, 0x3f803f80\n\t.endr\n\t"
      G_ST4(104, 150, "0 offen")
      "s_nop 4\n\t"
      F_PACK(16, 0, 48)
      G_ST4(48, 149, "0 offen") G_ST4(52, 149, "0 offen offset:512")
      G_LBAR
      F_PACK(32, 0, 56)
      G_ST4(56, 149, "0 offen offset:2048") G_ST4(60, 149, "0 offen offset:2560")
      A_GREAD(0)
      "111:\n\t"
      G_LBAR A_GDOT A_GREAD(2) M_PAR0
      G_LBAR A_GDOT A_GREAD(4) M_PAR1
      G_LBAR A_GDOT A_GREAD(6) M_PAR2
      G_LBAR A_GDOT
      "v_mov_b32 v94, s86\n\tv_mov_b32 v95, s87\n\tv_and_b32 v92, 0xfffff0ff, v154\n\t"
      "v_add_f32 v88, v88, v90\n\tv_add_f32 v89, v89, v91\n\t"
      "ds_write2st64_b32 v154, v88, v89 offset1:1\n\t"
      G_BAR(0)
      "ds_read2st64_b32 v[64:65], v92 offset1:1\n\tds_read2st64_b32 v[66:67], v92 offset0:2 offset1:3\n\t"
      "ds_read2st64_b32 v[68:69], v92 offset0:4 offset1:5\n\tds_read2st64_b32 v[70:71], v92 offset0:6 offset1:7\n\t"
      "buffer_store_dwordx2 v[94:95], v155, s[72:75], 0 offen sc1\n\t"
      "s_waitcnt lgkmcnt(0)\n\t"
      "v_add_f32 v64, v64, v66\n\tv_add_f32 v65, v65, v67\n\tv_add_f32 v68, v68, v70\n\tv_add_f32 v69, v69, v71\n\t"
      "v_add_f32 v64, v64, v68\n\tv_add_f32 v65, v65, v69\n\t"
      "s_nop 1\n\tv_permlane32_swap_b32 v64, v65\n\t"
      "v_add_f32 v64, v64, v65\n\t" SIGMOID(64, 253)
      "s_nop 0\n\tv_mov_b32 v252, v253\n\t"
      ST(2)
      G_LBAR "s_setprio 1\n\t" "v_permlane32_swap_b32 v253, v252\n\t" G_READ(48, 64, 80, 0) G_ALL(D_S2(19))
      G_LBAR G_APPLY(G_FIRST, D_S2(20))
      G_LBAR G_APPLY(G_EVEN, 2, D_S2(21)) G_BAR(8) G_APPLY(G_ODD, 3, D_S2(22))
      G_BAR(8) G_APPLY(G_EVEN, 4, D_S2(23))
      G_BAR(8) G_ODD(5, "buffer_load_dword v9, v158, s[72:75], 0 offen offset:8 sc1\n\t", G_KV1(200, 216, 62), G_KV1(204, 224, 63), G_KV1(208, 232, 64))
      G_BAR(14) G_EVEN(6, G_Z("160,162,164,166,168"), G_Z("170,172,174,176,178"), G_Z("180,182,184,186"), G_Z("188,190,244,246"))
      G_BAR(9) G_ODD(7, DPPMAX(9, "quad_perm:[1,0,3,2]"), DPPMAX(9, "quad_perm:[2,3,0,1]"), DPPMAX(9, "row_half_mirror"), "s_nop 0\n\t" DPPMAX(9, "row_mirror"))
      "s_waitcnt lgkmcnt(0)\n\t"
      G_MMA2(96, 112, 128, 0) G_MMA2(96, 112, 128, 1) G_MMA2(96, 112, 128, 2) G_MMA2(96, 112, 128, 3)
      "v_add_u32 v1, s65, v159\n\t"
      "s_setprio 2\n\t"
      G_QPRE G_Z("152,154,156,158") "v_mov_b32 v8, v9\n\t"
      A_PACK
      ".irp i,0,1,2,3\n\tds_write_b128 v1, v[48+4*\\i:51+4*\\i] offset:1024*\\i\n\t.endr\n\t"
      G_C0 G_C1
      ST(3)
      G_LBAR
      G_QRD
      "s_branch 300f\n\t"
      ATTN_ROUTINE
      "200:\n\t"
      ST(1)
      G_BAR(8) "s_setprio 1\n\t" G_EPOCH G_READ(48, 64, 80, 0) G_ALL(D_S1(11))
      G_BAR(9) G_APPLY(G_FIRST, D_S1(12))
      G_BAR(9) G_APPLY(G_EVEN, 2, D_S1(13)) G_BAR(8) G_APPLY(G_ODD, 3, D_S1(14))
      G_BAR(8) G_APPLY(G_EVEN, 4, D_S1(15)) G_BAR(8) G_APPLY(G_ODD, 5, D_S2(16))
      G_BAR(8) G_APPLY(G_EVEN, 6, D_S2(17)) G_BAR(8) G_APPLY(G_ODD, 7, D_S2(18))
      ST(2)
      G_BAR(8)
      "s_setprio 0\n\t"
      "ds_read_b64_tr_b16 v[64:65], v146\n\tds_read_b64_tr_b16 v[66:67], v146 offset:256\n\t"
      "ds_read_b64_tr_b16 v[68:69], v146 offset:512\n\tds_read_b64_tr_b16 v[70:71], v146 offset:768\n\t"
      "ds_read2st64_b32 v[72:73], v154 offset1:2\n\tds_read2st64_b32 v[74:75], v154 offset0:4 offset1:6\n\t"
      G_MMA2(96, 112, 128, 0) G_EPOCH_USE G_MMA2(96, 112, 128, 1)
      "s_waitcnt lgkmcnt(0)\n\t"
      "v_add_f32 v72, v72, v74\n\tv_add_f32 v73, v73, v75\n\t"
      ".irp r,64,65,66,67\n\tv_xor_b32 v\\r, v147, v\\r\n\t.endr\n\t"
      "v_add_f32 v72, v72, v73\n\t"
      G_MMA2(96, 112, 128, 2)
      "v_mov_b32 v73, v72\n\t"
      ".irp r,68,69,70,71\n\tv_xor_b32 v\\r, v147, v\\r\n\t.endr\n\t"
      "v_permlane32_swap_b32 v72, v73\n\t"
      G_MMA2(96, 112, 128, 3)
      "v_add_f32 v72, v72, v73\n\t" SIGMOID(72, 253)
      "s_nop 5\n\t"
      A_PACK
      G_BAR(4)
      "s_setprio 2\n\t"
      "v_mfma_f32_32x32x16_bf16 v[16:31], v[64:67], v[48:51], 0\n\t"
      "v_mfma_f32_32x32x16_bf16 v[32:47], v[64:67], v[52:55], 0\n\t"
      ".irp j,0,1,2,3\n\tv_bfe_i32 v[128+\\j], v157, \\j, 1\n\t.endr\n\t"
      "v_mfma_f32_32x32x16_bf16 v[96:111], v[68:71], v[56:59], 0\n\t"
      ".irp j,4,5,6,7\n\tv_bfe_i32 v[128+\\j], v157, \\j, 1\n\t.endr\n\t"
      "v_mfma_f32_32x32x16_bf16 v[112:127], v[68:71], v[60:63], 0\n\t"
      "s_setprio 0\n\t"
      ".irp j,0,1,2,3,4,5,6,7\n\tv_and_b32 v[128+\\j], v156, v[128+\\j]\n\t.endr\n\t"
      F_JOBS(F_MAX1) "s_nop 0\n\t" F_JOBS(F_SWAP) F_JOBS(F_MAX2) F_JOBS(F_NEG) F_JOBS(F_DUP) F_JOBS(F_SWAP)
      G_BAR(0)
      F_EXP(16, 80) F_FMA(32, 84) F_EX(32, "0,8,1,9")
      G_LBAR
      G_POLL(140) F_EX(32, "2,10,3,11,4,12,5,13,6,14,7,15") F_EXP(96, 88)
      G_LBAR
      F_EXP(112, 92) F_SUM1(16, 80, 48) F_SUM1(32, 84, 56) G_POLLWAIT(22, 140)
      G_LBAR
      F_SUM1(96, 88, 64) DPPMAX(9, "quad_perm:[1,0,3,2]") F_SUM1(112, 92, 72) DPPMAX(9, "quad_perm:[2,3,0,1]") F_JOBS(F_SUM2)
      DPPMAX(9, "row_half_mirror") F_JOBS(F_SUM3) DPPMAX(9, "row_mirror") F_JOBS(F_SWAP) F_JOBS(F_ADD) F_JOBS(F_RCP) F_JOBS(F_DUP) F_JOBS(F_SWAP)
      G_LBAR
      G_KV1(200, 216, 62) F_SCALE(16, 80, 48) G_KV1(204, 224, 63) F_SCALE(32, 84, 56) G_KV1(208, 232, 64) F_SCALE(96, 88, 64)
      G_LBAR
      F_SCALE(112, 92, 72)
      "s_cmp_lg_u32 s61, 0\n\t"
      "s_cbranch_scc1 230f\n\t"
      "v_add_u32 v136, s67, v159\n\t"
      ".irp i,0,1,2,3,4,5,6,7\n\tds_write_b128 v136, v[48+4*\\i:51+4*\\i] offset:1024*\\i\n\t.endr\n\t"
      G_ZO G_C0
      "s_branch 231f\n\t"
      "230:\n\t"
      F_JOBS(F_TR0) F_JOBS(F_TR1) G_ZO F_JOBS(F_PACK)
      ".irp r,96,97,98,99,100,101,102,103,104,105,106,107,108,109,110,111\n\tv_mov_b32 v\\r, v152\n\t.endr\n\t"
      "v_add_u32 v136, s69, v159\n\tv_mov_b32 v2, s86\n\tv_mov_b32 v3, s87\n\t"
      "231:\n\t"
      G_QPRE G_Z("152,154,156,158") "v_mov_b32 v8, v9\n\t" G_C1
      ST(3)
      G_LBAR
      G_QRD
      "s_cmp_eq_u32 s61, 0\n\t"
      "s_cbranch_scc1 151f\n\t"
      "s_setprio 3\n\t"
      ".irp i,0,1,2,3\n\tds_read_b128 v[80+4*\\i:83+4*\\i], v136 offset:1024*\\i\n\t.endr\n\t"
      "s_waitcnt lgkmcnt(2)\n\t"
      "v_mfma_f32_32x32x16_bf16 v[16:31], v[80:83], v[128:131], v[96:111]\n\t"
      "v_mfma_f32_32x32x16_bf16 v[32:47], v[84:87], v[128:131], v[96:111]\n\t"
      "s_waitcnt lgkmcnt(0)\n\t"
      "v_mfma_f32_32x32x16_bf16 v[112:127], v[88:91], v[128:131], v[96:111]\n\t"
      "v_mfma_f32_32x32x16_bf16 v[96:111], v[92:95], v[128:131], v[96:111]\n\t"
      "s_mov_b32 s52, 32768\n\ts_mov_b32 s54, 65536\n\ts_mov_b32 s55, 98304\n\t"
      "s_nop 5\n\t"
      F_PACK(16, 0, 80) F_PACK(32, 0, 88)
      "v_mfma_f32_32x32x16_bf16 v[16:31], v[48:51], v[80:83], 0\n\t"
      F_PACK(112, 0, 128)
      "v_mfma_f32_32x32x16_bf16 v[16:31], v[52:55], v[84:87], v[16:31]\n\t"
      F_PACK(96, 0, 136)
      "v_mfma_f32_32x32x16_bf16 v[16:31], v[56:59], v[88:91], v[16:31]\n\t"
      "v_mfma_f32_32x32x16_bf16 v[32:47], v[64:67], v[128:131], 0\n\t"
      "v_mfma_f32_32x32x16_bf16 v[16:31], v[60:63], v[92:95], v[16:31]\n\t"
      "v_mfma_f32_32x32x16_bf16 v[32:47], v[68:71], v[132:135], v[32:47]\n\t"
      "v_mfma_f32_32x32x16_bf16 v[32:47], v[72:75], v[136:139], v[32:47]\n\t"
      "v_mfma_f32_32x32x16_bf16 v[32:47], v[76:79], v[140:143], v[32:47]\n\t"
      "s_nop 8\n\t"
      F_PACK(16, 0, 80)
      G_ST4(80, 149, "0 offen") G_ST4(84, 149, "s52 offen")
      "s_nop 0\n\t"
      F_PACK(32, 0, 88)
      G_ST4(88, 149, "s54 offen") G_ST4(92, 149, "s55 offen")
      "s_setprio 1\n\t"
      "s_waitcnt vmcnt(0)\n\t"
      "buffer_store_dwordx2 v[2:3], v150, s[72:75], 0 offen sc1\n\t"
      G_C0 G_C1
      "s_branch 301f\n\t"
      "151:\n\t"
      "s_setprio 1\n\t"
      "300:\n\t"
      "s_waitcnt vmcnt(0) lgkmcnt(0)\n\t"
      "301:\n\t"
      "v_mov_b64 v[16:17], 0\n\t"
      "v_permlane16_swap_b32 v9, v8\n\t"
      QK(128, 143, 200, 203, 192, 195, 96, 111)
      ".irp j,0,1,2,3\n\tv_dot2c_f32_bf16 v16, v[192+\\j], v[192+\\j]\n\tv_dot2c_f32_bf16 v17, v[196+\\j], v[196+\\j]\n\t.endr\n\t"
      "v_max_f32 v9, v9, v8\n\t"
      "s_nop 1\n\t"
      "v_permlane32_swap_b32 v16, v17\n\t"
      "v_add_f32 v16, v16, v17\n\t"
      "v_mul_f32 v16, v16, v9\n\t"
      "v_cmp_nge_f32 vcc, 0x45100000, v16\n\t"
      "s_cbranch_vccnz 310f\n\t"
      "311:\n\t"
      ST(4)
      "s_cmp_eq_u32 s56, 0\n\t"
      "s_cbranch_scc1 320f\n\t"
      RUN("5")
      "s_branch 330f\n\t"
      "310:\n\t"
      "v_mov_b32 v96, 0xff7fffff\n\tv_mov_b32 v112, 0xff7fffff\n\t"
      "s_mov_b32 s52, s39\n\ts_mov_b32 s54, 63\n\t"
      "312:\n\t"
      "buffer_load_dwordx4 v[16:19], v4, s[72:75], s52 offen sc1\n\t"
      "s_add_u32 s52, s52, 0x800\n\t"
      "s_waitcnt vmcnt(0)\n\t"
      "v_mfma_f32_32x32x16_bf16 v[32:47], v[16:19], v[192:195], 0\n\t"
      "v_mfma_f32_32x32x16_bf16 v[48:63], v[16:19], v[196:199], 0\n\t"
      "s_nop 11\n\t"
      ".irp i,0,2,4,6,8,10,12,14\n\tv_max3_f32 v96, v96, v[32+\\i], v[33+\\i]\n\tv_max3_f32 v112, v112, v[48+\\i], v[49+\\i]\n\t.endr\n\t"
      "s_sub_u32 s54, s54, 1\n\t"
      "s_cbranch_scc0 312b\n\t"
      "v_mov_b32 v97, v96\n\tv_mov_b32 v113, v112\n\ts_nop 1\n\t"
      "v_permlane32_swap_b32 v96, v97\n\tv_permlane32_swap_b32 v112, v113\n\t"
      "v_max_f32 v96, v96, v97\n\tv_max_f32 v112, v112, v113\n\t"
      "v_xor_b32 v96, 0x80000000, v96\n\tv_xor_b32 v112, 0x80000000, v112\n\t"
      ".irp r,1,2,3,4,5,6,7,8,9,10,11,12,13,14,15\n\tv_mov_b32 v[96+\\r], v96\n\tv_mov_b32 v[112+\\r], v112\n\t.endr\n\t"
      "s_nop 1\n\t"
      QK(128, 143, 200, 203, 192, 195, 96, 111)
      "s_nop 11\n\t"
      "s_branch 311b\n\t"
      "320:\n\t"
      RUN("0")
      ST(5)
      "v_mov_b64 v[16:17], -1\n\tv_mov_b64 v[18:19], -1\n\t"
      G_ST4(16, 4, "s24 offen")
      RUN("0")
      "s_cmp_lg_u32 s37, 0\n\t"
      "s_cbranch_scc1 322f\n\t"
      "v_and_b32 v20, 0x1f0, v4\n\tv_lshlrev_b32 v20, 3, v20\n\ts_add_u32 s52, s29, 4096\n\t"
      "3210:\n\tbuffer_load_dwordx2 v[22:23], v20, s[72:75], s28 offen sc1\n\ts_waitcnt vmcnt(0)\n\t"
      "v_cmp_ne_u64 vcc, s[86:87], v[22:23]\n\t" G_SPIN(321, "s_sleep 4\n\t")
      P_LOAD8(32, 29, 52)
      "322:\n\t"
      RUN("0")
      ST(6)
      G_ST4(16, 4, "s25 offen")
      "s_cmp_lg_u32 s37, 0\n\t"
      "s_cbranch_scc1 323f\n\t"
      G_Z("24,26,28,30")
      "v_mov_b32 v20, 0x3f80\n\tv_mov_b32 v21, 0x3f800000\n\t"
      ".irp i,0,1,2,3,4,5,6,7\n\t.irp j,0,1,2,3\n\t"
      "v_dot2c_f32_bf16 v[24+2*\\j], v[32+4*\\i+\\j], v20\n\tv_dot2c_f32_bf16 v[25+2*\\j], v[32+4*\\i+\\j], v21\n\t"
      ".endr\n\t.endr\n\t"
      "v_and_b32 v20, 0xf0, v4\n\tv_lshrrev_b32 v21, 5, v4\n\tv_lshrrev_b32 v22, 7, v4\n\t"
      ".irp e,0,1,2,3\n\tv_permlane16_swap_b32 v[24+\\e], v[28+\\e]\n\t.endr\n\t"
      ".irp e,0,1,2,3\n\tv_add_f32 v[24+\\e], v[24+\\e], v[28+\\e]\n\t.endr\n\t"
      "v_and_or_b32 v20, v21, 8, v20\n\tv_and_or_b32 v20, v22, 4, v20\n\t"
      "v_permlane32_swap_b32 v24, v26\n\tv_permlane32_swap_b32 v25, v27\n\t"
      "v_add_f32 v24, v24, v26\n\tv_add_f32 v25, v25, v27\n\t"
      "v_cvt_pk_bf16_f32 v24, v24, v25\n\tv_min_u32 v24, -2, v24\n\t"
      "buffer_store_dword v24, v20, s[72:75], s30 offen sc1\n\t"
      "323:\n\t"
      RUN("0")
      ST(7)
      "v_add_u32 v20, s35, v4\n\t"
      P_LOAD(16, 0, 31) P_LOAD(20, 1, 31)
      ".irp i,0,1,2,3\n\tds_read_b128 v[32+4*\\i:35+4*\\i], v20 offset:1024*\\i\n\t.endr\n\t"
      RUN("0")
      ST(8)
      "3240:\n\t"
      "v_max3_u32 v80, v16, v17, v18\n\tv_max3_u32 v81, v19, v24, v25\n\tv_max3_u32 v80, v80, v26, v27\n\tv_max_u32 v80, v80, v81\n\t"
      "v_cmp_eq_u32 vcc, -1, v80\n\t"
      G_SPIN(324, "s_sleep 4\n\t" P_LOAD(16, 0, 31) P_LOAD(20, 1, 31) "s_waitcnt vmcnt(0)\n\t")
      "s_waitcnt lgkmcnt(0)\n\t"
      "v_mfma_f32_32x32x16_bf16 v[48:63], v[16:19], v[32:35], 0\n\t"
      "v_mfma_f32_32x32x16_bf16 v[64:79], v[16:19], v[40:43], 0\n\t"
      "v_mfma_f32_32x32x16_bf16 v[48:63], v[24:27], v[36:39], v[48:63]\n\t"
      "v_mfma_f32_32x32x16_bf16 v[64:79], v[24:27], v[44:47], v[64:79]\n\t"
      "v_sub_f32 v80, 1.0, v253\n\tv_sub_f32 v81, 1.0, v252\n\t"
      "v_mov_b32 v82, s86\n\tv_mov_b32 v83, s87\n\tv_mov_b32 v84, s36\n\t"
      "ds_write_b64 v84, v[82:83]\n\t"
      "s_nop 4\n\t"
      "v_add_f32 v56, 0x358637bd, v56\n\tv_rcp_f32 v56, v56\n\t"
      "v_add_f32 v72, 0x358637bd, v72\n\tv_rcp_f32 v72, v72\n\t"
      "v_mul_f32 v80, v80, v56\n\t"
      ".irp r,48,49,50,51,52,53,54,55\n\tv_mul_f32 v\\r, v80, v\\r\n\t.endr\n\t"
      "v_mul_f32 v81, v81, v72\n\t"
      ".irp i,0,1,2,3\n\tv_cvt_pk_bf16_f32 v[16+\\i], v[48+2*\\i], v[49+2*\\i]\n\t.endr\n\t"
      G_ST4(16, 4, "s26 offen")
      ".irp r,64,65,66,67,68,69,70,71\n\tv_mul_f32 v\\r, v81, v\\r\n\t.endr\n\t"
      ".irp i,0,1,2,3\n\tv_cvt_pk_bf16_f32 v[20+\\i], v[64+2*\\i], v[65+2*\\i]\n\t.endr\n\t"
      G_ST4(20, 4, "s27 offen")
      RUNV("0", STR(HALF_SWITCH))
      "v_cmp_eq_u32 vcc, 0, v4\n\tv_mov_b32 v84, s32\n\tv_bfrev_b32 v80, 15\n\t"
      "s_setprio 0\n\t"
      "v_cndmask_b32 v84, v80, v84, vcc\n\t"
      "buffer_store_dwordx2 v[82:83], v84, s[72:75], 0 offen sc1\n\t"
      "330:\n\t"
      ST(9)
      "v_and_b32 v20, 0x70, v4\n\tv_lshlrev_b32 v20, 3, v20\n\t"
      "buffer_load_dwordx2 v[22:23], v20, s[72:75], s33 offen sc1\n\t"
      RUN("0")
      ST(10)
      "v_mov_b32 v84, s36\n\ts_add_u32 s52, s34, 4096\n\t"
      "3310:\n\tds_read_b64 v[24:25], v84\n\ts_waitcnt lgkmcnt(0)\n\tv_cmp_ne_u64 vcc, s[86:87], v[24:25]\n\t" G_SPIN(331, "s_sleep 2\n\t")
      "3320:\n\tv_cmp_ne_u64 vcc, s[86:87], v[22:23]\n\t"
      G_SPIN(332, "s_sleep 4\n\tbuffer_load_dwordx2 v[22:23], v20, s[72:75], s33 offen sc1\n\ts_waitcnt vmcnt(0)\n\t")
      P_LOAD8(48, 34, 52)
      "v_add_u32 v6, s88, v4\n\tv_lshrrev_b32 v7, 2, v4\n\tv_add_u32 v7, s89, v7\n\t"
      "v_xor_b32 v8, 0x800, v6\n\tv_xor_b32 v9, 0x400, v7\n\tv_xor_b32 v1, 64, v0\n\t"
      "s_add_u32 s54, s34, 0x200000\n\ts_add_u32 s55, s34, 0x201000\n\t"
      RUN("0")
      ST(12)
      PV(176, 191, 244, 247, 152, 159)
      "ds_write_b128 v6, v[160:163]\n\tds_write_b128 v6, v[164:167] offset:1024\n\tds_write_b32 v7, v168\n\t"
      G_LBAR
      "ds_read_b32 v24, v9\n\tds_read_b128 v[16:19], v8\n\tds_read_b128 v[20:23], v8 offset:1024\n\t"
      "s_nop 3\n\t"
      "s_waitcnt lgkmcnt(2)\n\t"
      "v_add_f32 v24, v184, v24\n\tv_rcp_f32 v24, v24\n\t"
      "s_waitcnt lgkmcnt(0)\n\t"
      ".irp i,0,1,2,3,4,5,6,7\n\tv_add_f32 v[16+\\i], v[176+\\i], v[16+\\i]\n\t.endr\n\t"
      "v_mul_f32 v24, v253, v24\n\t"
      ".irp i,0,1,2,3,4,5,6,7\n\tv_mul_f32 v[16+\\i], v24, v[16+\\i]\n\t.endr\n\t"
      ".irp i,0,1,2,3\n\tv_cvt_pk_bf16_f32 v[84+\\i], v[16+2*\\i], v[17+2*\\i]\n\t.endr\n\t"
      ".irp i,0,1,2,3\n\tv_min_u32 v[84+\\i], -2, v[84+\\i]\n\t.endr\n\t"
      G_ST4(84, 4, "s38 offen")
      ST(13)
      P_RB(0) P_RB(1)
      P_USEB(0, 4, "0", "0", P_RB(2) P_RA(0) P_AT(0)) P_USEB(1, 8, G_A0, G_A1, P_RB(3) P_RA(1) P_AT(1))
      P_USEB(2, 12, G_A0, G_A1, P_RB(4) P_RA(2) P_AT(2)) P_USEB(3, 12, G_A0, G_A1, P_RB(5) P_RA(3) P_AT(3))
      P_USEB(4, 12, G_A0, G_A1, P_RB(6) P_RA(4) P_AT(4)) P_USEB(5, 12, G_A0, G_A1, P_RB(7) P_RA(5) P_AT(5))
      P_USEB(6, 12, G_A0, G_A1, P_RA(6) P_AT(6)) P_USEB(7, 8, G_A0, G_A1, P_RA(7) P_AT(7))
      "v_xor_b32 v6, 32, v248\n\tv_xor_b32 v7, 64, v248\n\tv_xor_b32 v8, 96, v248\n\t"
      "v_lshrrev_b32 v9, 7, v4\n\tv_bfe_u32 v10, v4, 3, 4\n\tv_xor_b32 v10, v10, v9\n\tv_lshlrev_b32 v10, 4, v10\n\t"
      "v_lshl_add_u32 v10, v9, 8, v10\n\tv_add_u32 v10, s90, v10\n\tv_xor_b32 v11, 16, v10\n\t"
      "v_and_b32 v12, 0x70, v4\n\tv_lshl_or_b32 v12, v9, 10, v12\n\tv_add_u32 v12, s91, v12\n\t"
      G_LBAR
      P_USEA(0, 7) P_USEA(1, 6) P_USEA(2, 5) P_USEA(3, 4) P_USEA(4, 3) P_USEA(5, 2) P_USEA(6, 1) P_USEA(7, 0)
      "s_nop 10\n\t"
      "ds_write_b128 v248, v[16:19]\n\tds_write_b128 v6, v[20:23]\n\tds_write_b128 v7, v[24:27]\n\tds_write_b128 v8, v[28:31]\n\t"
      "ds_write_b128 v248, v[32:35] offset:128\n\tds_write_b128 v6, v[36:39] offset:128\n\t"
      "ds_write_b128 v7, v[40:43] offset:128\n\tds_write_b128 v8, v[44:47] offset:128\n\t"
      ST(15)
      G_LBAR
      ".irp k,0,1,2,3\n\tds_read_b128 v[16+8*\\k:19+8*\\k], v10 offset:16384*\\k\n\tds_read_b128 v[20+8*\\k:23+8*\\k], v11 offset:16384*\\k\n\t.endr\n\t"
      "s_waitcnt lgkmcnt(4)\n\t"
      ".irp i,0,1,2,3,4,5,6,7\n\tv_add_f32 v[16+\\i], v[16+\\i], v[24+\\i]\n\t.endr\n\t"
      "s_waitcnt lgkmcnt(0)\n\t"
      ".irp i,0,1,2,3,4,5,6,7\n\tv_add_f32 v[32+\\i], v[32+\\i], v[40+\\i]\n\t.endr\n\t"
      ".irp i,0,1,2,3,4,5,6,7\n\tv_add_f32 v[16+\\i], v[16+\\i], v[32+\\i]\n\t.endr\n\t"
      ".irp i,0,1,2,3\n\tv_cvt_pk_bf16_f32 v[16+\\i], v[16+2*\\i], v[17+2*\\i]\n\t.endr\n\t"
      "buffer_store_dwordx4 v[16:19], v12, s[20:23], 0 offen" OUT_POL "\n\t"
      "v_cmp_eq_u32 vcc, 0, v4\n\tv_mov_b32 v20, s68\n\tv_bfrev_b32 v21, 15\n\tv_mov_b32 v22, s86\n\tv_mov_b32 v23, s87\n\t"
      "v_cndmask_b32 v20, v21, v20, vcc\n\t"
      "buffer_store_dwordx2 v[22:23], v20, s[72:75], 0 offen sc1\n\t"
      ST_END
      "s_branch 599f\n\t"
      P_STUBS(57, 56, 580) P_SLOW(580, 96, 54, 55)
      "599:\n\t"
      ".Lgm_code_to:"
      : "+{v[4:7]}"(xa), "+{v8}"(wa), "+{v[144:159]}"(vp), "+{v[10:15]}"(sr.wv), "+{s[24:27]}"(base) ST_OP
      : "{v0}"(pc.wo0), "{v248}"(pc.red), "{v249}"(pc.koff), "{v250}"(pc.voff0), "{v251}"(pc.voff1), "{v254}"(pc.wov), "{s[20:23]}"(pc.out),
        "{s37}"(w), "{s[40:43]}"(sr.ra), "{s[44:47]}"(sr.rb), "{s[48:51]}"(sr.rx), "{s[56:71]}"(sp), "{s[72:75]}"(pc.ws),
        "{s[80:83]}"(pc.wo), "{s91}"(outoff)
      : "memory", "scc", "vcc", "s28", "s29", "s30", "s31", "s32", "s33", "s34", "s35", "s36", "s38", "s39", "s52", "s53", "s54", "s55", "s76", "s77",
        "s84", "s85", "s88", "s89", "s90", "s78", "s79", "s86", "s87", "s92", "s93", "v1", "v2", "v3", "v9", "v252", "v253",
#define R8(a, b, c, d, e, f, g, h) "v" #a, "v" #b, "v" #c, "v" #d, "v" #e, "v" #f, "v" #g, "v" #h
        R8(16, 17, 18, 19, 20, 21, 22, 23), R8(24, 25, 26, 27, 28, 29, 30, 31), R8(32, 33, 34, 35, 36, 37, 38, 39),
        R8(40, 41, 42, 43, 44, 45, 46, 47), R8(48, 49, 50, 51, 52, 53, 54, 55), R8(56, 57, 58, 59, 60, 61, 62, 63),
        R8(64, 65, 66, 67, 68, 69, 70, 71), R8(72, 73, 74, 75, 76, 77, 78, 79), R8(80, 81, 82, 83, 84, 85, 86, 87),
        R8(88, 89, 90, 91, 92, 93, 94, 95), R8(96, 97, 98, 99, 100, 101, 102, 103), R8(104, 105, 106, 107, 108, 109, 110, 111),
        R8(112, 113, 114, 115, 116, 117, 118, 119), R8(120, 121, 122, 123, 124, 125, 126, 127), R8(128, 129, 130, 131, 132, 133, 134, 135),
        R8(136, 137, 138, 139, 140, 141, 142, 143), R8(160, 161, 162, 163, 164, 165, 166, 167), R8(168, 169, 170, 171, 172, 173, 174, 175),
        R8(176, 177, 178, 179, 180, 181, 182, 183), R8(184, 185, 186, 187, 188, 189, 190, 191), R8(192, 193, 194, 195, 196, 197, 198, 199),
        R8(200, 201, 202, 203, 204, 205, 206, 207), R8(208, 209, 210, 211, 212, 213, 214, 215), R8(216, 217, 218, 219, 220, 221, 222, 223),
        R8(224, 225, 226, 227, 228, 229, 230, 231), R8(232, 233, 234, 235, 236, 237, 238, 239), R8(240, 241, 242, 243, 244, 245, 246, 247) ST_CL);
}
DI void identity_seed(int r32, int h, unsigned& val, unsigned& hot) {
  val = 0x3F80u << ((r32 & 1) * 16);
  hot = (((r32 >> 2) & 1) == h ? 1u : 0u) << (((r32 >> 1) & 1) | ((r32 >> 2) & 6));
}

#define HG 2
#define ARGS_AT_ONCE 0
#define TLBALL 0
#define WARM 6
#define CODE_LINES 1
#define DBG_OFF 0u
#define WS_SIZE WS_END

struct Scalars { u32x16 sp; u32x4 base; unsigned outoff; };
DI Scalars scalars(unsigned b, unsigned w, unsigned lds0) {
  const unsigned xc = b & 7, in = b >> 3, g = HG * (xc % (8 / HG)) + in % HG, tb = (32 / HG) * (xc / (8 / HG)) + in / HG;
  const unsigned wi = w & 3, grp = w >> 2, pr = wi & 1, rot = (in / HG + (8 / HG) * (in % HG)) & 7;
  const unsigned hd = 4 * g + wi, otb = 4 * xc + (in & 3), og = in >> 2, rh = 4 * g + (tb & 3), sl = tb >> 2;
  Scalars s;
  s.sp = u32x16{grp, lds0 + L_RING + wi * 2048, lds0 + L_X + wi * 2048, rot, rot * 128, wi >> 1, tb * 2048, ((tb + 1) & 31) * 2048,
                ((tb + 2) & 31) * 2048, L_Q + (wi >> 1) * (L_VB - L_Q) + pr * 4096, L_Q + wi * 2048 + grp * 1024, L_PQ + pr * 8192,
                (b | w) ? OOB : WS_EPOCH, L_VB + pr * 4096, DBG_OFF, 0x3FB8AA3Bu  };
  s.base = u32x4{hd * 1024, rh * 8 + sl, ((otb * 2 + grp) * 32 + wi * 8) * 1024, 0u};
  s.outoff = (64 * otb + 8 * w) * 1024 + og * 128;
  return s;
}

__global__ __launch_bounds__(512, 2) void gated_mix_kernel(const char* __restrict__ x, const char* __restrict__ wqa,
                                                           const char* __restrict__ woa, const char* __restrict__ wqb,
                                                           const char* __restrict__ wphi, const char* __restrict__ wob,
                                                           const char* __restrict__ gatew, char* __restrict__ out,
                                                           char* __restrict__ ws) {
  __shared__ __attribute__((aligned(16))) char lds_raw[L_BYTES];
  LAS char* lds = (LAS char*)lds_raw;
  const unsigned lds0 = (unsigned)(uintptr_t)lds;
  const int tid = threadIdx.x, lane = tid & 63, w = __builtin_amdgcn_readfirstlane(tid >> 6);
  const int xc = blockIdx.x & 7, in = blockIdx.x >> 3;
  const int g = HG * (xc % (8 / HG)) + in % HG, tb = (32 / HG) * (xc / (8 / HG)) + in / HG;
  const int r32 = lane & 31, h = lane >> 5;
  const int wi = w & 3, grp = w >> 2, pr = wi & 1;
  STAMP(0); STAMP_RT(62);

  const int l8 = lane >> 3, dcp = lane & 7;
  Src sr;
  sr.ra = __builtin_amdgcn_make_buffer_rsrc((void*)wqa, 0, 512 * 3072, 0x00020000);
  sr.rx = __builtin_amdgcn_make_buffer_rsrc((void*)x, 0, 2048 * 1024, 0x00020000);
  const unsigned wv = (unsigned)(16 * wi + l8) * 3072 + g * 128 + ((dcp ^ ((lane >> 2) & 4)) * 16);
#pragma unroll
  for (int c = 0; c < 3; ++c) { sr.wv[2 * c] = wv + c * 1024; sr.wv[2 * c + 1] = wv + c * 1024 + 8 * 3072 - 1024; }
  const unsigned rot = (in / HG + (8 / HG) * (in % HG)) & 7;
  const unsigned xv0 = (unsigned)(64 * tb + 16 * wi + l8) * 1024 + ((dcp ^ (l8 >> 1)) * 16), xv1 = ((xv0 + 8192) ^ 64) - 1024;
  const unsigned ml = lds0 + L_RING + wi * 2048, mx = lds0 + L_X + wi * 2048;
  const unsigned rotx = rot * 128;
  if (grp) first_chunks(sr, xv0, xv1, ml, mx, rot, rotx);
  SBAR();
  const unsigned hd = 4 * g + wi, og = in >> 2;
  sr.rb = __builtin_amdgcn_make_buffer_rsrc((void*)wqb, 0, 512 * 3072, 0x00020000);
  const rsrc_t rws = __builtin_amdgcn_make_buffer_rsrc((void*)ws, 0, WS_SIZE, 0x00020000);
  if (grp == 0) {
    if (wi < 2) dma16(__builtin_amdgcn_make_buffer_rsrc((void*)wphi, 0, 32 * 512, 0x00020000), lane * 16, g * 2048 + wi * 1024, lds0 + L_WPHI + wi * 1024);
    if (wi == 2) dma16(__builtin_amdgcn_make_buffer_rsrc((void*)gatew, 0, 1024, 0x00020000), lane * 16, 0, lds0 + L_GW);
    {
      const unsigned sh = in / HG, nsh = 32 / HG;
      if (wi < 2 * HG / 2 + (HG == 1)) {
        const unsigned i = (sh * HG + wi % HG) * 64 + lane;
        const unsigned row = (i >> 1) & 511, sg = i & 1, st = i >> 10;
        const rsrc_t r = (st && !sg) ? sr.ra : sr.rb;
      }
      if (wi == 0) {
        asm volatile("s_mov_b32 m0, %0\n\ts_nop 0\n\tbuffer_load_dword %1, %2, 0 offen sc1 lds" :: "s"(lds0 + L_DUMMY), "v"((TLBALL ? lane + 64 * (in & 3) : in * 8 + l8) * 65536 + 0xFF80), "s"(rws) : "memory");
      }
      if (wi == 1) {
        const rsrc_t ro = __builtin_amdgcn_make_buffer_rsrc((void*)out, 0, 2048 * 1024, 0x00020000);
        asm volatile("s_mov_b32 m0, %0\n\ts_nop 0\n\tbuffer_load_dword %1, %2, 0 offen sc1 lds" :: "s"(lds0 + L_DUMMY), "v"((TLBALL ? (lane & 31) : in) * 65536 + 0xFF80), "s"(ro) : "memory");
      }
      if (wi == 2) {
        const unsigned i = blockIdx.x * 32 + (lane & 31);
        const rsrc_t r = __builtin_amdgcn_make_buffer_rsrc((void*)(blockIdx.x & 128 ? wob : woa), 0, 512 * 1024, 0x00020000);
        asm volatile("s_mov_b32 m0, %0\n\ts_nop 0\n\tbuffer_load_dword %1, %2, 0 offen lds" :: "s"(lds0 + L_DUMMY), "v"((i & 4095) * 128), "s"(r) : "memory");
      }
    }
    if (wi == 3) {
      unsigned long long pc;
      unsigned len;
      asm volatile("s_getpc_b64 %0\n.Lgm_code_from:\n\ts_mov_b32 %1, .Lgm_code_to-.Lgm_code_from" : "=s"(pc), "=s"(len));
      const rsrc_t rc = __builtin_amdgcn_make_buffer_rsrc((void*)pc, 0, len, 0x00020000);
      asm volatile("s_mov_b32 m0, %0\n\ts_nop 0\n\tbuffer_load_dword %1, %2, 0 offen lds" :: "s"(lds0 + L_DUMMY), "v"((in + 32 * l8) * 128), "s"(rc) : "memory");
    }
  }

  const unsigned kvb = WS_KV + (unsigned)hd * 131072, kvh = kvb + grp * 65536;
  Prog pc;
  pc.ws = rws;
  pc.koff = kvh + lane * 16;
  {
    const unsigned vreal = kvh + h * 256 + (lane & 15) * 16, vones = WS_ONES + g * 256;
    const bool pad = lane & 16, one = (lane & 11) == 0;
    pc.voff0 = pad ? (one ? vones - 1024 : OOB) : vreal; pc.voff1 = pad ? (one ? vones - 1536 : OOB) : vreal;
  }
  const int tq = (lane & 15) >> 2, tp_ = lane & 3, tnb = (lane >> 4) & 1;
  const unsigned wlane = L_RING + (8 * h + tq) * 128 + (((2 * pr + tnb) ^ (tq & 2)) * 32) + tp_ * 8;
  u32x4 xa;
#pragma unroll
  for (int s = 0; s < 4; ++s) xa[s] = L_X + r32 * 128 + (((2 * s + h) ^ ((r32 >> 1) & 7)) * 16);
  unsigned vp[16];
  if (grp == 0) {
    const unsigned kb = WS_KV + (unsigned)(4 * g + 2 * pr) * 131072 + (unsigned)(2 * tb) * 2048, fl = WS_FKV + (g * 32 + tb) * FL;
    const int sw = (r32 & 19) | ((r32 & 4) << 1) | ((r32 & 8) >> 1);
    vp[0] = vp[1] = vp[2] = vp[3] = vp[4] = 0;
    vp[5] = wi < 2 ? kb + h * 512 + sw * 16 : kb + (r32 >> 4) * 131072 + 1024 + h * 256 + (r32 & 15) * 16;
    vp[6] = wi < 2 ? (r32 == 0 ? fl + (2 * pr + h) * 16 + 8 : OOB) : (wi == 2 && lane < 8 ? WS_ONES + tb * 2048 + g * 256 + lane * 16 : OOB);
    vp[7] = w == 0 ? WS_KR + ((4 * g + (tb & 3)) * 8 + (tb >> 2)) * 256 + lane * 4 : OOB;
    vp[8] = L_X + r32 * 128 + (((2 * wi + h) ^ ((r32 >> 1) & 7)) * 16);
    vp[9] = L_GW + rotx + wi * 32 + h * 16;
    vp[10] = L_GP + wi * 512 + lane * 4;
    vp[11] = tid < 4 ? fl + lane * 16 : OOB;
  } else {
    vp[0] = xv0; vp[1] = xv1; vp[4] = vp[7] = vp[9] = vp[11] = 0;
    vp[2] = L_WPHI + 2 * pr * 512 + (4 * h + tq) * 32 + tp_ * 8;
    vp[3] = tnb ? 0x80008000u : 0u;
    vp[5] = WS_KP + (unsigned)(((4 * g + 2 * pr) * 8 + (lane >> 4)) * 32 + tb) * 256 + (lane & 15) * 16;
    vp[6] = lane == 0 ? WS_FKP + ((g * 2 + pr) * 32 + tb) * FL : OOB;
    vp[8] = tnb ? 0x3F800000u : 0u;
    vp[10] = L_GP + 256 + lane * 4;
  }
  {
    unsigned val, hot;
    identity_seed(r32, h, val, hot);
    vp[12] = val; vp[13] = hot; vp[14] = WS_FKV + (g * 32 + r32) * FL + wi * 16; vp[15] = lane * 16;
  }
  pc.out = __builtin_amdgcn_make_buffer_rsrc((void*)out, 0, 2048 * 1024, 0x00020000);
  pc.wo = __builtin_amdgcn_make_buffer_rsrc((void*)(grp ? woa : wob), 0, 512 * 1024, 0x00020000);
  pc.wov = (unsigned)(128 * wi + l8) * 1024 + og * 128 + ((dcp ^ ((lane >> 2) & 4)) * 16);
  pc.wo0 = L_WO + wi * 32768 + (unsigned)(4 * h + tq) * 128 + ((tnb ^ (tq & 2)) * 32) + tp_ * 8;
  pc.red = L_RED + wi * 16384 + (grp * 32 + r32) * 256 + ((h ^ (r32 & 7)) * 16);

  unsigned bi = blockIdx.x, wv_ = w;
  asm volatile("" : "+s"(bi), "+s"(wv_));
  const Scalars sc = scalars(bi, wv_, lds0);
  program(sr, xa, wlane + (wi >> 1) * 8192,
          u32x16{vp[0], vp[1], vp[2], vp[3], vp[4], vp[5], vp[6], vp[7], vp[8], vp[9], vp[10], vp[11], vp[12], vp[13], vp[14], vp[15]}, sc.sp,
          sc.base, wv_, sc.outoff, pc ST_ARG);
}

}

extern "C" void kernel_launch(void* const* d_in, const int* in_sizes, int n_in, void* d_out, int out_size, void* d_ws,
                              size_t ws_size, hipStream_t stream) {
  gated_mix_kernel<<<256, 512, 0, stream>>>((const char*)d_in[0], (const char*)d_in[1], (const char*)d_in[2], (const char*)d_in[4],
                                            (const char*)d_in[5], (const char*)d_in[6], (const char*)d_in[7], (char*)d_out, (char*)d_ws);
}
```

```cpp
#include <hip/hip_runtime.h>
#include <hip/hip_bf16.h>
#include <stdint.h>

namespace {

typedef __bf16 bf16x8 __attribute__((ext_vector_type(8)));
typedef __bf16 bf16x2 __attribute__((ext_vector_type(2)));
typedef float f32x16 __attribute__((ext_vector_type(16)));
typedef float f32x8 __attribute__((ext_vector_type(8)));
typedef float f32x4 __attribute__((ext_vector_type(4)));
typedef float f32x2 __attribute__((ext_vector_type(2)));
typedef unsigned u32x16 __attribute__((ext_vector_type(16)));
typedef unsigned u32x8 __attribute__((ext_vector_type(8)));
typedef unsigned u32x4 __attribute__((ext_vector_type(4)));
typedef unsigned u32x2 __attribute__((ext_vector_type(2)));
typedef short s16x4 __attribute__((ext_vector_type(4)));
typedef __amdgpu_buffer_rsrc_t rsrc_t;
#define LAS __attribute__((address_space(3)))
#define DI __device__ __forceinline__

constexpr unsigned WS_EPOCH = 0;
constexpr unsigned FL = 128;
constexpr unsigned WS_KR = 0x10000;
constexpr unsigned WS_ONES = 0x40000;
constexpr unsigned WS_FKV = 0x60000;
constexpr unsigned WS_FKP = 0x70000;
constexpr unsigned WS_FOB = 0x80000;
constexpr unsigned WS_KP = 0x100000;
constexpr unsigned WS_OB = 0x500000;
constexpr unsigned WS_OA = 0x700000;
constexpr unsigned WS_KV = 0x900000;
constexpr unsigned WS_END = 0xD00000;

constexpr unsigned L_X = 0;
constexpr unsigned L_RING = 65536;
constexpr unsigned L_WO = 0;
constexpr unsigned L_RED = 0;
constexpr unsigned L_PQ = 131072;
constexpr unsigned L_Q = 147456;
constexpr unsigned L_WPHI = L_Q;
constexpr unsigned L_GW = L_Q + 2048;
constexpr unsigned L_DUMMY = L_Q + 3072;
constexpr unsigned L_GP = L_Q + 4096;
constexpr unsigned L_VB = 155648;
constexpr unsigned L_BYTES = 163840;

constexpr int SC1 = 16;
constexpr unsigned OOB = 0xF0000000u;

DI void dma16(rsrc_t r, unsigned voff, unsigned soff, unsigned lds_dst) {
  asm volatile("s_mov_b32 m0, %0\n\ts_nop 0\n\tbuffer_load_dwordx4 %1, %2, %3 offen lds"
               :: "s"(lds_dst), "v"(voff), "s"(r), "s"(soff) : "memory");
}
#define SBAR() __builtin_amdgcn_sched_barrier(0)

#define EX(r) "v_exp_f32 v" #r ", v" #r "\n\t"
#define QK(s0, s1, k0, k1, q0, q1, c0, c1) "v_mfma_f32_32x32x16_bf16 v[" #s0 ":" #s1 "], v[" #k0 ":" #k1 "], v[" #q0 ":" #q1 "], v[" #c0 ":" #c1 "]\n\t"
#define PV(o0, o1, a0, a1, p0, p1) "v_smfmac_f32_32x32x32_bf16 v[" #o0 ":" #o1 "], v[" #a0 ":" #a1 "], v[" #p0 ":" #p1 "], v5\n\t"
#define LOADS(k0, k1, a0, a1, b0, b1)                                                     \
  "s_add_u32 s76, s76, 0x800\n\t"                                                         \
  "s_and_b32 s76, s76, 0xffff\n\t"                                                        \
  "buffer_load_dwordx4 v[" #k0 ":" #k1 "], v249, s[72:75], s76 offen sc1\n\t"             \
  "buffer_load_dwordx4 v[" #a0 ":" #a1 "], v250, s[72:75], s76 offen offset:1024 sc1\n\t" \
  "buffer_load_dwordx4 v[" #b0 ":" #b1 "], v251, s[72:75], s76 offen offset:1536 sc1\n\t"
#define WODMA                                                                             \
  "s_mov_b32 m0, s84\n\t"                                                                 \
  "s_add_u32 s84, s84, 0x400\n\t"                                                         \
  "buffer_load_dwordx4 v254, s[80:83], s85 offen lds\n\t"                                 \
  "s_add_u32 s85, s85, 0x2000\n\t"
#define ITER(ck0, ck1, ca0, ca1, cb0, cb1, pk0, pk1, pa0, pa1, pb0, pb1, nk0, nk1, EXTRA, WAIT) \
  EX(128) EX(129) PV(176, 191, pb0, pb1, 152, 159) EX(130) EX(131)                        \
  LOADS(pk0, pk1, pa0, pa1, pb0, pb1)                                                     \
  EX(132) EX(133) QK(144, 159, ck0, ck1, 196, 199, 112, 127) EX(134) EX(135) EXTRA EX(136) EX(137) EX(138) \
  PV(160, 175, ca0, ca1, 128, 135) EX(139) EX(140) EX(141) EX(142) EX(143)                 \
  EX(144) EX(145) PV(160, 175, cb0, cb1, 136, 143) EX(146) EX(147) EX(148) EX(149)         \
  WAIT                                                                                    \
  QK(128, 143, nk0, nk1, 192, 195, 96, 111) EX(150) EX(151) EX(152) EX(153) EX(154)      \
  PV(176, 191, ca0, ca1, 144, 151) EX(155) EX(156) EX(157) EX(158) EX(159)

#define ATTN_ROUTINE                                                                                  \
  "400:\n\t"                                                                                          \
  ITER(200, 203, 216, 219, 220, 223, 212, 215, 240, 243, 244, 247, 204, 207, WODMA, "")               \
  ITER(204, 207, 224, 227, 228, 231, 200, 203, 216, 219, 220, 223, 208, 211, "", "")                  \
  "s_cbranch_vccnz 402f\n\t401:\n\t"                                                                 \
  ITER(208, 211, 232, 235, 236, 239, 204, 207, 224, 227, 228, 231, 212, 215, WODMA, "s_waitcnt vmcnt(8)\n\t") \
  ITER(212, 215, 240, 243, 244, 247, 208, 211, 232, 235, 236, 239, 200, 203, "", "s_waitcnt vmcnt(7)\n\t")    \
  "s_sub_u32 s77, s77, 1\n\ts_waitcnt vmcnt(0)\n\ts_cbranch_scc0 400b\n\ts_setpc_b64 s[78:79]\n\t"  \
  "402:\n\ts_setprio 0\n\ts_branch 401b\n\t"
#define RUNV(N, V) "s_mov_b64 vcc, " V "\n\ts_mov_b32 s77, " N "\n\ts_swappc_b64 s[78:79], s[92:93]\n\t"
#define RUN(N) RUNV(N, "0")

#define STAMP(i) do { } while (0)
#define STAMP_RT(i) do { } while (0)
#define ST(i) ""
#define ST_END ""
#define ST_OP
#define ST_CL
#define ST_PAR
#define ST_ARG

#define G_BAR(N) "s_waitcnt vmcnt(" #N ") lgkmcnt(0)\n\ts_barrier\n\t"
#define G_LBAR "s_waitcnt lgkmcnt(0)\n\ts_barrier\n\t"
#define G_EPOCH "buffer_load_dwordx2 v[2:3], off, s[72:75], 0 sc1\n\t"
#define G_EPOCH_USE "v_readfirstlane_b32 s86, v2\n\tv_readfirstlane_b32 s87, v3\n\ts_add_u32 s86, s86, 1\n\ts_addc_u32 s87, s87, 0\n\t"
#define D_ROW(KC) "s_xor_b32 s52, s59, " KC "\n\ts_mul_i32 s52, s52, 196608\n\t"
#define D_A(R, COL, LOFF) "s_add_u32 m0, s57, " LOFF "\n\ts_nop 0\n\tbuffer_load_dwordx4 v[10+2*" #COL "], " R ", s52 offen lds\n\t"
#define D_B(R, COL) "buffer_load_dwordx4 v[11+2*" #COL "], " R ", s52 offen offset:1024 lds\n\t"
#define RA "s[40:43]"
#define RB "s[44:47]"
#define D_X(C)                                                                                        \
  "s_add_u32 m0, s58, 8192*" #C "\n\ts_xor_b32 s52, s60, 128*" #C "\n\t"                              \
  "buffer_load_dwordx4 v144, s[48:51], s52 offen lds\n\tbuffer_load_dwordx4 v145, s[48:51], s52 offen offset:1024 lds\n\t"
#define D_W(N, R0, C0, R1, C1) D_ROW(#N "&7") D_A(R0, C0, "16384*(" #N "&3)"), D_B(R0, C0), D_A(R1, C1, "16384*(" #N "&3)+8192"), D_B(R1, C1)
#define D_S0(N) D_W(N, RA, 1, RA, 2)
#define D_S1(N) D_W(N, RB, 0, RB, 1)
#define D_S2(N) D_W(N, RA, 0, RB, 2)
#define G_CAT(A, B, C, D) A B C D
#define G_APPLY(M, ...) M(__VA_ARGS__)
#define G_ALL(L) G_APPLY(G_CAT, L)

#define G_RW(W, C, S)                                                                                 \
  "ds_read_b64_tr_b16 v[" #W "+4*" #S ":" #W "+4*" #S "+1], v8 offset:16384*(" #C "&3)+2048*" #S "\n\t"       \
  "ds_read_b64_tr_b16 v[" #W "+4*" #S "+2:" #W "+4*" #S "+3], v8 offset:16384*(" #C "&3)+2048*" #S "+512\n\t"
#define G_RX(X, C, S, T) "ds_read_b128 v[" #X "+4*" #S ":" #X "+4*" #S "+3], v[4+" #S "] offset:8192*" #C "+4096*" #T "\n\t"
#define G_RS(W, X0, X1, C, S) G_RW(W, C, S) G_RX(X0, C, S, 0) G_RX(X1, C, S, 1)
#define G_READ(W, X0, X1, C) G_RS(W, X0, X1, C, 0) G_RS(W, X0, X1, C, 1) G_RS(W, X0, X1, C, 2) G_RS(W, X0, X1, C, 3)
#define G_M(ACC, W, X, S, CIN) \
  "v_mfma_f32_32x32x16_bf16 " ACC ", v[" #W "+4*" #S ":" #W "+4*" #S "+3], v[" #X "+4*" #S ":" #X "+4*" #S "+3], " CIN "\n\t"
#define G_A0 "v[16:31]"
#define G_A1 "v[32:47]"
#define G_MMA2(W, X0, X1, S) G_M(G_A0, W, X0, S, G_A0) G_M(G_A1, W, X1, S, G_A1)
#define G_STEP(RW, RX0, RX1, C, W, X0, X1, C0, C1, D0, D1, D2, D3)                                    \
  G_M(G_A0, W, X0, 0, C0) G_RW(RW, C, 0) G_RX(RX0, C, 0, 0) G_M(G_A1, W, X1, 0, C1) G_RX(RX1, C, 0, 1) G_RW(RW, C, 1) \
  G_M(G_A0, W, X0, 1, G_A0) G_RX(RX0, C, 1, 0) G_RX(RX1, C, 1, 1) D0 G_M(G_A1, W, X1, 1, G_A1) G_RW(RW, C, 2) G_RX(RX0, C, 2, 0) \
  G_M(G_A0, W, X0, 2, G_A0) G_RX(RX1, C, 2, 1) G_RW(RW, C, 3) D1 G_M(G_A1, W, X1, 2, G_A1) G_RX(RX0, C, 3, 0) G_RX(RX1, C, 3, 1) \
  G_M(G_A0, W, X0, 3, G_A0) D2 G_M(G_A1, W, X1, 3, G_A1) D3
#define G_EVEN(C, D0, D1, D2, D3) G_STEP(48, 64, 80, C, 96, 112, 128, G_A0, G_A1, D0, D1, D2, D3)
#define G_ODD(C, D0, D1, D2, D3) G_STEP(96, 112, 128, C, 48, 64, 80, G_A0, G_A1, D0, D1, D2, D3)
#define G_FIRST(D0, D1, D2, D3) G_STEP(96, 112, 128, 1, 48, 64, 80, "0", "0", D0, D1, D2, D3)
#define A_PACK                                                                                        \
  ".irp i,0,1,2,3\n\tv_cvt_pk_bf16_f32 v[48+\\i], v[16+2*\\i], v[17+2*\\i]\n\tv_cvt_pk_bf16_f32 v[56+\\i], v[24+2*\\i], v[25+2*\\i]\n\t.endr\n\t" \
  ".irp i,0,1,2,3\n\tv_cvt_pk_bf16_f32 v[52+\\i], v[32+2*\\i], v[33+2*\\i]\n\tv_cvt_pk_bf16_f32 v[60+\\i], v[40+2*\\i], v[41+2*\\i]\n\t.endr\n\t"
#define G_Z(L) ".irp r," L "\n\tv_mov_b64 v[\\r:\\r+1], 0\n\t.endr\n\t"
#define G_ZO G_Z("160,162,164,166,168,170,172,174,176,178,180,182,184,186,188,190,244,246")
#define G_QPRE "v_add_u32 v6, s66, v159\n\tv_mov_b32 v4, v159\n\tv_xor_b32 v7, 0x400, v6\n\tv_mov_b32 v5, 0xdddddddd\n\t"
#define G_QRD "ds_read_b128 v[192:195], v7\n\tds_read_b128 v[196:199], v6\n\t"
#define G_C(L) ".irp r," L "\n\tv_mov_b32 v\\r, 0xc2400000\n\t.endr\n\t"
#define G_C0 G_C("96,97,98,99,100,101,102,103,104,105,106,107,108,109,110,111")
#define G_C1 G_C("112,113,114,115,116,117,118,119,120,121,122,123,124,125,126,127")
#define G_ST4(R, V, O) "buffer_store_dwordx4 v[" #R ":" #R "+3], v" #V ", s[72:75], " O " sc1\n\t"
#define G_POLL(R) "buffer_load_dwordx3 v[" #R ":" #R "+2], v158, s[72:75], 0 offen sc1\n\t"
#define G_SPIN(L, AGAIN) "s_cbranch_vccz " #L "1f\n\t" AGAIN "s_sub_u32 s53, s53, 1\n\ts_cbranch_scc0 " #L "0b\n\ts_mov_b32 s53, 0\n\t" #L "1:\n\t"
#define G_POLLWAIT(L, R)                                                                              \
  #L "0:\n\ts_waitcnt vmcnt(0)\n\tv_cmp_ne_u64 vcc, s[86:87], v[" #R ":" #R "+1]\n\t"                  \
  G_SPIN(L, "s_sleep 4\n\t" G_POLL(R)) "s_waitcnt vmcnt(0)\n\tv_mov_b32 v9, v[" #R "+2]\n\t"
#define G_KV1(K, V, T)                                                                                \
  "buffer_load_dwordx4 v[" #K ":" #K "+3], v249, s[72:75], s" #T " offen sc1\n\t"                      \
  "buffer_load_dwordx4 v[" #V ":" #V "+3], v250, s[72:75], s" #T " offen offset:1024 sc1\n\t"          \
  "buffer_load_dwordx4 v[" #V "+4:" #V "+7], v251, s[72:75], s" #T " offen offset:1536 sc1\n\t"
#define G_KV G_KV1(200, 216, 62) G_KV1(204, 224, 63) G_KV1(208, 232, 64)
#define DPPMAX(R, CTRL) "v_max_f32_dpp v" #R ", v" #R ", v" #R " " CTRL " row_mask:0xf bank_mask:0xf\n\t"
#define SIGMOID(R, OUT) \
  "v_mul_f32 v" #R ", 0xbfb8aa3b, v" #R "\n\tv_exp_f32 v" #R ", v" #R "\n\ts_nop 0\n\tv_add_f32 v" #R ", 1.0, v" #R "\n\tv_rcp_f32 v" #OUT ", v" #R "\n\t"
#define A_GREAD(C)                                                                                    \
  "v_xor_b32 v92, 128*" #C ", v153\n\tv_xor_b32 v93, 128*(" #C "+1), v153\n\t"                     \
  "ds_read_b128 v[72:75], v152 offset:8192*" #C "\n\tds_read_b128 v[80:83], v152 offset:8192*" #C "+4096\n\t" \
  "ds_read_b128 v[76:79], v152 offset:8192*(" #C "+1)\n\tds_read_b128 v[84:87], v152 offset:8192*(" #C "+1)+4096\n\t" \
  "ds_read_b128 v[64:67], v92\n\tds_read_b128 v[68:71], v93\n\t"
#define A_GDOT                                                                                        \
  ".irp j,0,1,2,3\n\t"                                                                                \
  "v_dot2c_f32_bf16 v88, v[72+\\j], v[64+\\j]\n\tv_dot2c_f32_bf16 v89, v[80+\\j], v[64+\\j]\n\t"       \
  "v_dot2c_f32_bf16 v90, v[76+\\j], v[68+\\j]\n\tv_dot2c_f32_bf16 v91, v[84+\\j], v[68+\\j]\n\t"       \
  ".endr\n\t"
#define A_DPPMAX(CTRL) "s_nop 1\n\tv_max_f32_dpp v96, v96, v96 " CTRL " row_mask:0xf bank_mask:0xf\n\t"
#define F_JOBS(M) M(16, 80, 48) M(32, 84, 56) M(96, 88, 64) M(112, 92, 72)
#define F_MAX1(E, T, P)                                                                               \
  "v_max3_f32 v[" #T "], v[" #E "], v[" #E "+1], v[" #E "+2]\n\t"                                      \
  "v_max3_f32 v[" #T "+2], v[" #E "+8], v[" #E "+9], v[" #E "+10]\n\t"                                 \
  "v_max3_f32 v[" #T "+1], v[" #E "+3], v[" #E "+4], v[" #E "+5]\n\t"                                  \
  "v_max3_f32 v[" #T "+3], v[" #E "+11], v[" #E "+12], v[" #E "+13]\n\t"                               \
  "v_max3_f32 v[" #T "], v[" #T "], v[" #E "+6], v[" #E "+7]\n\t"                                      \
  "v_max3_f32 v[" #T "+2], v[" #T "+2], v[" #E "+14], v[" #E "+15]\n\t"                                \
  "v_max_f32 v[" #T "], v[" #T "], v[" #T "+1]\n\t"                                                    \
  "v_max_f32 v[" #T "+2], v[" #T "+2], v[" #T "+3]\n\t"
#define F_SWAP(E, T, P) "v_permlane32_swap_b32 v[" #T "], v[" #T "+2]\n\t"
#define F_MAX2(E, T, P) "v_max_f32 v[" #T "], v[" #T "], v[" #T "+2]\n\t"
#define F_NEG(E, T, P) "v_mul_f32 v[" #T "], 0xbfb8aa3b, v[" #T "]\n\t"
#define F_DUP(E, T, P) "v_mov_b32 v[" #T "+2], v[" #T "]\n\t"
#define F_FMA(E, T)                                                                                   \
  ".irp i,0,1,2,3,4,5,6,7\n\t"                                                                        \
  "v_fma_f32 v[" #E "+\\i], v[" #E "+\\i], s71, v[" #T "]\n\t"                                      \
  "v_fma_f32 v[" #E "+8+\\i], v[" #E "+8+\\i], s71, v[" #T "+2]\n\t"                                \
  ".endr\n\t"
#define F_EX(E, L) ".irp i," L "\n\tv_exp_f32 v[" #E "+\\i], v[" #E "+\\i]\n\t.endr\n\t"
#define F_EXP(E, T) F_FMA(E, T) F_EX(E, "0,8,1,9,2,10,3,11,4,12,5,13,6,14,7,15")
#define F_SUM1(E, T, P) ".irp i,0,1,2,3,4,5,6,7\n\tv_add_f32 v[" #P "+\\i], v[" #E "+2*\\i], v[" #E "+2*\\i+1]\n\t.endr\n\t"
#define F_SUM2(E, T, P) ".irp i,0,2,4,6\n\tv_add_f32 v[" #P "+\\i], v[" #P "+\\i], v[" #P "+\\i+1]\n\t.endr\n\t"
#define F_SUM3(E, T, P) "v_add_f32 v[" #T "], v[" #P "], v[" #P "+2]\n\tv_add_f32 v[" #T "+2], v[" #P "+4], v[" #P "+6]\n\t"
#define F_ADD(E, T, P) "v_add_f32 v[" #T "], v[" #T "], v[" #T "+2]\n\t"
#define F_RCP(E, T, P) "v_rcp_f32 v[" #T "], v[" #T "]\n\t"
#define F_PACK(E, T, P) ".irp i,0,1,2,3,4,5,6,7\n\tv_cvt_pk_bf16_f32 v[" #P "+\\i], v[" #E "+2*\\i], v[" #E "+2*\\i+1]\n\t.endr\n\t"
#define F_SCALE(E, T, P)                                                                              \
  ".irp i,0,1,2,3,4,5,6,7\n\t"                                                                        \
  "v_mul_f32 v[" #E "+\\i], v[" #E "+\\i], v[" #T "]\n\t"                                              \
  "v_mul_f32 v[" #E "+8+\\i], v[" #E "+8+\\i], v[" #T "+2]\n\t"                                        \
  ".endr\n\t" F_PACK(E, T, P)
#define F_TR0(E, T, P) "v_mfma_f32_32x32x16_bf16 v[" #E ":" #E "+15], v[" #P ":" #P "+3], v[128:131], 0\n\t"
#define F_TR1(E, T, P) "v_mfma_f32_32x32x16_bf16 v[" #E ":" #E "+15], v[" #P "+4:" #P "+7], v[132:135], v[" #E ":" #E "+15]\n\t"
#define P_TEST(F, I)                                                                                  \
  "v_max3_u32 v80, v[" #F "+4*" #I "], v[" #F "+1+4*" #I "], v[" #F "+2+4*" #I "]\n\t"                 \
  "v_max_u32 v80, v80, v[" #F "+3+4*" #I "]\n\t"                                                       \
  "v_cmp_eq_u32 vcc, -1, v80\n\t"
#define P_LOAD(F, I, SO) "buffer_load_dwordx4 v[" #F "+4*" #I ":" #F "+3+4*" #I "], v4, s[72:75], s" #SO " offen offset:1024*(" #I "&3) sc1\n\t"
#define P_RD(R, I, OFF)                                                                               \
  "ds_read_b64_tr_b16 v[" R ":" R "+1], v0 offset:" #OFF "+2048*" #I "\n\t"                         \
  "ds_read_b64_tr_b16 v[" R "+2:" R "+3], v0 offset:" #OFF "+2048*" #I "+1024\n\t"                  \
  "ds_read_b64_tr_b16 v[" R "+4:" R "+5], v1 offset:" #OFF "+2048*" #I "\n\t"                       \
  "ds_read_b64_tr_b16 v[" R "+6:" R "+7], v1 offset:" #OFF "+2048*" #I "+1024\n\t"
#define P_RB(I) P_RD("192+8*(" #I "&3)", I, 0)
#define P_RA(I) P_RD("128+8*" #I, I, 16384)
#define P_MM(W, F, I, C0, C1)                                                                         \
  "v_mfma_f32_32x32x16_bf16 v[16:31], v[" W ":" W "+3], v[" #F "+4*" #I ":" #F "+3+4*" #I "], " C0 "\n\t"   \
  "v_mfma_f32_32x32x16_bf16 v[32:47], v[" W "+4:" W "+7], v[" #F "+4*" #I ":" #F "+3+4*" #I "], " C1 "\n\t"
#define P_USEB(I, N, C0, C1, MORE)                                                                    \
  "s_waitcnt lgkmcnt(" #N ")\n\t" P_MM("192+8*(" #I "&3)", 48, I, C0, C1) MORE
#define P_USEA(I, N) "s_waitcnt vmcnt(" #N ")\n\t" P_TEST(96, I) "s_cbranch_vccnz 57" #I "f\n\t56" #I ":\n\t" P_MM("128+8*" #I, 96, I, G_A0, G_A1)
#define P_STUB(A, B, I, R) #A #I ":\n\ts_call_b64 s[78:79], " #R "f\n\ts_branch " #B #I "b\n\t"
#define P_STUBS(A, B, R) P_STUB(A, B, 0, R) P_STUB(A, B, 1, R) P_STUB(A, B, 2, R) P_STUB(A, B, 3, R) P_STUB(A, B, 4, R) P_STUB(A, B, 5, R) P_STUB(A, B, 6, R) P_STUB(A, B, 7, R)
#define P_RETRY(F, I, SO) P_TEST(F, I) "s_cbranch_vccz 1f\n\ts_mov_b32 s52, 1\n\t" P_LOAD(F, I, SO) "1:\n\t"
#define P_SLOW(R, F, S0, S1)                                                                          \
  #R ":\n\ts_waitcnt vmcnt(0)\n\ts_mov_b32 s52, 0\n\t"                                             \
  P_RETRY(F, 0, S0) P_RETRY(F, 1, S0) P_RETRY(F, 2, S0) P_RETRY(F, 3, S0) P_RETRY(F, 4, S1) P_RETRY(F, 5, S1) P_RETRY(F, 6, S1) P_RETRY(F, 7, S1) \
  "s_sub_u32 s53, s53, 1\n\ts_cselect_b32 s52, 0, s52\n\ts_cselect_b32 s53, 0, s53\n\t" \
  "s_cmp_lg_u32 s52, 0\n\ts_cbranch_scc1 " #R "b\n\ts_waitcnt vmcnt(0)\n\ts_setpc_b64 s[78:79]\n\t"
#define P_LOAD8(F, S0, S1) P_LOAD(F, 0, S0) P_LOAD(F, 1, S0) P_LOAD(F, 2, S0) P_LOAD(F, 3, S0) P_LOAD(F, 4, S1) P_LOAD(F, 5, S1) P_LOAD(F, 6, S1) P_LOAD(F, 7, S1)
#define P_LOADA P_LOAD8(96, 54, 55)
#define OUT_POL " sc1"
#define HALF_SWITCH 0
#define A_AT 7
#define P_AT(I) ".if " STR(A_AT) " == " #I "\n\t" P_LOADA ".endif\n\t"
#define STR_(x) #x
#define STR(x) STR_(x)
typedef unsigned u32x6 __attribute__((ext_vector_type(6)));
struct Src { u32x6 wv; rsrc_t ra, rb, rx; };
#define M_PAR0                                                                                        \
  "s_add_u32 s34, s26, 0x500000\n\ts_lshl_b32 s33, s56, 15\n\ts_sub_u32 s33, s26, s33\n\ts_lshr_b32 s33, s33, 3\n\ts_add_u32 s33, s33, 0x80000\n\t" \
  "s_lshl_b32 s29, s25, 13\n\ts_add_u32 s29, s29, 0x100000\n\ts_lshl_b32 s30, s25, 8\n\ts_add_u32 s30, s30, 0x10000\n\t" \
  "s_lshr_b32 s28, s25, 4\n\ts_lshl_b32 s28, s28, 12\n\ts_add_u32 s28, s28, 0x70000\n\t"
#define M_PAR1                                                                                        \
  "s_lshl_b32 s31, s24, 1\n\ts_add_u32 s31, s31, 0x10000\n\ts_lshl_b32 s39, s24, 7\n\ts_add_u32 s39, s39, 0x900000\n\t" \
  "s_lshl_b32 s32, s62, 5\n\ts_add_u32 s32, s32, s24\n\ts_add_u32 s24, s32, 0x700000\n\ts_add_u32 s25, s32, 0x708000\n\t" \
  "s_add_u32 s26, s32, 0x500000\n\ts_add_u32 s27, s32, 0x508000\n\ts_lshr_b32 s32, s32, 3\n\ts_add_u32 s32, s32, 0x80000\n\t"
#define M_PAR2                                                                                        \
  "s_lshl_b32 s38, s56, 15\n\ts_add_u32 s38, s38, s24\n\ts_lshl_b32 s35, s58, 1\n\ts_add_u32 s35, s35, 0x20000\n\t" \
  "s_add_u32 s36, s58, 0x26200\n\ts_lshl_b32 s88, s56, 11\n\ts_add_u32 s88, s88, s35\n\ts_lshl_b32 s89, s56, 10\n\t" \
  "s_add_u32 s89, s89, s58\n\ts_add_u32 s89, s89, 0x26000\n\ts_lshl_b32 s90, s37, 11\n\t"              \
  "s_mov_b32 s76, s64\n\ts_lshl_b32 s84, s58, 4\n\ts_lshl_b32 s85, s56, 14\n\ts_add_u32 s84, s84, s85\n\ts_mov_b32 s85, 0\n\t"
static_assert(WS_OB == 0x500000 && WS_OA == 0x700000 && WS_FOB == 0x80000 && WS_KP == 0x100000 && WS_KR == 0x10000 && WS_FKP == 0x70000 &&
              WS_KV == 0x900000 && L_PQ == 0x20000 && L_VB == 0x26000 && L_X == 0 && L_WO == 0 && L_RED == 0 && FL == 128, "literals above");
DI void first_chunks(const Src& sr, unsigned xv0, unsigned xv1, unsigned ml, unsigned mx, unsigned rot, unsigned rotx) {
  asm volatile(D_X(0) G_ALL(D_S0(0)) D_X(1) G_ALL(D_S0(1))
               :: "{v[10:15]}"(sr.wv), "{v144}"(xv0), "{v145}"(xv1), "{s[40:43]}"(sr.ra), "{s[48:51]}"(sr.rx), "{s57}"(ml), "{s58}"(mx),
                  "{s59}"(rot), "{s60}"(rotx)
               : "memory", "scc", "s52");
}
struct Prog { unsigned wo0, red, koff, voff0, voff1, wov; rsrc_t out, ws, wo; };
DI void program(Src sr, u32x4 xa, unsigned wa, u32x16 vp, u32x16 sp, u32x4 base, unsigned w, unsigned outoff, const Prog& pc, unsigned long long rt0 ST_PAR) {
  asm volatile(
      "s_getpc_b64 s[92:93]\n\t"
      "99:\n\t"
      "s_add_u32 s92, s92, 400f-99b\n\ts_addc_u32 s93, s93, 0\n\t"
      "s_mov_b32 s53, 0x40000\n\t"
      "s_cmp_eq_u32 s56, 0\n\t"
      "s_cbranch_scc1 100f\n\t"
      G_BAR(6) D_X(2) G_ALL(D_S0(2)) G_BAR(6) D_X(3) G_ALL(D_S0(3)) G_BAR(6) D_X(4) G_ALL(D_S0(4)) M_PAR0
      G_BAR(6) D_X(5) G_ALL(D_S0(5)) M_PAR1 G_BAR(6) D_X(6) G_ALL(D_S0(6)) M_PAR2 G_BAR(6) D_X(7) G_ALL(D_S0(7))
      G_BAR(6) G_ALL(D_S1(8)) G_ALL(D_S1(9)) G_BAR(8) G_ALL(D_S1(10))
      "s_branch 200f\n\t"
      "100:\n\t"
      G_EPOCH
      G_Z("16,18,20,22,24,26,28,30,32,34,36,38,40,42,44,46")
      "s_mov_b32 s52, 0\n\t"
      G_LBAR G_READ(48, 64, 80, 0)
      "s_branch 102f\n\t"
      "101:\n\t"
      G_BAR(0) G_EVEN(0, "", "", "", "")
      "102:\n\t"
      G_LBAR G_ODD(1, "", "", "", "") G_LBAR G_EVEN(2, "", "", "", "") G_LBAR G_ODD(3, "", "", "", "")
      ".irp r,4,5,6,7\n\tv_xor_b32 v\\r, 0x8000, v\\r\n\t.endr\n\t"
      "s_xor_b32 s52, s52, 1\n\t"
      "s_cbranch_scc1 101b\n\t"
      ST(1)
      G_LBAR
      G_MMA2(96, 112, 128, 0) G_MMA2(96, 112, 128, 1) G_MMA2(96, 112, 128, 2) G_MMA2(96, 112, 128, 3)
      G_EPOCH_USE G_Z("88,90,92,94")
      "s_cmp_lg_u32 s61, 0\n\t"
      "s_cbranch_scc1 110f\n\t"
      "s_nop 0\n\t"
      ".irp r,16,17,18,19,20,21,22,23,24,25,26,27,28,29,30,31\n\tv_mul_f32 v\\r, 0x3eb8aa3b, v\\r\n\t.endr\n\t"
      G_LBAR
      ".irp r,32,33,34,35,36,37,38,39,40,41,42,43,44,45,46,47\n\tv_mul_f32 v\\r, 0x3eb8aa3b, v\\r\n\t.endr\n\t"
      A_PACK
      G_ST4(48, 149, "0 offen") G_ST4(52, 149, "0 offen offset:2048")
      ".irp j,0,1,2,3\n\t"
      "v_dot2c_f32_bf16 v92, v[48+\\j], v[48+\\j]\n\tv_dot2c_f32_bf16 v93, v[52+\\j], v[52+\\j]\n\t"
      "v_dot2c_f32_bf16 v94, v[56+\\j], v[56+\\j]\n\tv_dot2c_f32_bf16 v95, v[60+\\j], v[60+\\j]\n\t"
      ".endr\n\t"
      "s_mov_b32 s54, 131072\n\t"
      G_LBAR
      G_ST4(56, 149, "s54 offen") G_ST4(60, 149, "s54 offen offset:2048")
      "v_permlane32_swap_b32 v92, v94\n\tv_permlane32_swap_b32 v93, v95\n\t"
      "v_add_f32 v96, v92, v94\n\tv_add_f32 v97, v93, v95\n\t"
      A_GREAD(0)
      "v_max_f32 v96, v96, v97\n\t"
      A_DPPMAX("quad_perm:[1,0,3,2]") A_DPPMAX("quad_perm:[2,3,0,1]") A_DPPMAX("row_half_mirror") A_DPPMAX("row_mirror")
      "v_mov_b32 v97, v96\n\tv_mov_b32 v100, -1\n\ts_nop 0\n\tv_permlane16_swap_b32 v96, v97\n\tv_max_f32 v96, v96, v97\n\t"
      "buffer_store_dword v96, v150, s[72:75], 0 offen sc1\n\t"
      "buffer_store_dword v100, v151, s[72:75], 0 offen sc1\n\t"
      "s_branch 111f\n\t"
      "110:\n\t"
      ".irp j,0,1,2,3,4,5,6,7\n\tv_bfe_i32 v[96+\\j], v157, \\j, 1\n\t.endr\n\t"
      A_PACK
      ".irp j,0,1,2,3,4,5,6,7\n\tv_and_b32 v[96+\\j], v156, v[96+\\j]\n\t.endr\n\t"
      G_LBAR
      "s_setprio 2\n\t"
      "v_mfma_f32_32x32x16_bf16 v[16:31], v[48:51], v[96:99], 0\n\t"
      "v_mfma_f32_32x32x16_bf16 v[32:47], v[52:55], v[96:99], 0\n\t"
      "v_mfma_f32_32x32x16_bf16 v[16:31], v[56:59], v[100:103], v[16:31]\n\t"
      "v_mfma_f32_32x32x16_bf16 v[32:47], v[60:63], v[100:103], v[32:47]\n\t"
      "s_setprio 0\n\t"
      ".irp r,104,105,106,107\n\tv_mov_b32 v\\r, 0x3f803f80\n\t.endr\n\t"
      G_ST4(104, 150, "0 offen")
      "s_nop 4\n\t"
      F_PACK(16, 0, 48)
      G_ST4(48, 149, "0 offen") G_ST4(52, 149, "0 offen offset:512")
      G_LBAR
      F_PACK(32, 0, 56)
      G_ST4(56, 149, "0 offen offset:2048") G_ST4(60, 149, "0 offen offset:2560")
      A_GREAD(0)
      "111:\n\t"
      G_LBAR A_GDOT A_GREAD(2) M_PAR0
      G_LBAR A_GDOT A_GREAD(4) M_PAR1
      G_LBAR A_GDOT A_GREAD(6) M_PAR2
      G_LBAR A_GDOT
      "v_mov_b32 v94, s86\n\tv_mov_b32 v95, s87\n\tv_and_b32 v92, 0xfffff0ff, v154\n\t"
      "v_add_f32 v88, v88, v90\n\tv_add_f32 v89, v89, v91\n\t"
      "ds_write2st64_b32 v154, v88, v89 offset1:1\n\t"
      G_BAR(0)
      "ds_read2st64_b32 v[64:65], v92 offset1:1\n\tds_read2st64_b32 v[66:67], v92 offset0:2 offset1:3\n\t"
      "ds_read2st64_b32 v[68:69], v92 offset0:4 offset1:5\n\tds_read2st64_b32 v[70:71], v92 offset0:6 offset1:7\n\t"
      "buffer_store_dwordx2 v[94:95], v155, s[72:75], 0 offen sc1\n\t"
      "s_waitcnt lgkmcnt(0)\n\t"
      "v_add_f32 v64, v64, v66\n\tv_add_f32 v65, v65, v67\n\tv_add_f32 v68, v68, v70\n\tv_add_f32 v69, v69, v71\n\t"
      "v_add_f32 v64, v64, v68\n\tv_add_f32 v65, v65, v69\n\t"
      "s_nop 1\n\tv_permlane32_swap_b32 v64, v65\n\t"
      "v_add_f32 v64, v64, v65\n\t" SIGMOID(64, 253)
      "s_nop 0\n\tv_mov_b32 v252, v253\n\t"
      ST(2)
      G_LBAR "s_setprio 1\n\t" "v_permlane32_swap_b32 v253, v252\n\t" G_READ(48, 64, 80, 0) G_ALL(D_S2(19))
      G_LBAR G_APPLY(G_FIRST, D_S2(20))
      G_LBAR G_APPLY(G_EVEN, 2, D_S2(21)) G_BAR(8) G_APPLY(G_ODD, 3, D_S2(22))
      G_BAR(8) G_APPLY(G_EVEN, 4, D_S2(23))
      G_BAR(8) G_ODD(5, "buffer_load_dword v9, v158, s[72:75], 0 offen offset:8 sc1\n\t", G_KV1(200, 216, 62), G_KV1(204, 224, 63), G_KV1(208, 232, 64))
      G_BAR(14) G_EVEN(6, G_Z("160,162,164,166,168"), G_Z("170,172,174,176,178"), G_Z("180,182,184,186"), G_Z("188,190,244,246"))
      G_BAR(9) G_ODD(7, DPPMAX(9, "quad_perm:[1,0,3,2]"), DPPMAX(9, "quad_perm:[2,3,0,1]"), DPPMAX(9, "row_half_mirror"), "s_nop 0\n\t" DPPMAX(9, "row_mirror"))
      "s_waitcnt lgkmcnt(0)\n\t"
      G_MMA2(96, 112, 128, 0) G_MMA2(96, 112, 128, 1) G_MMA2(96, 112, 128, 2) G_MMA2(96, 112, 128, 3)
      "v_add_u32 v1, s65, v159\n\t"
      "s_setprio 2\n\t"
      G_QPRE G_Z("152,154,156,158") "v_mov_b32 v8, v9\n\t"
      A_PACK
      ".irp i,0,1,2,3\n\tds_write_b128 v1, v[48+4*\\i:51+4*\\i] offset:1024*\\i\n\t.endr\n\t"
      G_C0 G_C1
      ST(3)
      G_LBAR
      G_QRD
      "s_branch 300f\n\t"
      ATTN_ROUTINE
      "200:\n\t"
      ST(1)
      G_BAR(8) "s_setprio 1\n\t" G_EPOCH G_READ(48, 64, 80, 0) G_ALL(D_S1(11))
      G_BAR(9) G_APPLY(G_FIRST, D_S1(12))
      G_BAR(9) G_APPLY(G_EVEN, 2, D_S1(13)) G_BAR(8) G_APPLY(G_ODD, 3, D_S1(14))
      G_BAR(8) G_APPLY(G_EVEN, 4, D_S1(15)) G_BAR(8) G_APPLY(G_ODD, 5, D_S2(16))
      G_BAR(8) G_APPLY(G_EVEN, 6, D_S2(17)) G_BAR(8) G_APPLY(G_ODD, 7, D_S2(18))
      ST(2)
      G_BAR(8)
      "s_setprio 0\n\t"
      "ds_read_b64_tr_b16 v[64:65], v146\n\tds_read_b64_tr_b16 v[66:67], v146 offset:256\n\t"
      "ds_read_b64_tr_b16 v[68:69], v146 offset:512\n\tds_read_b64_tr_b16 v[70:71], v146 offset:768\n\t"
      "ds_read2st64_b32 v[72:73], v154 offset1:2\n\tds_read2st64_b32 v[74:75], v154 offset0:4 offset1:6\n\t"
      G_MMA2(96, 112, 128, 0) G_EPOCH_USE G_MMA2(96, 112, 128, 1)
      "s_waitcnt lgkmcnt(0)\n\t"
      "v_add_f32 v72, v72, v74\n\tv_add_f32 v73, v73, v75\n\t"
      ".irp r,64,65,66,67\n\tv_xor_b32 v\\r, v147, v\\r\n\t.endr\n\t"
      "v_add_f32 v72, v72, v73\n\t"
      G_MMA2(96, 112, 128, 2)
      "v_mov_b32 v73, v72\n\t"
      ".irp r,68,69,70,71\n\tv_xor_b32 v\\r, v147, v\\r\n\t.endr\n\t"
      "v_permlane32_swap_b32 v72, v73\n\t"
      G_MMA2(96, 112, 128, 3)
      "v_add_f32 v72, v72, v73\n\t" SIGMOID(72, 253)
      "s_nop 5\n\t"
      A_PACK
      G_BAR(4)
      "s_setprio 2\n\t"
      "v_mfma_f32_32x32x16_bf16 v[16:31], v[64:67], v[48:51], 0\n\t"
      "v_mfma_f32_32x32x16_bf16 v[32:47], v[64:67], v[52:55], 0\n\t"
      ".irp j,0,1,2,3\n\tv_bfe_i32 v[128+\\j], v157, \\j, 1\n\t.endr\n\t"
      "v_mfma_f32_32x32x16_bf16 v[96:111], v[68:71], v[56:59], 0\n\t"
      ".irp j,4,5,6,7\n\tv_bfe_i32 v[128+\\j], v157, \\j, 1\n\t.endr\n\t"
      "v_mfma_f32_32x32x16_bf16 v[112:127], v[68:71], v[60:63], 0\n\t"
      "s_setprio 0\n\t"
      ".irp j,0,1,2,3,4,5,6,7\n\tv_and_b32 v[128+\\j], v156, v[128+\\j]\n\t.endr\n\t"
      F_JOBS(F_MAX1) "s_nop 0\n\t" F_JOBS(F_SWAP) F_JOBS(F_MAX2) F_JOBS(F_NEG) F_JOBS(F_DUP) F_JOBS(F_SWAP)
      G_BAR(0)
      F_EXP(16, 80) F_FMA(32, 84) F_EX(32, "0,8,1,9")
      G_LBAR
      G_POLL(140) F_EX(32, "2,10,3,11,4,12,5,13,6,14,7,15") F_EXP(96, 88)
      G_LBAR
      F_EXP(112, 92) F_SUM1(16, 80, 48) F_SUM1(32, 84, 56) G_POLLWAIT(22, 140)
      G_LBAR
      F_SUM1(96, 88, 64) DPPMAX(9, "quad_perm:[1,0,3,2]") F_SUM1(112, 92, 72) DPPMAX(9, "quad_perm:[2,3,0,1]") F_JOBS(F_SUM2)
      DPPMAX(9, "row_half_mirror") F_JOBS(F_SUM3) DPPMAX(9, "row_mirror") F_JOBS(F_SWAP) F_JOBS(F_ADD) F_JOBS(F_RCP) F_JOBS(F_DUP) F_JOBS(F_SWAP)
      G_LBAR
      G_KV1(200, 216, 62) F_SCALE(16, 80, 48) G_KV1(204, 224, 63) F_SCALE(32, 84, 56) G_KV1(208, 232, 64) F_SCALE(96, 88, 64)
      G_LBAR
      F_SCALE(112, 92, 72)
      "s_cmp_lg_u32 s61, 0\n\t"
      "s_cbranch_scc1 230f\n\t"
      "v_add_u32 v136, s67, v159\n\t"
      ".irp i,0,1,2,3,4,5,6,7\n\tds_write_b128 v136, v[48+4*\\i:51+4*\\i] offset:1024*\\i\n\t.endr\n\t"
      G_ZO G_C0
      "s_branch 231f\n\t"
      "230:\n\t"
      F_JOBS(F_TR0) F_JOBS(F_TR1) G_ZO F_JOBS(F_PACK)
      ".irp r,96,97,98,99,100,101,102,103,104,105,106,107,108,109,110,111\n\tv_mov_b32 v\\r, v152\n\t.endr\n\t"
      "v_add_u32 v136, s69, v159\n\tv_mov_b32 v2, s86\n\tv_mov_b32 v3, s87\n\t"
      "231:\n\t"
      G_QPRE G_Z("152,154,156,158") "v_mov_b32 v8, v9\n\t" G_C1
      ST(3)
      G_LBAR
      G_QRD
      "s_cmp_eq_u32 s61, 0\n\t"
      "s_cbranch_scc1 151f\n\t"
      "s_setprio 3\n\t"
      ".irp i,0,1,2,3\n\tds_read_b128 v[80+4*\\i:83+4*\\i], v136 offset:1024*\\i\n\t.endr\n\t"
      "s_waitcnt lgkmcnt(2)\n\t"
      "v_mfma_f32_32x32x16_bf16 v[16:31], v[80:83], v[128:131], v[96:111]\n\t"
      "v_mfma_f32_32x32x16_bf16 v[32:47], v[84:87], v[128:131], v[96:111]\n\t"
      "s_waitcnt lgkmcnt(0)\n\t"
      "v_mfma_f32_32x32x16_bf16 v[112:127], v[88:91], v[128:131], v[96:111]\n\t"
      "v_mfma_f32_32x32x16_bf16 v[96:111], v[92:95], v[128:131], v[96:111]\n\t"
      "s_mov_b32 s52, 32768\n\ts_mov_b32 s54, 65536\n\ts_mov_b32 s55, 98304\n\t"
      "s_nop 5\n\t"
      F_PACK(16, 0, 80) F_PACK(32, 0, 88)
      "v_mfma_f32_32x32x16_bf16 v[16:31], v[48:51], v[80:83], 0\n\t"
      F_PACK(112, 0, 128)
      "v_mfma_f32_32x32x16_bf16 v[16:31], v[52:55], v[84:87], v[16:31]\n\t"
      F_PACK(96, 0, 136)
      "v_mfma_f32_32x32x16_bf16 v[16:31], v[56:59], v[88:91], v[16:31]\n\t"
      "v_mfma_f32_32x32x16_bf16 v[32:47], v[64:67], v[128:131], 0\n\t"
      "v_mfma_f32_32x32x16_bf16 v[16:31], v[60:63], v[92:95], v[16:31]\n\t"
      "v_mfma_f32_32x32x16_bf16 v[32:47], v[68:71], v[132:135], v[32:47]\n\t"
      "v_mfma_f32_32x32x16_bf16 v[32:47], v[72:75], v[136:139], v[32:47]\n\t"
      "v_mfma_f32_32x32x16_bf16 v[32:47], v[76:79], v[140:143], v[32:47]\n\t"
      "s_nop 8\n\t"
      F_PACK(16, 0, 80)
      G_ST4(80, 149, "0 offen") G_ST4(84, 149, "s52 offen")
      "s_nop 0\n\t"
      F_PACK(32, 0, 88)
      G_ST4(88, 149, "s54 offen") G_ST4(92, 149, "s55 offen")
      "s_setprio 1\n\t"
      "s_waitcnt vmcnt(0)\n\t"
      "buffer_store_dwordx2 v[2:3], v150, s[72:75], 0 offen sc1\n\t"
      G_C0 G_C1
      "s_branch 301f\n\t"
      "151:\n\t"
      "s_setprio 1\n\t"
      "300:\n\t"
      "s_waitcnt vmcnt(0) lgkmcnt(0)\n\t"
      "301:\n\t"
      "v_mov_b64 v[16:17], 0\n\t"
      "v_permlane16_swap_b32 v9, v8\n\t"
      QK(128, 143, 200, 203, 192, 195, 96, 111)
      ".irp j,0,1,2,3\n\tv_dot2c_f32_bf16 v16, v[192+\\j], v[192+\\j]\n\tv_dot2c_f32_bf16 v17, v[196+\\j], v[196+\\j]\n\t.endr\n\t"
      "v_max_f32 v9, v9, v8\n\t"
      "s_nop 1\n\t"
      "v_permlane32_swap_b32 v16, v17\n\t"
      "v_add_f32 v16, v16, v17\n\t"
      "v_mul_f32 v16, v16, v9\n\t"
      "v_cmp_nge_f32 vcc, 0x45100000, v16\n\t"
      "s_cbranch_vccnz 310f\n\t"
      "311:\n\t"
      ST(4)
      "s_cmp_eq_u32 s56, 0\n\t"
      "s_cbranch_scc1 320f\n\t"
      RUN("5")
      "s_branch 330f\n\t"
      "310:\n\t"
      "v_mov_b32 v96, 0xff7fffff\n\tv_mov_b32 v112, 0xff7fffff\n\t"
      "s_mov_b32 s52, s39\n\ts_mov_b32 s54, 63\n\t"
      "312:\n\t"
      "buffer_load_dwordx4 v[16:19], v4, s[72:75], s52 offen sc1\n\t"
      "s_add_u32 s52, s52, 0x800\n\t"
      "s_waitcnt vmcnt(0)\n\t"
      "v_mfma_f32_32x32x16_bf16 v[32:47], v[16:19], v[192:195], 0\n\t"
      "v_mfma_f32_32x32x16_bf16 v[48:63], v[16:19], v[196:199], 0\n\t"
      "s_nop 11\n\t"
      ".irp i,0,2,4,6,8,10,12,14\n\tv_max3_f32 v96, v96, v[32+\\i], v[33+\\i]\n\tv_max3_f32 v112, v112, v[48+\\i], v[49+\\i]\n\t.endr\n\t"
      "s_sub_u32 s54, s54, 1\n\t"
      "s_cbranch_scc0 312b\n\t"
      "v_mov_b32 v97, v96\n\tv_mov_b32 v113, v112\n\ts_nop 1\n\t"
      "v_permlane32_swap_b32 v96, v97\n\tv_permlane32_swap_b32 v112, v113\n\t"
      "v_max_f32 v96, v96, v97\n\tv_max_f32 v112, v112, v113\n\t"
      "v_xor_b32 v96, 0x80000000, v96\n\tv_xor_b32 v112, 0x80000000, v112\n\t"
      ".irp r,1,2,3,4,5,6,7,8,9,10,11,12,13,14,15\n\tv_mov_b32 v[96+\\r], v96\n\tv_mov_b32 v[112+\\r], v112\n\t.endr\n\t"
      "s_nop 1\n\t"
      QK(128, 143, 200, 203, 192, 195, 96, 111)
      "s_nop 11\n\t"
      "s_branch 311b\n\t"
      "320:\n\t"
      RUN("0")
      ST(5)
      "v_mov_b64 v[16:17], -1\n\tv_mov_b64 v[18:19], -1\n\t"
      G_ST4(16, 4, "s24 offen")
      RUN("0")
      "s_cmp_lg_u32 s37, 0\n\t"
      "s_cbranch_scc1 322f\n\t"
      "v_and_b32 v20, 0x1f0, v4\n\tv_lshlrev_b32 v20, 3, v20\n\ts_add_u32 s52, s29, 4096\n\t"
      "3210:\n\tbuffer_load_dwordx2 v[22:23], v20, s[72:75], s28 offen sc1\n\ts_waitcnt vmcnt(0)\n\t"
      "v_cmp_ne_u64 vcc, s[86:87], v[22:23]\n\t" G_SPIN(321, "s_sleep 4\n\t")
      P_LOAD8(32, 29, 52)
      "322:\n\t"
      RUN("0")
      ST(6)
      G_ST4(16, 4, "s25 offen")
      "s_cmp_lg_u32 s37, 0\n\t"
      "s_cbranch_scc1 323f\n\t"
      G_Z("24,26,28,30")
      "v_mov_b32 v20, 0x3f80\n\tv_mov_b32 v21, 0x3f800000\n\t"
      ".irp i,0,1,2,3,4,5,6,7\n\t.irp j,0,1,2,3\n\t"
      "v_dot2c_f32_bf16 v[24+2*\\j], v[32+4*\\i+\\j], v20\n\tv_dot2c_f32_bf16 v[25+2*\\j], v[32+4*\\i+\\j], v21\n\t"
      ".endr\n\t.endr\n\t"
      "v_and_b32 v20, 0xf0, v4\n\tv_lshrrev_b32 v21, 5, v4\n\tv_lshrrev_b32 v22, 7, v4\n\t"
      ".irp e,0,1,2,3\n\tv_permlane16_swap_b32 v[24+\\e], v[28+\\e]\n\t.endr\n\t"
      ".irp e,0,1,2,3\n\tv_add_f32 v[24+\\e], v[24+\\e], v[28+\\e]\n\t.endr\n\t"
      "v_and_or_b32 v20, v21, 8, v20\n\tv_and_or_b32 v20, v22, 4, v20\n\t"
      "v_permlane32_swap_b32 v24, v26\n\tv_permlane32_swap_b32 v25, v27\n\t"
      "v_add_f32 v24, v24, v26\n\tv_add_f32 v25, v25, v27\n\t"
      "v_cvt_pk_bf16_f32 v24, v24, v25\n\tv_min_u32 v24, -2, v24\n\t"
      "buffer_store_dword v24, v20, s[72:75], s30 offen sc1\n\t"
      "323:\n\t"
      RUN("0")
      ST(7)
      "v_add_u32 v20, s35, v4\n\t"
      P_LOAD(16, 0, 31) P_LOAD(20, 1, 31)
      ".irp i,0,1,2,3\n\tds_read_b128 v[32+4*\\i:35+4*\\i], v20 offset:1024*\\i\n\t.endr\n\t"
      RUN("0")
      ST(8)
      "3240:\n\t"
      "v_max3_u32 v80, v16, v17, v18\n\tv_max3_u32 v81, v19, v24, v25\n\tv_max3_u32 v80, v80, v26, v27\n\tv_max_u32 v80, v80, v81\n\t"
      "v_cmp_eq_u32 vcc, -1, v80\n\t"
      G_SPIN(324, "s_sleep 4\n\t" P_LOAD(16, 0, 31) P_LOAD(20, 1, 31) "s_waitcnt vmcnt(0)\n\t")
      "s_waitcnt lgkmcnt(0)\n\t"
      "v_mfma_f32_32x32x16_bf16 v[48:63], v[16:19], v[32:35], 0\n\t"
      "v_mfma_f32_32x32x16_bf16 v[64:79], v[16:19], v[40:43], 0\n\t"
      "v_mfma_f32_32x32x16_bf16 v[48:63], v[24:27], v[36:39], v[48:63]\n\t"
      "v_mfma_f32_32x32x16_bf16 v[64:79], v[24:27], v[44:47], v[64:79]\n\t"
      "v_sub_f32 v80, 1.0, v253\n\tv_sub_f32 v81, 1.0, v252\n\t"
      "v_mov_b32 v82, s86\n\tv_mov_b32 v83, s87\n\tv_mov_b32 v84, s36\n\t"
      "ds_write_b64 v84, v[82:83]\n\t"
      "s_nop 4\n\t"
      "v_add_f32 v56, 0x358637bd, v56\n\tv_rcp_f32 v56, v56\n\t"
      "v_add_f32 v72, 0x358637bd, v72\n\tv_rcp_f32 v72, v72\n\t"
      "v_mul_f32 v80, v80, v56\n\t"
      ".irp r,48,49,50,51,52,53,54,55\n\tv_mul_f32 v\\r, v80, v\\r\n\t.endr\n\t"
      "v_mul_f32 v81, v81, v72\n\t"
      ".irp i,0,1,2,3\n\tv_cvt_pk_bf16_f32 v[16+\\i], v[48+2*\\i], v[49+2*\\i]\n\t.endr\n\t"
      G_ST4(16, 4, "s26 offen")
      ".irp r,64,65,66,67,68,69,70,71\n\tv_mul_f32 v\\r, v81, v\\r\n\t.endr\n\t"
      ".irp i,0,1,2,3\n\tv_cvt_pk_bf16_f32 v[20+\\i], v[64+2*\\i], v[65+2*\\i]\n\t.endr\n\t"
      G_ST4(20, 4, "s27 offen")
      RUNV("0", STR(HALF_SWITCH))
      "v_cmp_eq_u32 vcc, 0, v4\n\tv_mov_b32 v84, s32\n\tv_bfrev_b32 v80, 15\n\t"
      "s_setprio 0\n\t"
      "v_cndmask_b32 v84, v80, v84, vcc\n\t"
      "buffer_store_dwordx2 v[82:83], v84, s[72:75], 0 offen sc1\n\t"
      "330:\n\t"
      ST(9)
      "v_and_b32 v20, 0x70, v4\n\tv_lshlrev_b32 v20, 3, v20\n\t"
      "buffer_load_dwordx2 v[22:23], v20, s[72:75], s33 offen sc1\n\t"
      RUN("0")
      ST(10)
      "v_mov_b32 v84, s36\n\ts_add_u32 s52, s34, 4096\n\t"
      "3310:\n\tds_read_b64 v[24:25], v84\n\ts_waitcnt lgkmcnt(0)\n\tv_cmp_ne_u64 vcc, s[86:87], v[24:25]\n\t" G_SPIN(331, "s_sleep 2\n\t")
      "3320:\n\tv_cmp_ne_u64 vcc, s[86:87], v[22:23]\n\t"
      G_SPIN(332, "s_sleep 4\n\tbuffer_load_dwordx2 v[22:23], v20, s[72:75], s33 offen sc1\n\ts_waitcnt vmcnt(0)\n\t")
      P_LOAD8(48, 34, 52)
      "v_add_u32 v6, s88, v4\n\tv_lshrrev_b32 v7, 2, v4\n\tv_add_u32 v7, s89, v7\n\t"
      "v_xor_b32 v8, 0x800, v6\n\tv_xor_b32 v9, 0x400, v7\n\tv_xor_b32 v1, 64, v0\n\t"
      "s_add_u32 s54, s34, 0x200000\n\ts_add_u32 s55, s34, 0x201000\n\t"
      RUN("0")
      ST(12)
      PV(176, 191, 244, 247, 152, 159)
      "ds_write_b128 v6, v[160:163]\n\tds_write_b128 v6, v[164:167] offset:1024\n\tds_write_b32 v7, v168\n\t"
      G_LBAR
      "ds_read_b32 v24, v9\n\tds_read_b128 v[16:19], v8\n\tds_read_b128 v[20:23], v8 offset:1024\n\t"
      "s_nop 3\n\t"
      "s_waitcnt lgkmcnt(2)\n\t"
      "v_add_f32 v24, v184, v24\n\tv_rcp_f32 v24, v24\n\t"
      "s_waitcnt lgkmcnt(0)\n\t"
      ".irp i,0,1,2,3,4,5,6,7\n\tv_add_f32 v[16+\\i], v[176+\\i], v[16+\\i]\n\t.endr\n\t"
      "v_mul_f32 v24, v253, v24\n\t"
      ".irp i,0,1,2,3,4,5,6,7\n\tv_mul_f32 v[16+\\i], v24, v[16+\\i]\n\t.endr\n\t"
      ".irp i,0,1,2,3\n\tv_cvt_pk_bf16_f32 v[84+\\i], v[16+2*\\i], v[17+2*\\i]\n\t.endr\n\t"
      ".irp i,0,1,2,3\n\tv_min_u32 v[84+\\i], -2, v[84+\\i]\n\t.endr\n\t"
      G_ST4(84, 4, "s38 offen")
      ST(13)
      P_RB(0) P_RB(1)
      P_USEB(0, 4, "0", "0", P_RB(2) P_RA(0) P_AT(0)) P_USEB(1, 8, G_A0, G_A1, P_RB(3) P_RA(1) P_AT(1))
      P_USEB(2, 12, G_A0, G_A1, P_RB(4) P_RA(2) P_AT(2)) P_USEB(3, 12, G_A0, G_A1, P_RB(5) P_RA(3) P_AT(3))
      P_USEB(4, 12, G_A0, G_A1, P_RB(6) P_RA(4) P_AT(4)) P_USEB(5, 12, G_A0, G_A1, P_RB(7) P_RA(5) P_AT(5))
      P_USEB(6, 12, G_A0, G_A1, P_RA(6) P_AT(6)) P_USEB(7, 8, G_A0, G_A1, P_RA(7) P_AT(7))
      "v_xor_b32 v6, 32, v248\n\tv_xor_b32 v7, 64, v248\n\tv_xor_b32 v8, 96, v248\n\t"
      "v_lshrrev_b32 v9, 7, v4\n\tv_bfe_u32 v10, v4, 3, 4\n\tv_xor_b32 v10, v10, v9\n\tv_lshlrev_b32 v10, 4, v10\n\t"
      "v_lshl_add_u32 v10, v9, 8, v10\n\tv_add_u32 v10, s90, v10\n\tv_xor_b32 v11, 16, v10\n\t"
      "v_and_b32 v12, 0x70, v4\n\tv_lshl_or_b32 v12, v9, 10, v12\n\tv_add_u32 v12, s91, v12\n\t"
      G_LBAR
      P_USEA(0, 7) P_USEA(1, 6) P_USEA(2, 5) P_USEA(3, 4) P_USEA(4, 3) P_USEA(5, 2) P_USEA(6, 1) P_USEA(7, 0)
      "s_nop 10\n\t"
      "ds_write_b128 v248, v[16:19]\n\tds_write_b128 v6, v[20:23]\n\tds_write_b128 v7, v[24:27]\n\tds_write_b128 v8, v[28:31]\n\t"
      "ds_write_b128 v248, v[32:35] offset:128\n\tds_write_b128 v6, v[36:39] offset:128\n\t"
      "ds_write_b128 v7, v[40:43] offset:128\n\tds_write_b128 v8, v[44:47] offset:128\n\t"
      ST(15)
      G_LBAR
      ".irp k,0,1,2,3\n\tds_read_b128 v[16+8*\\k:19+8*\\k], v10 offset:16384*\\k\n\tds_read_b128 v[20+8*\\k:23+8*\\k], v11 offset:16384*\\k\n\t.endr\n\t"
      "s_waitcnt lgkmcnt(4)\n\t"
      ".irp i,0,1,2,3,4,5,6,7\n\tv_add_f32 v[16+\\i], v[16+\\i], v[24+\\i]\n\t.endr\n\t"
      "s_waitcnt lgkmcnt(0)\n\t"
      ".irp i,0,1,2,3,4,5,6,7\n\tv_add_f32 v[32+\\i], v[32+\\i], v[40+\\i]\n\t.endr\n\t"
      ".irp i,0,1,2,3,4,5,6,7\n\tv_add_f32 v[16+\\i], v[16+\\i], v[32+\\i]\n\t.endr\n\t"
      ".irp i,0,1,2,3\n\tv_cvt_pk_bf16_f32 v[16+\\i], v[16+2*\\i], v[17+2*\\i]\n\t.endr\n\t"
      "buffer_store_dwordx4 v[16:19], v12, s[20:23], 0 offen" OUT_POL "\n\t"
      "v_cmp_eq_u32 vcc, 0, v4\n\tv_mov_b32 v20, s68\n\tv_bfrev_b32 v21, 15\n\tv_mov_b32 v22, s86\n\tv_mov_b32 v23, s87\n\t"
      "v_cndmask_b32 v20, v21, v20, vcc\n\t"
      "buffer_store_dwordx2 v[22:23], v20, s[72:75], 0 offen sc1\n\t"
      "s_mov_b32 s96, s94\n\ts_memrealtime s[98:99]\n\ts_waitcnt lgkmcnt(0)\n\ts_add_u32 s96, s96, s98\n\t"
      "590:\n\ts_sleep 1\n\ts_memrealtime s[98:99]\n\ts_waitcnt lgkmcnt(0)\n\ts_sub_u32 s97, s98, s96\n\ts_cmp_lt_i32 s97, 0\n\ts_cbranch_scc1 590b\n\t"
      "s_branch 599f\n\t"
      P_STUBS(57, 56, 580) P_SLOW(580, 96, 54, 55)
      "599:\n\t"
      ".Lgm_code_to:"
      : "+{v[4:7]}"(xa), "+{v8}"(wa), "+{v[144:159]}"(vp), "+{v[10:15]}"(sr.wv), "+{s[24:27]}"(base) ST_OP
      : "{v0}"(pc.wo0), "{v248}"(pc.red), "{v249}"(pc.koff), "{v250}"(pc.voff0), "{v251}"(pc.voff1), "{v254}"(pc.wov), "{s[20:23]}"(pc.out),
        "{s37}"(w), "{s[40:43]}"(sr.ra), "{s[44:47]}"(sr.rb), "{s[48:51]}"(sr.rx), "{s[56:71]}"(sp), "{s[72:75]}"(pc.ws),
        "{s[80:83]}"(pc.wo), "{s91}"(outoff), "{s[94:95]}"(rt0)
      : "memory", "scc", "vcc", "s28", "s29", "s30", "s31", "s32", "s33", "s34", "s35", "s36", "s38", "s39", "s52", "s53", "s54", "s55", "s76", "s77",
        "s84", "s85", "s96", "s97", "s98", "s99", "s88", "s89", "s90", "s78", "s79", "s86", "s87", "s92", "s93", "v1", "v2", "v3", "v9", "v252", "v253",
#define R8(a, b, c, d, e, f, g, h) "v" #a, "v" #b, "v" #c, "v" #d, "v" #e, "v" #f, "v" #g, "v" #h
        R8(16, 17, 18, 19, 20, 21, 22, 23), R8(24, 25, 26, 27, 28, 29, 30, 31), R8(32, 33, 34, 35, 36, 37, 38, 39),
        R8(40, 41, 42, 43, 44, 45, 46, 47), R8(48, 49, 50, 51, 52, 53, 54, 55), R8(56, 57, 58, 59, 60, 61, 62, 63),
        R8(64, 65, 66, 67, 68, 69, 70, 71), R8(72, 73, 74, 75, 76, 77, 78, 79), R8(80, 81, 82, 83, 84, 85, 86, 87),
        R8(88, 89, 90, 91, 92, 93, 94, 95), R8(96, 97, 98, 99, 100, 101, 102, 103), R8(104, 105, 106, 107, 108, 109, 110, 111),
        R8(112, 113, 114, 115, 116, 117, 118, 119), R8(120, 121, 122, 123, 124, 125, 126, 127), R8(128, 129, 130, 131, 132, 133, 134, 135),
        R8(136, 137, 138, 139, 140, 141, 142, 143), R8(160, 161, 162, 163, 164, 165, 166, 167), R8(168, 169, 170, 171, 172, 173, 174, 175),
        R8(176, 177, 178, 179, 180, 181, 182, 183), R8(184, 185, 186, 187, 188, 189, 190, 191), R8(192, 193, 194, 195, 196, 197, 198, 199),
        R8(200, 201, 202, 203, 204, 205, 206, 207), R8(208, 209, 210, 211, 212, 213, 214, 215), R8(216, 217, 218, 219, 220, 221, 222, 223),
        R8(224, 225, 226, 227, 228, 229, 230, 231), R8(232, 233, 234, 235, 236, 237, 238, 239), R8(240, 241, 242, 243, 244, 245, 246, 247) ST_CL);
}
DI void identity_seed(int r32, int h, unsigned& val, unsigned& hot) {
  val = 0x3F80u << ((r32 & 1) * 16);
  hot = (((r32 >> 2) & 1) == h ? 1u : 0u) << (((r32 >> 1) & 1) | ((r32 >> 2) & 6));
}

#define HG 2
#define ARGS_AT_ONCE 1
#define TLBALL 0
#define WARM 6
#define CODE_LINES 1
#define DBG_OFF 0u
#define WS_SIZE WS_END

struct Scalars { u32x16 sp; u32x4 base; unsigned outoff; };
DI Scalars scalars(unsigned b, unsigned w, unsigned lds0) {
  const unsigned xc = b & 7, in = b >> 3, g = HG * (xc % (8 / HG)) + in % HG, tb = (32 / HG) * (xc / (8 / HG)) + in / HG;
  const unsigned wi = w & 3, grp = w >> 2, pr = wi & 1, rot = (in / HG + (8 / HG) * (in % HG)) & 7;
  const unsigned hd = 4 * g + wi, otb = 4 * xc + (in & 3), og = in >> 2, rh = 4 * g + (tb & 3), sl = tb >> 2;
  Scalars s;
  s.sp = u32x16{grp, lds0 + L_RING + wi * 2048, lds0 + L_X + wi * 2048, rot, rot * 128, wi >> 1, tb * 2048, ((tb + 1) & 31) * 2048,
                ((tb + 2) & 31) * 2048, L_Q + (wi >> 1) * (L_VB - L_Q) + pr * 4096, L_Q + wi * 2048 + grp * 1024, L_PQ + pr * 8192,
                (b | w) ? OOB : WS_EPOCH, L_VB + pr * 4096, DBG_OFF, 0x3FB8AA3Bu  };
  s.base = u32x4{hd * 1024, rh * 8 + sl, ((otb * 2 + grp) * 32 + wi * 8) * 1024, 0u};
  s.outoff = (64 * otb + 8 * w) * 1024 + og * 128;
  return s;
}

__global__ __launch_bounds__(512, 2) void gated_mix_kernel(const char* __restrict__ x, const char* __restrict__ wqa,
                                                           const char* __restrict__ woa, const char* __restrict__ wqb,
                                                           const char* __restrict__ wphi, const char* __restrict__ wob,
                                                           const char* __restrict__ gatew, char* __restrict__ out,
                                                           char* __restrict__ ws) {
  __shared__ __attribute__((aligned(16))) char lds_raw[L_BYTES];
  LAS char* lds = (LAS char*)lds_raw;
  const unsigned lds0 = (unsigned)(uintptr_t)lds;
  const int tid = threadIdx.x, lane = tid & 63, w = __builtin_amdgcn_readfirstlane(tid >> 6);
  const int xc = blockIdx.x & 7, in = blockIdx.x >> 3;
  const int g = HG * (xc % (8 / HG)) + in % HG, tb = (32 / HG) * (xc / (8 / HG)) + in / HG;
  const int r32 = lane & 31, h = lane >> 5;
  const int wi = w & 3, grp = w >> 2, pr = wi & 1;
  unsigned long long rt0, rt1;
  asm volatile("s_memrealtime %0" : "={s[94:95]}"(rt0));
  asm volatile("" : "+s"(x), "+s"(wqa), "+s"(woa), "+s"(wqb), "+s"(wphi), "+s"(wob), "+s"(gatew), "+s"(out), "+s"(ws));
  asm volatile("s_waitcnt lgkmcnt(0)\n\ts_memrealtime %1\n\ts_waitcnt lgkmcnt(0)\n\ts_sub_u32 s94, s96, s94\n\ts_lshl_b32 s94, s94, 2\n\ts_min_u32 s94, s94, 4000" : "+{s[94:95]}"(rt0), "={s[96:97]}"(rt1) :: "scc");

  const int l8 = lane >> 3, dcp = lane & 7;
  Src sr;
  sr.ra = __builtin_amdgcn_make_buffer_rsrc((void*)wqa, 0, 512 * 3072, 0x00020000);
  sr.rx = __builtin_amdgcn_make_buffer_rsrc((void*)x, 0, 2048 * 1024, 0x00020000);
  const unsigned wv = (unsigned)(16 * wi + l8) * 3072 + g * 128 + ((dcp ^ ((lane >> 2) & 4)) * 16);
#pragma unroll
  for (int c = 0; c < 3; ++c) { sr.wv[2 * c] = wv + c * 1024; sr.wv[2 * c + 1] = wv + c * 1024 + 8 * 3072 - 1024; }
  const unsigned rot = (in / HG + (8 / HG) * (in % HG)) & 7;
  const unsigned xv0 = (unsigned)(64 * tb + 16 * wi + l8) * 1024 + ((dcp ^ (l8 >> 1)) * 16), xv1 = ((xv0 + 8192) ^ 64) - 1024;
  const unsigned ml = lds0 + L_RING + wi * 2048, mx = lds0 + L_X + wi * 2048;
  const unsigned rotx = rot * 128;
  if (grp) first_chunks(sr, xv0, xv1, ml, mx, rot, rotx);
  SBAR();
  const unsigned hd = 4 * g + wi, og = in >> 2;
  sr.rb = __builtin_amdgcn_make_buffer_rsrc((void*)wqb, 0, 512 * 3072, 0x00020000);
  const rsrc_t rws = __builtin_amdgcn_make_buffer_rsrc((void*)ws, 0, WS_SIZE, 0x00020000);
  if (grp == 0) {
    if (wi < 2) dma16(__builtin_amdgcn_make_buffer_rsrc((void*)wphi, 0, 32 * 512, 0x00020000), lane * 16, g * 2048 + wi * 1024, lds0 + L_WPHI + wi * 1024);
    if (wi == 2) dma16(__builtin_amdgcn_make_buffer_rsrc((void*)gatew, 0, 1024, 0x00020000), lane * 16, 0, lds0 + L_GW);
    {
      const unsigned sh = in / HG, nsh = 32 / HG;
      if (wi < 2 * HG / 2 + (HG == 1)) {
        const unsigned i = (sh * HG + wi % HG) * 64 + lane;
        const unsigned row = (i >> 1) & 511, sg = i & 1, st = i >> 10;
        const rsrc_t r = (st && !sg) ? sr.ra : sr.rb;
      }
      if (wi == 0) {
        asm volatile("s_mov_b32 m0, %0\n\ts_nop 0\n\tbuffer_load_dword %1, %2, 0 offen sc1 lds" :: "s"(lds0 + L_DUMMY), "v"((TLBALL ? lane + 64 * (in & 3) : in * 8 + l8) * 65536 + 0xFF80), "s"(rws) : "memory");
      }
      if (wi == 1) {
        const rsrc_t ro = __builtin_amdgcn_make_buffer_rsrc((void*)out, 0, 2048 * 1024, 0x00020000);
        asm volatile("s_mov_b32 m0, %0\n\ts_nop 0\n\tbuffer_load_dword %1, %2, 0 offen sc1 lds" :: "s"(lds0 + L_DUMMY), "v"((TLBALL ? (lane & 31) : in) * 65536 + 0xFF80), "s"(ro) : "memory");
      }
      if (wi == 2) {
        const unsigned i = blockIdx.x * 32 + (lane & 31);
        const rsrc_t r = __builtin_amdgcn_make_buffer_rsrc((void*)(blockIdx.x & 128 ? wob : woa), 0, 512 * 1024, 0x00020000);
        asm volatile("s_mov_b32 m0, %0\n\ts_nop 0\n\tbuffer_load_dword %1, %2, 0 offen lds" :: "s"(lds0 + L_DUMMY), "v"((i & 4095) * 128), "s"(r) : "memory");
      }
    }
    if (wi == 3) {
      unsigned long long pc;
      unsigned len;
      asm volatile("s_getpc_b64 %0\n.Lgm_code_from:\n\ts_mov_b32 %1, .Lgm_code_to-.Lgm_code_from" : "=s"(pc), "=s"(len));
      const rsrc_t rc = __builtin_amdgcn_make_buffer_rsrc((void*)pc, 0, len, 0x00020000);
      asm volatile("s_mov_b32 m0, %0\n\ts_nop 0\n\tbuffer_load_dword %1, %2, 0 offen lds" :: "s"(lds0 + L_DUMMY), "v"((in + 32 * l8) * 128), "s"(rc) : "memory");
    }
  }

  const unsigned kvb = WS_KV + (unsigned)hd * 131072, kvh = kvb + grp * 65536;
  Prog pc;
  pc.ws = rws;
  pc.koff = kvh + lane * 16;
  {
    const unsigned vreal = kvh + h * 256 + (lane & 15) * 16, vones = WS_ONES + g * 256;
    const bool pad = lane & 16, one = (lane & 11) == 0;
    pc.voff0 = pad ? (one ? vones - 1024 : OOB) : vreal; pc.voff1 = pad ? (one ? vones - 1536 : OOB) : vreal;
  }
  const int tq = (lane & 15) >> 2, tp_ = lane & 3, tnb = (lane >> 4) & 1;
  const unsigned wlane = L_RING + (8 * h + tq) * 128 + (((2 * pr + tnb) ^ (tq & 2)) * 32) + tp_ * 8;
  u32x4 xa;
#pragma unroll
  for (int s = 0; s < 4; ++s) xa[s] = L_X + r32 * 128 + (((2 * s + h) ^ ((r32 >> 1) & 7)) * 16);
  unsigned vp[16];
  if (grp == 0) {
    const unsigned kb = WS_KV + (unsigned)(4 * g + 2 * pr) * 131072 + (unsigned)(2 * tb) * 2048, fl = WS_FKV + (g * 32 + tb) * FL;
    const int sw = (r32 & 19) | ((r32 & 4) << 1) | ((r32 & 8) >> 1);
    vp[0] = vp[1] = vp[2] = vp[3] = vp[4] = 0;
    vp[5] = wi < 2 ? kb + h * 512 + sw * 16 : kb + (r32 >> 4) * 131072 + 1024 + h * 256 + (r32 & 15) * 16;
    vp[6] = wi < 2 ? (r32 == 0 ? fl + (2 * pr + h) * 16 + 8 : OOB) : (wi == 2 && lane < 8 ? WS_ONES + tb * 2048 + g * 256 + lane * 16 : OOB);
    vp[7] = w == 0 ? WS_KR + ((4 * g + (tb & 3)) * 8 + (tb >> 2)) * 256 + lane * 4 : OOB;
    vp[8] = L_X + r32 * 128 + (((2 * wi + h) ^ ((r32 >> 1) & 7)) * 16);
    vp[9] = L_GW + rotx + wi * 32 + h * 16;
    vp[10] = L_GP + wi * 512 + lane * 4;
    vp[11] = tid < 4 ? fl + lane * 16 : OOB;
  } else {
    vp[0] = xv0; vp[1] = xv1; vp[4] = vp[7] = vp[9] = vp[11] = 0;
    vp[2] = L_WPHI + 2 * pr * 512 + (4 * h + tq) * 32 + tp_ * 8;
    vp[3] = tnb ? 0x80008000u : 0u;
    vp[5] = WS_KP + (unsigned)(((4 * g + 2 * pr) * 8 + (lane >> 4)) * 32 + tb) * 256 + (lane & 15) * 16;
    vp[6] = lane == 0 ? WS_FKP + ((g * 2 + pr) * 32 + tb) * FL : OOB;
    vp[8] = tnb ? 0x3F800000u : 0u;
    vp[10] = L_GP + 256 + lane * 4;
  }
  {
    unsigned val, hot;
    identity_seed(r32, h, val, hot);
    vp[12] = val; vp[13] = hot; vp[14] = WS_FKV + (g * 32 + r32) * FL + wi * 16; vp[15] = lane * 16;
  }
  pc.out = __builtin_amdgcn_make_buffer_rsrc((void*)out, 0, 2048 * 1024, 0x00020000);
  pc.wo = __builtin_amdgcn_make_buffer_rsrc((void*)(grp ? woa : wob), 0, 512 * 1024, 0x00020000);
  pc.wov = (unsigned)(128 * wi + l8) * 1024 + og * 128 + ((dcp ^ ((lane >> 2) & 4)) * 16);
  pc.wo0 = L_WO + wi * 32768 + (unsigned)(4 * h + tq) * 128 + ((tnb ^ (tq & 2)) * 32) + tp_ * 8;
  pc.red = L_RED + wi * 16384 + (grp * 32 + r32) * 256 + ((h ^ (r32 & 7)) * 16);

  unsigned bi = blockIdx.x, wv_ = w;
  asm volatile("" : "+s"(bi), "+s"(wv_));
  const Scalars sc = scalars(bi, wv_, lds0);
  program(sr, xa, wlane + (wi >> 1) * 8192,
          u32x16{vp[0], vp[1], vp[2], vp[3], vp[4], vp[5], vp[6], vp[7], vp[8], vp[9], vp[10], vp[11], vp[12], vp[13], vp[14], vp[15]}, sc.sp,
          sc.base, wv_, sc.outoff, pc, rt0 ST_ARG);
}

}

extern "C" void kernel_launch(void* const* d_in, const int* in_sizes, int n_in, void* d_out, int out_size, void* d_ws,
                              size_t ws_size, hipStream_t stream) {
  gated_mix_kernel<<<256, 512, 0, stream>>>((const char*)d_in[0], (const char*)d_in[1], (const char*)d_in[2], (const char*)d_in[4],
                                            (const char*)d_in[5], (const char*)d_in[6], (const char*)d_in[7], (char*)d_out, (char*)d_ws);
}
```

```cpp
#include <hip/hip_runtime.h>
#include <hip/hip_bf16.h>
#include <stdint.h>

namespace {

typedef __bf16 bf16x8 __attribute__((ext_vector_type(8)));
typedef __bf16 bf16x2 __attribute__((ext_vector_type(2)));
typedef float f32x16 __attribute__((ext_vector_type(16)));
typedef float f32x8 __attribute__((ext_vector_type(8)));
typedef float f32x4 __attribute__((ext_vector_type(4)));
typedef float f32x2 __attribute__((ext_vector_type(2)));
typedef unsigned u32x16 __attribute__((ext_vector_type(16)));
typedef unsigned u32x8 __attribute__((ext_vector_type(8)));
typedef unsigned u32x4 __attribute__((ext_vector_type(4)));
typedef unsigned u32x2 __attribute__((ext_vector_type(2)));
typedef short s16x4 __attribute__((ext_vector_type(4)));
typedef __amdgpu_buffer_rsrc_t rsrc_t;
#define LAS __attribute__((address_space(3)))
#define DI __device__ __forceinline__

constexpr unsigned WS_EPOCH = 0;
constexpr unsigned FL = 128;
constexpr unsigned WS_KR = 0x10000;
constexpr unsigned WS_ONES = 0x40000;
constexpr unsigned WS_FKV = 0x60000;
constexpr unsigned WS_FKP = 0x70000;
constexpr unsigned WS_FOB = 0x80000;
constexpr unsigned WS_KP = 0x100000;
constexpr unsigned WS_OB = 0x500000;
constexpr unsigned WS_OA = 0x700000;
constexpr unsigned WS_KV = 0x900000;
constexpr unsigned WS_END = 0xD00000;

constexpr unsigned L_X = 0;
constexpr unsigned L_RING = 65536;
constexpr unsigned L_WO = 0;
constexpr unsigned L_RED = 0;
constexpr unsigned L_PQ = 131072;
constexpr unsigned L_Q = 147456;
constexpr unsigned L_WPHI = L_Q;
constexpr unsigned L_GW = L_Q + 2048;
constexpr unsigned L_DUMMY = L_Q + 3072;
constexpr unsigned L_GP = L_Q + 4096;
constexpr unsigned L_VB = 155648;
constexpr unsigned L_BYTES = 163840;

constexpr int SC1 = 16;
constexpr unsigned OOB = 0xF0000000u;

DI void dma16(rsrc_t r, unsigned voff, unsigned soff, unsigned lds_dst) {
  asm volatile("s_mov_b32 m0, %0\n\ts_nop 0\n\tbuffer_load_dwordx4 %1, %2, %3 offen lds"
               :: "s"(lds_dst), "v"(voff), "s"(r), "s"(soff) : "memory");
}
#define SBAR() __builtin_amdgcn_sched_barrier(0)

#define EX(r) "v_exp_f32 v" #r ", v" #r "\n\t"
#define QK(s0, s1, k0, k1, q0, q1, c0, c1) "v_mfma_f32_32x32x16_bf16 v[" #s0 ":" #s1 "], v[" #k0 ":" #k1 "], v[" #q0 ":" #q1 "], v[" #c0 ":" #c1 "]\n\t"
#define PV(o0, o1, a0, a1, p0, p1) "v_smfmac_f32_32x32x32_bf16 v[" #o0 ":" #o1 "], v[" #a0 ":" #a1 "], v[" #p0 ":" #p1 "], v5\n\t"
#define LOADS(k0, k1, a0, a1, b0, b1)                                                     \
  "s_add_u32 s76, s76, 0x800\n\t"                                                         \
  "s_and_b32 s76, s76, 0xffff\n\t"                                                        \
  "buffer_load_dwordx4 v[" #k0 ":" #k1 "], v249, s[72:75], s76 offen sc1\n\t"             \
  "buffer_load_dwordx4 v[" #a0 ":" #a1 "], v250, s[72:75], s76 offen offset:1024 sc1\n\t" \
  "buffer_load_dwordx4 v[" #b0 ":" #b1 "], v251, s[72:75], s76 offen offset:1536 sc1\n\t"
#define WODMA                                                                             \
  "s_mov_b32 m0, s84\n\t"                                                                 \
  "s_add_u32 s84, s84, 0x400\n\t"                                                         \
  "buffer_load_dwordx4 v254, s[80:83], s85 offen lds\n\t"                                 \
  "s_add_u32 s85, s85, 0x2000\n\t"
#define ITER(ck0, ck1, ca0, ca1, cb0, cb1, pk0, pk1, pa0, pa1, pb0, pb1, nk0, nk1, EXTRA, WAIT) \
  EX(128) EX(129) PV(176, 191, pb0, pb1, 152, 159) EX(130) EX(131)                        \
  LOADS(pk0, pk1, pa0, pa1, pb0, pb1)                                                     \
  EX(132) EX(133) QK(144, 159, ck0, ck1, 196, 199, 112, 127) EX(134) EX(135) EXTRA EX(136) EX(137) EX(138) \
  PV(160, 175, ca0, ca1, 128, 135) EX(139) EX(140) EX(141) EX(142) EX(143)                 \
  EX(144) EX(145) PV(160, 175, cb0, cb1, 136, 143) EX(146) EX(147) EX(148) EX(149)         \
  WAIT                                                                                    \
  QK(128, 143, nk0, nk1, 192, 195, 96, 111) EX(150) EX(151) EX(152) EX(153) EX(154)      \
  PV(176, 191, ca0, ca1, 144, 151) EX(155) EX(156) EX(157) EX(158) EX(159)

#define ATTN_ROUTINE                                                                                  \
  "400:\n\t"                                                                                          \
  ITER(200, 203, 216, 219, 220, 223, 212, 215, 240, 243, 244, 247, 204, 207, WODMA, "")               \
  ITER(204, 207, 224, 227, 228, 231, 200, 203, 216, 219, 220, 223, 208, 211, "", "")                  \
  "s_cbranch_vccnz 402f\n\t401:\n\t"                                                                 \
  ITER(208, 211, 232, 235, 236, 239, 204, 207, 224, 227, 228, 231, 212, 215, WODMA, "s_waitcnt vmcnt(8)\n\t") \
  ITER(212, 215, 240, 243, 244, 247, 208, 211, 232, 235, 236, 239, 200, 203, "", "s_waitcnt vmcnt(7)\n\t")    \
  "s_sub_u32 s77, s77, 1\n\ts_waitcnt vmcnt(0)\n\ts_cbranch_scc0 400b\n\ts_setpc_b64 s[78:79]\n\t"  \
  "402:\n\ts_setprio 0\n\ts_branch 401b\n\t"
#define RUNV(N, V) "s_mov_b64 vcc, " V "\n\ts_mov_b32 s77, " N "\n\ts_swappc_b64 s[78:79], s[92:93]\n\t"
#define RUN(N) RUNV(N, "0")

#define STAMP(i) do { } while (0)
#define STAMP_RT(i) do { } while (0)
#define ST(i) ""
#define ST_END ""
#define ST_OP
#define ST_CL
#define ST_PAR
#define ST_ARG

#define G_BAR(N) "s_waitcnt vmcnt(" #N ") lgkmcnt(0)\n\ts_barrier\n\t"
#define G_LBAR "s_waitcnt lgkmcnt(0)\n\ts_barrier\n\t"
#define G_EPOCH "buffer_load_dwordx2 v[2:3], off, s[72:75], 0 sc1\n\t"
#define G_EPOCH_USE "v_readfirstlane_b32 s86, v2\n\tv_readfirstlane_b32 s87, v3\n\ts_add_u32 s86, s86, 1\n\ts_addc_u32 s87, s87, 0\n\t"
#define D_ROW(KC) "s_xor_b32 s52, s59, " KC "\n\ts_mul_i32 s52, s52, 196608\n\t"
#define D_A(R, COL, LOFF) "s_add_u32 m0, s57, " LOFF "\n\ts_nop 0\n\tbuffer_load_dwordx4 v[10+2*" #COL "], " R ", s52 offen lds\n\t"
#define D_B(R, COL) "buffer_load_dwordx4 v[11+2*" #COL "], " R ", s52 offen offset:1024 lds\n\t"
#define RA "s[40:43]"
#define RB "s[44:47]"
#define D_X(C)                                                                                        \
  "s_add_u32 m0, s58, 8192*" #C "\n\ts_xor_b32 s52, s60, 128*" #C "\n\t"                              \
  "buffer_load_dwordx4 v144, s[48:51], s52 offen lds\n\tbuffer_load_dwordx4 v145, s[48:51], s52 offen offset:1024 lds\n\t"
#define D_W(N, R0, C0, R1, C1) D_ROW(#N "&7") D_A(R0, C0, "16384*(" #N "&3)"), D_B(R0, C0), D_A(R1, C1, "16384*(" #N "&3)+8192"), D_B(R1, C1)
#define D_S0(N) D_W(N, RA, 1, RA, 2)
#define D_S1(N) D_W(N, RB, 0, RB, 1)
#define D_S2(N) D_W(N, RA, 0, RB, 2)
#define G_CAT(A, B, C, D) A B C D
#define G_APPLY(M, ...) M(__VA_ARGS__)
#define G_ALL(L) G_APPLY(G_CAT, L)

#define G_RW(W, C, S)                                                                                 \
  "ds_read_b64_tr_b16 v[" #W "+4*" #S ":" #W "+4*" #S "+1], v8 offset:16384*(" #C "&3)+2048*" #S "\n\t"       \
  "ds_read_b64_tr_b16 v[" #W "+4*" #S "+2:" #W "+4*" #S "+3], v8 offset:16384*(" #C "&3)+2048*" #S "+512\n\t"
#define G_RX(X, C, S, T) "ds_read_b128 v[" #X "+4*" #S ":" #X "+4*" #S "+3], v[4+" #S "] offset:8192*" #C "+4096*" #T "\n\t"
#define G_RS(W, X0, X1, C, S) G_RW(W, C, S) G_RX(X0, C, S, 0) G_RX(X1, C, S, 1)
#define G_READ(W, X0, X1, C) G_RS(W, X0, X1, C, 0) G_RS(W, X0, X1, C, 1) G_RS(W, X0, X1, C, 2) G_RS(W, X0, X1, C, 3)
#define G_M(ACC, W, X, S, CIN) \
  "v_mfma_f32_32x32x16_bf16 " ACC ", v[" #W "+4*" #S ":" #W "+4*" #S "+3], v[" #X "+4*" #S ":" #X "+4*" #S "+3], " CIN "\n\t"
#define G_A0 "v[16:31]"
#define G_A1 "v[32:47]"
#define G_MMA2(W, X0, X1, S) G_M(G_A0, W, X0, S, G_A0) G_M(G_A1, W, X1, S, G_A1)
#define G_STEP(RW, RX0, RX1, C, W, X0, X1, C0, C1, D0, D1, D2, D3)                                    \
  G_M(G_A0, W, X0, 0, C0) G_RW(RW, C, 0) G_RX(RX0, C, 0, 0) G_M(G_A1, W, X1, 0, C1) G_RX(RX1, C, 0, 1) G_RW(RW, C, 1) \
  G_M(G_A0, W, X0, 1, G_A0) G_RX(RX0, C, 1, 0) G_RX(RX1, C, 1, 1) D0 G_M(G_A1, W, X1, 1, G_A1) G_RW(RW, C, 2) G_RX(RX0, C, 2, 0) \
  G_M(G_A0, W, X0, 2, G_A0) G_RX(RX1, C, 2, 1) G_RW(RW, C, 3) D1 G_M(G_A1, W, X1, 2, G_A1) G_RX(RX0, C, 3, 0) G_RX(RX1, C, 3, 1) \
  G_M(G_A0, W, X0, 3, G_A0) D2 G_M(G_A1, W, X1, 3, G_A1) D3
#define G_EVEN(C, D0, D1, D2, D3) G_STEP(48, 64, 80, C, 96, 112, 128, G_A0, G_A1, D0, D1, D2, D3)
#define G_ODD(C, D0, D1, D2, D3) G_STEP(96, 112, 128, C, 48, 64, 80, G_A0, G_A1, D0, D1, D2, D3)
#define G_FIRST(D0, D1, D2, D3) G_STEP(96, 112, 128, 1, 48, 64, 80, "0", "0", D0, D1, D2, D3)
#define A_PACK                                                                                        \
  ".irp i,0,1,2,3\n\tv_cvt_pk_bf16_f32 v[48+\\i], v[16+2*\\i], v[17+2*\\i]\n\tv_cvt_pk_bf16_f32 v[56+\\i], v[24+2*\\i], v[25+2*\\i]\n\t.endr\n\t" \
  ".irp i,0,1,2,3\n\tv_cvt_pk_bf16_f32 v[52+\\i], v[32+2*\\i], v[33+2*\\i]\n\tv_cvt_pk_bf16_f32 v[60+\\i], v[40+2*\\i], v[41+2*\\i]\n\t.endr\n\t"
#define G_Z(L) ".irp r," L "\n\tv_mov_b64 v[\\r:\\r+1], 0\n\t.endr\n\t"
#define G_ZO G_Z("160,162,164,166,168,170,172,174,176,178,180,182,184,186,188,190,244,246")
#define G_QPRE "v_add_u32 v6, s66, v159\n\tv_mov_b32 v4, v159\n\tv_xor_b32 v7, 0x400, v6\n\tv_mov_b32 v5, 0xdddddddd\n\t"
#define G_QRD "ds_read_b128 v[192:195], v7\n\tds_read_b128 v[196:199], v6\n\t"
#define G_C(L) ".irp r," L "\n\tv_mov_b32 v\\r, 0xc2400000\n\t.endr\n\t"
#define G_C0 G_C("96,97,98,99,100,101,102,103,104,105,106,107,108,109,110,111")
#define G_C1 G_C("112,113,114,115,116,117,118,119,120,121,122,123,124,125,126,127")
#define G_ST4(R, V, O) "buffer_store_dwordx4 v[" #R ":" #R "+3], v" #V ", s[72:75], " O " sc1\n\t"
#define G_POLL(R) "buffer_load_dwordx3 v[" #R ":" #R "+2], v158, s[72:75], 0 offen sc1\n\t"
#define G_SPIN(L, AGAIN) "s_cbranch_vccz " #L "1f\n\t" AGAIN "s_sub_u32 s53, s53, 1\n\ts_cbranch_scc0 " #L "0b\n\ts_mov_b32 s53, 0\n\t" #L "1:\n\t"
#define G_POLLWAIT(L, R)                                                                              \
  #L "0:\n\ts_waitcnt vmcnt(0)\n\tv_cmp_ne_u64 vcc, s[86:87], v[" #R ":" #R "+1]\n\t"                  \
  G_SPIN(L, "s_sleep 4\n\t" G_POLL(R)) "s_waitcnt vmcnt(0)\n\tv_mov_b32 v9, v[" #R "+2]\n\t"
#define G_KV1(K, V, T)                                                                                \
  "buffer_load_dwordx4 v[" #K ":" #K "+3], v249, s[72:75], s" #T " offen sc1\n\t"                      \
  "buffer_load_dwordx4 v[" #V ":" #V "+3], v250, s[72:75], s" #T " offen offset:1024 sc1\n\t"          \
  "buffer_load_dwordx4 v[" #V "+4:" #V "+7], v251, s[72:75], s" #T " offen offset:1536 sc1\n\t"
#define G_KV G_KV1(200, 216, 62) G_KV1(204, 224, 63) G_KV1(208, 232, 64)
#define DPPMAX(R, CTRL) "v_max_f32_dpp v" #R ", v" #R ", v" #R " " CTRL " row_mask:0xf bank_mask:0xf\n\t"
#define SIGMOID(R, OUT) \
  "v_mul_f32 v" #R ", 0xbfb8aa3b, v" #R "\n\tv_exp_f32 v" #R ", v" #R "\n\ts_nop 0\n\tv_add_f32 v" #R ", 1.0, v" #R "\n\tv_rcp_f32 v" #OUT ", v" #R "\n\t"
#define A_GREAD(C)                                                                                    \
  "v_xor_b32 v92, 128*" #C ", v153\n\tv_xor_b32 v93, 128*(" #C "+1), v153\n\t"                     \
  "ds_read_b128 v[72:75], v152 offset:8192*" #C "\n\tds_read_b128 v[80:83], v152 offset:8192*" #C "+4096\n\t" \
  "ds_read_b128 v[76:79], v152 offset:8192*(" #C "+1)\n\tds_read_b128 v[84:87], v152 offset:8192*(" #C "+1)+4096\n\t" \
  "ds_read_b128 v[64:67], v92\n\tds_read_b128 v[68:71], v93\n\t"
#define A_GDOT                                                                                        \
  ".irp j,0,1,2,3\n\t"                                                                                \
  "v_dot2c_f32_bf16 v88, v[72+\\j], v[64+\\j]\n\tv_dot2c_f32_bf16 v89, v[80+\\j], v[64+\\j]\n\t"       \
  "v_dot2c_f32_bf16 v90, v[76+\\j], v[68+\\j]\n\tv_dot2c_f32_bf16 v91, v[84+\\j], v[68+\\j]\n\t"       \
  ".endr\n\t"
#define A_DPPMAX(CTRL) "s_nop 1\n\tv_max_f32_dpp v96, v96, v96 " CTRL " row_mask:0xf bank_mask:0xf\n\t"
#define F_JOBS(M) M(16, 80, 48) M(32, 84, 56) M(96, 88, 64) M(112, 92, 72)
#define F_MAX1(E, T, P)                                                                               \
  "v_max3_f32 v[" #T "], v[" #E "], v[" #E "+1], v[" #E "+2]\n\t"                                      \
  "v_max3_f32 v[" #T "+2], v[" #E "+8], v[" #E "+9], v[" #E "+10]\n\t"                                 \
  "v_max3_f32 v[" #T "+1], v[" #E "+3], v[" #E "+4], v[" #E "+5]\n\t"                                  \
  "v_max3_f32 v[" #T "+3], v[" #E "+11], v[" #E "+12], v[" #E "+13]\n\t"                               \
  "v_max3_f32 v[" #T "], v[" #T "], v[" #E "+6], v[" #E "+7]\n\t"                                      \
  "v_max3_f32 v[" #T "+2], v[" #T "+2], v[" #E "+14], v[" #E "+15]\n\t"                                \
  "v_max_f32 v[" #T "], v[" #T "], v[" #T "+1]\n\t"                                                    \
  "v_max_f32 v[" #T "+2], v[" #T "+2], v[" #T "+3]\n\t"
#define F_SWAP(E, T, P) "v_permlane32_swap_b32 v[" #T "], v[" #T "+2]\n\t"
#define F_MAX2(E, T, P) "v_max_f32 v[" #T "], v[" #T "], v[" #T "+2]\n\t"
#define F_NEG(E, T, P) "v_mul_f32 v[" #T "], 0xbfb8aa3b, v[" #T "]\n\t"
#define F_DUP(E, T, P) "v_mov_b32 v[" #T "+2], v[" #T "]\n\t"
#define F_FMA(E, T)                                                                                   \
  ".irp i,0,1,2,3,4,5,6,7\n\t"                                                                        \
  "v_fma_f32 v[" #E "+\\i], v[" #E "+\\i], s71, v[" #T "]\n\t"                                      \
  "v_fma_f32 v[" #E "+8+\\i], v[" #E "+8+\\i], s71, v[" #T "+2]\n\t"                                \
  ".endr\n\t"
#define F_EX(E, L) ".irp i," L "\n\tv_exp_f32 v[" #E "+\\i], v[" #E "+\\i]\n\t.endr\n\t"
#define F_EXP(E, T) F_FMA(E, T) F_EX(E, "0,8,1,9,2,10,3,11,4,12,5,13,6,14,7,15")
#define F_SUM1(E, T, P) ".irp i,0,1,2,3,4,5,6,7\n\tv_add_f32 v[" #P "+\\i], v[" #E "+2*\\i], v[" #E "+2*\\i+1]\n\t.endr\n\t"
#define F_SUM2(E, T, P) ".irp i,0,2,4,6\n\tv_add_f32 v[" #P "+\\i], v[" #P "+\\i], v[" #P "+\\i+1]\n\t.endr\n\t"
#define F_SUM3(E, T, P) "v_add_f32 v[" #T "], v[" #P "], v[" #P "+2]\n\tv_add_f32 v[" #T "+2], v[" #P "+4], v[" #P "+6]\n\t"
#define F_ADD(E, T, P) "v_add_f32 v[" #T "], v[" #T "], v[" #T "+2]\n\t"
#define F_RCP(E, T, P) "v_rcp_f32 v[" #T "], v[" #T "]\n\t"
#define F_PACK(E, T, P) ".irp i,0,1,2,3,4,5,6,7\n\tv_cvt_pk_bf16_f32 v[" #P "+\\i], v[" #E "+2*\\i], v[" #E "+2*\\i+1]\n\t.endr\n\t"
#define F_SCALE(E, T, P)                                                                              \
  ".irp i,0,1,2,3,4,5,6,7\n\t"                                                                        \
  "v_mul_f32 v[" #E "+\\i], v[" #E "+\\i], v[" #T "]\n\t"                                              \
  "v_mul_f32 v[" #E "+8+\\i], v[" #E "+8+\\i], v[" #T "+2]\n\t"                                        \
  ".endr\n\t" F_PACK(E, T, P)
#define F_TR0(E, T, P) "v_mfma_f32_32x32x16_bf16 v[" #E ":" #E "+15], v[" #P ":" #P "+3], v[128:131], 0\n\t"
#define F_TR1(E, T, P) "v_mfma_f32_32x32x16_bf16 v[" #E ":" #E "+15], v[" #P "+4:" #P "+7], v[132:135], v[" #E ":" #E "+15]\n\t"
#define P_TEST(F, I)                                                                                  \
  "v_max3_u32 v80, v[" #F "+4*" #I "], v[" #F "+1+4*" #I "], v[" #F "+2+4*" #I "]\n\t"                 \
  "v_max_u32 v80, v80, v[" #F "+3+4*" #I "]\n\t"                                                       \
  "v_cmp_eq_u32 vcc, -1, v80\n\t"
#define P_LOAD(F, I, SO) "buffer_load_dwordx4 v[" #F "+4*" #I ":" #F "+3+4*" #I "], v4, s[72:75], s" #SO " offen offset:1024*(" #I "&3) sc1\n\t"
#define P_RD(R, I, OFF)                                                                               \
  "ds_read_b64_tr_b16 v[" R ":" R "+1], v0 offset:" #OFF "+2048*" #I "\n\t"                         \
  "ds_read_b64_tr_b16 v[" R "+2:" R "+3], v0 offset:" #OFF "+2048*" #I "+1024\n\t"                  \
  "ds_read_b64_tr_b16 v[" R "+4:" R "+5], v1 offset:" #OFF "+2048*" #I "\n\t"                       \
  "ds_read_b64_tr_b16 v[" R "+6:" R "+7], v1 offset:" #OFF "+2048*" #I "+1024\n\t"
#define P_RB(I) P_RD("192+8*(" #I "&3)", I, 0)
#define P_RA(I) P_RD("128+8*" #I, I, 16384)
#define P_MM(W, F, I, C0, C1)                                                                         \
  "v_mfma_f32_32x32x16_bf16 v[16:31], v[" W ":" W "+3], v[" #F "+4*" #I ":" #F "+3+4*" #I "], " C0 "\n\t"   \
  "v_mfma_f32_32x32x16_bf16 v[32:47], v[" W "+4:" W "+7], v[" #F "+4*" #I ":" #F "+3+4*" #I "], " C1 "\n\t"
#define P_USEB(I, N, C0, C1, MORE)                                                                    \
  "s_waitcnt lgkmcnt(" #N ")\n\t" P_MM("192+8*(" #I "&3)", 48, I, C0, C1) MORE
#define P_USEA(I, N) "s_waitcnt vmcnt(" #N ")\n\t" P_TEST(96, I) "s_cbranch_vccnz 57" #I "f\n\t56" #I ":\n\t" P_MM("128+8*" #I, 96, I, G_A0, G_A1)
#define P_STUB(A, B, I, R) #A #I ":\n\ts_call_b64 s[78:79], " #R "f\n\ts_branch " #B #I "b\n\t"
#define P_STUBS(A, B, R) P_STUB(A, B, 0, R) P_STUB(A, B, 1, R) P_STUB(A, B, 2, R) P_STUB(A, B, 3, R) P_STUB(A, B, 4, R) P_STUB(A, B, 5, R) P_STUB(A, B, 6, R) P_STUB(A, B, 7, R)
#define P_RETRY(F, I, SO) P_TEST(F, I) "s_cbranch_vccz 1f\n\ts_mov_b32 s52, 1\n\t" P_LOAD(F, I, SO) "1:\n\t"
#define P_SLOW(R, F, S0, S1)                                                                          \
  #R ":\n\ts_waitcnt vmcnt(0)\n\ts_mov_b32 s52, 0\n\t"                                             \
  P_RETRY(F, 0, S0) P_RETRY(F, 1, S0) P_RETRY(F, 2, S0) P_RETRY(F, 3, S0) P_RETRY(F, 4, S1) P_RETRY(F, 5, S1) P_RETRY(F, 6, S1) P_RETRY(F, 7, S1) \
  "s_sub_u32 s53, s53, 1\n\ts_cselect_b32 s52, 0, s52\n\ts_cselect_b32 s53, 0, s53\n\t" \
  "s_cmp_lg_u32 s52, 0\n\ts_cbranch_scc1 " #R "b\n\ts_waitcnt vmcnt(0)\n\ts_setpc_b64 s[78:79]\n\t"
#define P_LOAD8(F, S0, S1) P_LOAD(F, 0, S0) P_LOAD(F, 1, S0) P_LOAD(F, 2, S0) P_LOAD(F, 3, S0) P_LOAD(F, 4, S1) P_LOAD(F, 5, S1) P_LOAD(F, 6, S1) P_LOAD(F, 7, S1)
#define P_LOADA P_LOAD8(96, 54, 55)
#define OUT_POL " sc1"
#define HALF_SWITCH 0
#define A_AT 7
#define P_AT(I) ".if " STR(A_AT) " == " #I "\n\t" P_LOADA ".endif\n\t"
#define STR_(x) #x
#define STR(x) STR_(x)
typedef unsigned u32x6 __attribute__((ext_vector_type(6)));
struct Src { u32x6 wv; rsrc_t ra, rb, rx; };
#define M_PAR0                                                                                        \
  "s_add_u32 s34, s26, 0x500000\n\ts_lshl_b32 s33, s56, 15\n\ts_sub_u32 s33, s26, s33\n\ts_lshr_b32 s33, s33, 3\n\ts_add_u32 s33, s33, 0x80000\n\t" \
  "s_lshl_b32 s29, s25, 13\n\ts_add_u32 s29, s29, 0x100000\n\ts_lshl_b32 s30, s25, 8\n\ts_add_u32 s30, s30, 0x10000\n\t" \
  "s_lshr_b32 s28, s25, 4\n\ts_lshl_b32 s28, s28, 12\n\ts_add_u32 s28, s28, 0x70000\n\t"
#define M_PAR1                                                                                        \
  "s_lshl_b32 s31, s24, 1\n\ts_add_u32 s31, s31, 0x10000\n\ts_lshl_b32 s39, s24, 7\n\ts_add_u32 s39, s39, 0x900000\n\t" \
  "s_lshl_b32 s32, s62, 5\n\ts_add_u32 s32, s32, s24\n\ts_add_u32 s24, s32, 0x700000\n\ts_add_u32 s25, s32, 0x708000\n\t" \
  "s_add_u32 s26, s32, 0x500000\n\ts_add_u32 s27, s32, 0x508000\n\ts_lshr_b32 s32, s32, 3\n\ts_add_u32 s32, s32, 0x80000\n\t"
#define M_PAR2                                                                                        \
  "s_lshl_b32 s38, s56, 15\n\ts_add_u32 s38, s38, s24\n\ts_lshl_b32 s35, s58, 1\n\ts_add_u32 s35, s35, 0x20000\n\t" \
  "s_add_u32 s36, s58, 0x26200\n\ts_lshl_b32 s88, s56, 11\n\ts_add_u32 s88, s88, s35\n\ts_lshl_b32 s89, s56, 10\n\t" \
  "s_add_u32 s89, s89, s58\n\ts_add_u32 s89, s89, 0x26000\n\ts_lshl_b32 s90, s37, 11\n\t"              \
  "s_mov_b32 s76, s64\n\ts_lshl_b32 s84, s58, 4\n\ts_lshl_b32 s85, s56, 14\n\ts_add_u32 s84, s84, s85\n\ts_mov_b32 s85, 0\n\t"
static_assert(WS_OB == 0x500000 && WS_OA == 0x700000 && WS_FOB == 0x80000 && WS_KP == 0x100000 && WS_KR == 0x10000 && WS_FKP == 0x70000 &&
              WS_KV == 0x900000 && L_PQ == 0x20000 && L_VB == 0x26000 && L_X == 0 && L_WO == 0 && L_RED == 0 && FL == 128, "literals above");
DI void first_chunks(const Src& sr, unsigned xv0, unsigned xv1, unsigned ml, unsigned mx, unsigned rot, unsigned rotx) {
  asm volatile(D_X(0) G_ALL(D_S0(0)) D_X(1) G_ALL(D_S0(1))
               :: "{v[10:15]}"(sr.wv), "{v144}"(xv0), "{v145}"(xv1), "{s[40:43]}"(sr.ra), "{s[48:51]}"(sr.rx), "{s57}"(ml), "{s58}"(mx),
                  "{s59}"(rot), "{s60}"(rotx)
               : "memory", "scc", "s52");
}
struct Prog { unsigned wo0, red, koff, voff0, voff1, wov; rsrc_t out, ws, wo; };
DI void program(Src sr, u32x4 xa, unsigned wa, u32x16 vp, u32x16 sp, u32x4 base, unsigned w, unsigned outoff, const Prog& pc ST_PAR) {
  asm volatile(
      "s_getpc_b64 s[92:93]\n\t"
      "99:\n\t"
      "s_add_u32 s92, s92, 400f-99b\n\ts_addc_u32 s93, s93, 0\n\t"
      "s_mov_b32 s53, 0x40000\n\t"
      "s_cmp_eq_u32 s56, 0\n\t"
      "s_cbranch_scc1 100f\n\t"
      G_BAR(6) D_X(2) G_ALL(D_S0(2)) G_BAR(6) D_X(3) G_ALL(D_S0(3)) G_BAR(6) D_X(4) G_ALL(D_S0(4)) M_PAR0
      G_BAR(6) D_X(5) G_ALL(D_S0(5)) M_PAR1 G_BAR(6) D_X(6) G_ALL(D_S0(6)) M_PAR2 G_BAR(6) D_X(7) G_ALL(D_S0(7))
      G_BAR(6) G_ALL(D_S1(8)) G_ALL(D_S1(9)) G_BAR(8) G_ALL(D_S1(10))
      "s_branch 200f\n\t"
      "100:\n\t"
      G_EPOCH
      G_Z("16,18,20,22,24,26,28,30,32,34,36,38,40,42,44,46")
      "s_mov_b32 s52, 0\n\t"
      G_LBAR G_READ(48, 64, 80, 0)
      "s_branch 102f\n\t"
      "101:\n\t"
      G_BAR(0) G_EVEN(0, "", "", "", "")
      "102:\n\t"
      G_LBAR G_ODD(1, "", "", "", "") G_LBAR G_EVEN(2, "", "", "", "") G_LBAR G_ODD(3, "", "", "", "")
      ".irp r,4,5,6,7\n\tv_xor_b32 v\\r, 0x8000, v\\r\n\t.endr\n\t"
      "s_xor_b32 s52, s52, 1\n\t"
      "s_cbranch_scc1 101b\n\t"
      ST(1)
      G_LBAR
      G_MMA2(96, 112, 128, 0) G_MMA2(96, 112, 128, 1) G_MMA2(96, 112, 128, 2) G_MMA2(96, 112, 128, 3)
      G_EPOCH_USE G_Z("88,90,92,94")
      "s_cmp_lg_u32 s61, 0\n\t"
      "s_cbranch_scc1 110f\n\t"
      "s_nop 0\n\t"
      ".irp r,16,17,18,19,20,21,22,23,24,25,26,27,28,29,30,31\n\tv_mul_f32 v\\r, 0x3eb8aa3b, v\\r\n\t.endr\n\t"
      G_LBAR
      ".irp r,32,33,34,35,36,37,38,39,40,41,42,43,44,45,46,47\n\tv_mul_f32 v\\r, 0x3eb8aa3b, v\\r\n\t.endr\n\t"
      A_PACK
      G_ST4(48, 149, "0 offen") G_ST4(52, 149, "0 offen offset:2048")
      ".irp j,0,1,2,3\n\t"
      "v_dot2c_f32_bf16 v92, v[48+\\j], v[48+\\j]\n\tv_dot2c_f32_bf16 v93, v[52+\\j], v[52+\\j]\n\t"
      "v_dot2c_f32_bf16 v94, v[56+\\j], v[56+\\j]\n\tv_dot2c_f32_bf16 v95, v[60+\\j], v[60+\\j]\n\t"
      ".endr\n\t"
      "s_mov_b32 s54, 131072\n\t"
      G_LBAR
      G_ST4(56, 149, "s54 offen") G_ST4(60, 149, "s54 offen offset:2048")
      "v_permlane32_swap_b32 v92, v94\n\tv_permlane32_swap_b32 v93, v95\n\t"
      "v_add_f32 v96, v92, v94\n\tv_add_f32 v97, v93, v95\n\t"
      A_GREAD(0)
      "v_max_f32 v96, v96, v97\n\t"
      A_DPPMAX("quad_perm:[1,0,3,2]") A_DPPMAX("quad_perm:[2,3,0,1]") A_DPPMAX("row_half_mirror") A_DPPMAX("row_mirror")
      "v_mov_b32 v97, v96\n\tv_mov_b32 v100, -1\n\ts_nop 0\n\tv_permlane16_swap_b32 v96, v97\n\tv_max_f32 v96, v96, v97\n\t"
      "buffer_store_dword v96, v150, s[72:75], 0 offen sc1\n\t"
      "buffer_store_dword v100, v151, s[72:75], 0 offen sc1\n\t"
      "s_branch 111f\n\t"
      "110:\n\t"
      ".irp j,0,1,2,3,4,5,6,7\n\tv_bfe_i32 v[96+\\j], v157, \\j, 1\n\t.endr\n\t"
      A_PACK
      ".irp j,0,1,2,3,4,5,6,7\n\tv_and_b32 v[96+\\j], v156, v[96+\\j]\n\t.endr\n\t"
      G_LBAR
      "s_setprio 2\n\t"
      "v_mfma_f32_32x32x16_bf16 v[16:31], v[48:51], v[96:99], 0\n\t"
      "v_mfma_f32_32x32x16_bf16 v[32:47], v[52:55], v[96:99], 0\n\t"
      "v_mfma_f32_32x32x16_bf16 v[16:31], v[56:59], v[100:103], v[16:31]\n\t"
      "v_mfma_f32_32x32x16_bf16 v[32:47], v[60:63], v[100:103], v[32:47]\n\t"
      "s_setprio 0\n\t"
      ".irp r,104,105,106,107\n\tv_mov_b32 v\\r, 0x3f803f80\n\t.endr\n\t"
      G_ST4(104, 150, "0 offen")
      "s_nop 4\n\t"
      F_PACK(16, 0, 48)
      G_ST4(48, 149, "0 offen") G_ST4(52, 149, "0 offen offset:512")
      G_LBAR
      F_PACK(32, 0, 56)
      G_ST4(56, 149, "0 offen offset:2048") G_ST4(60, 149, "0 offen offset:2560")
      A_GREAD(0)
      "111:\n\t"
      G_LBAR A_GDOT A_GREAD(2) M_PAR0
      G_LBAR A_GDOT A_GREAD(4) M_PAR1
      G_LBAR A_GDOT A_GREAD(6) M_PAR2
      G_LBAR A_GDOT
      "v_mov_b32 v94, s86\n\tv_mov_b32 v95, s87\n\tv_and_b32 v92, 0xfffff0ff, v154\n\t"
      "v_add_f32 v88, v88, v90\n\tv_add_f32 v89, v89, v91\n\t"
      "ds_write2st64_b32 v154, v88, v89 offset1:1\n\t"
      G_BAR(0)
      "ds_read2st64_b32 v[64:65], v92 offset1:1\n\tds_read2st64_b32 v[66:67], v92 offset0:2 offset1:3\n\t"
      "ds_read2st64_b32 v[68:69], v92 offset0:4 offset1:5\n\tds_read2st64_b32 v[70:71], v92 offset0:6 offset1:7\n\t"
      "buffer_store_dwordx2 v[94:95], v155, s[72:75], 0 offen sc1\n\t"
      "s_waitcnt lgkmcnt(0)\n\t"
      "v_add_f32 v64, v64, v66\n\tv_add_f32 v65, v65, v67\n\tv_add_f32 v68, v68, v70\n\tv_add_f32 v69, v69, v71\n\t"
      "v_add_f32 v64, v64, v68\n\tv_add_f32 v65, v65, v69\n\t"
      "s_nop 1\n\tv_permlane32_swap_b32 v64, v65\n\t"
      "v_add_f32 v64, v64, v65\n\t" SIGMOID(64, 253)
      "s_nop 0\n\tv_mov_b32 v252, v253\n\t"
      ST(2)
      G_LBAR "s_setprio 1\n\t" "v_permlane32_swap_b32 v253, v252\n\t" G_READ(48, 64, 80, 0) G_ALL(D_S2(19))
      G_LBAR G_APPLY(G_FIRST, D_S2(20))
      G_LBAR G_APPLY(G_EVEN, 2, D_S2(21)) G_BAR(8) G_APPLY(G_ODD, 3, D_S2(22))
      G_BAR(8) G_APPLY(G_EVEN, 4, D_S2(23))
      G_BAR(8) G_ODD(5, "buffer_load_dword v9, v158, s[72:75], 0 offen offset:8 sc1\n\t", G_KV1(200, 216, 62), G_KV1(204, 224, 63), G_KV1(208, 232, 64))
      G_BAR(14) G_EVEN(6, G_Z("160,162,164,166,168"), G_Z("170,172,174,176,178"), G_Z("180,182,184,186"), G_Z("188,190,244,246"))
      G_BAR(9) G_ODD(7, DPPMAX(9, "quad_perm:[1,0,3,2]"), DPPMAX(9, "quad_perm:[2,3,0,1]"), DPPMAX(9, "row_half_mirror"), "s_nop 0\n\t" DPPMAX(9, "row_mirror"))
      "s_waitcnt lgkmcnt(0)\n\t"
      G_MMA2(96, 112, 128, 0) G_MMA2(96, 112, 128, 1) G_MMA2(96, 112, 128, 2) G_MMA2(96, 112, 128, 3)
      "v_add_u32 v1, s65, v159\n\t"
      "s_setprio 2\n\t"
      G_QPRE G_Z("152,154,156,158") "v_mov_b32 v8, v9\n\t"
      A_PACK
      ".irp i,0,1,2,3\n\tds_write_b128 v1, v[48+4*\\i:51+4*\\i] offset:1024*\\i\n\t.endr\n\t"
      G_C0 G_C1
      ST(3)
      G_LBAR
      G_QRD
      "s_branch 300f\n\t"
      ATTN_ROUTINE
      "200:\n\t"
      ST(1)
      G_BAR(8) "s_setprio 1\n\t" G_EPOCH G_READ(48, 64, 80, 0) G_ALL(D_S1(11))
      G_BAR(9) G_APPLY(G_FIRST, D_S1(12))
      G_BAR(9) G_APPLY(G_EVEN, 2, D_S1(13)) G_BAR(8) G_APPLY(G_ODD, 3, D_S1(14))
      G_BAR(8) G_APPLY(G_EVEN, 4, D_S1(15)) G_BAR(8) G_APPLY(G_ODD, 5, D_S2(16))
      G_BAR(8) G_APPLY(G_EVEN, 6, D_S2(17)) G_BAR(8) G_APPLY(G_ODD, 7, D_S2(18))
      ST(2)
      G_BAR(8)
      "s_setprio 0\n\t"
      "ds_read_b64_tr_b16 v[64:65], v146\n\tds_read_b64_tr_b16 v[66:67], v146 offset:256\n\t"
      "ds_read_b64_tr_b16 v[68:69], v146 offset:512\n\tds_read_b64_tr_b16 v[70:71], v146 offset:768\n\t"
      "ds_read2st64_b32 v[72:73], v154 offset1:2\n\tds_read2st64_b32 v[74:75], v154 offset0:4 offset1:6\n\t"
      G_MMA2(96, 112, 128, 0) G_EPOCH_USE G_MMA2(96, 112, 128, 1)
      "s_waitcnt lgkmcnt(0)\n\t"
      "v_add_f32 v72, v72, v74\n\tv_add_f32 v73, v73, v75\n\t"
      ".irp r,64,65,66,67\n\tv_xor_b32 v\\r, v147, v\\r\n\t.endr\n\t"
      "v_add_f32 v72, v72, v73\n\t"
      G_MMA2(96, 112, 128, 2)
      "v_mov_b32 v73, v72\n\t"
      ".irp r,68,69,70,71\n\tv_xor_b32 v\\r, v147, v\\r\n\t.endr\n\t"
      "v_permlane32_swap_b32 v72, v73\n\t"
      G_MMA2(96, 112, 128, 3)
      "v_add_f32 v72, v72, v73\n\t" SIGMOID(72, 253)
      "s_nop 5\n\t"
      A_PACK
      G_BAR(4)
      "s_setprio 2\n\t"
      "v_mfma_f32_32x32x16_bf16 v[16:31], v[64:67], v[48:51], 0\n\t"
      "v_mfma_f32_32x32x16_bf16 v[32:47], v[64:67], v[52:55], 0\n\t"
      ".irp j,0,1,2,3\n\tv_bfe_i32 v[128+\\j], v157, \\j, 1\n\t.endr\n\t"
      "v_mfma_f32_32x32x16_bf16 v[96:111], v[68:71], v[56:59], 0\n\t"
      ".irp j,4,5,6,7\n\tv_bfe_i32 v[128+\\j], v157, \\j, 1\n\t.endr\n\t"
      "v_mfma_f32_32x32x16_bf16 v[112:127], v[68:71], v[60:63], 0\n\t"
      "s_setprio 0\n\t"
      ".irp j,0,1,2,3,4,5,6,7\n\tv_and_b32 v[128+\\j], v156, v[128+\\j]\n\t.endr\n\t"
      F_JOBS(F_MAX1) "s_nop 0\n\t" F_JOBS(F_SWAP) F_JOBS(F_MAX2) F_JOBS(F_NEG) F_JOBS(F_DUP) F_JOBS(F_SWAP)
      G_BAR(0)
      F_EXP(16, 80) F_FMA(32, 84) F_EX(32, "0,8,1,9")
      G_LBAR
      G_POLL(140) F_EX(32, "2,10,3,11,4,12,5,13,6,14,7,15") F_EXP(96, 88)
      G_LBAR
      F_EXP(112, 92) F_SUM1(16, 80, 48) F_SUM1(32, 84, 56) G_POLLWAIT(22, 140)
      G_LBAR
      F_SUM1(96, 88, 64) DPPMAX(9, "quad_perm:[1,0,3,2]") F_SUM1(112, 92, 72) DPPMAX(9, "quad_perm:[2,3,0,1]") F_JOBS(F_SUM2)
      DPPMAX(9, "row_half_mirror") F_JOBS(F_SUM3) DPPMAX(9, "row_mirror") F_JOBS(F_SWAP) F_JOBS(F_ADD) F_JOBS(F_RCP) F_JOBS(F_DUP) F_JOBS(F_SWAP)
      G_LBAR
      G_KV1(200, 216, 62) F_SCALE(16, 80, 48) G_KV1(204, 224, 63) F_SCALE(32, 84, 56) G_KV1(208, 232, 64) F_SCALE(96, 88, 64)
      G_LBAR
      F_SCALE(112, 92, 72)
      "s_cmp_lg_u32 s61, 0\n\t"
      "s_cbranch_scc1 230f\n\t"
      "v_add_u32 v136, s67, v159\n\t"
      ".irp i,0,1,2,3,4,5,6,7\n\tds_write_b128 v136, v[48+4*\\i:51+4*\\i] offset:1024*\\i\n\t.endr\n\t"
      G_ZO G_C0
      "s_branch 231f\n\t"
      "230:\n\t"
      F_JOBS(F_TR0) F_JOBS(F_TR1) G_ZO F_JOBS(F_PACK)
      ".irp r,96,97,98,99,100,101,102,103,104,105,106,107,108,109,110,111\n\tv_mov_b32 v\\r, v152\n\t.endr\n\t"
      "v_add_u32 v136, s69, v159\n\tv_mov_b32 v2, s86\n\tv_mov_b32 v3, s87\n\t"
      "231:\n\t"
      G_QPRE G_Z("152,154,156,158") "v_mov_b32 v8, v9\n\t" G_C1
      ST(3)
      G_LBAR
      G_QRD
      "s_cmp_eq_u32 s61, 0\n\t"
      "s_cbranch_scc1 151f\n\t"
      "s_setprio 3\n\t"
      ".irp i,0,1,2,3\n\tds_read_b128 v[80+4*\\i:83+4*\\i], v136 offset:1024*\\i\n\t.endr\n\t"
      "s_waitcnt lgkmcnt(2)\n\t"
      "v_mfma_f32_32x32x16_bf16 v[16:31], v[80:83], v[128:131], v[96:111]\n\t"
      "v_mfma_f32_32x32x16_bf16 v[32:47], v[84:87], v[128:131], v[96:111]\n\t"
      "s_waitcnt lgkmcnt(0)\n\t"
      "v_mfma_f32_32x32x16_bf16 v[112:127], v[88:91], v[128:131], v[96:111]\n\t"
      "v_mfma_f32_32x32x16_bf16 v[96:111], v[92:95], v[128:131], v[96:111]\n\t"
      "s_mov_b32 s52, 32768\n\ts_mov_b32 s54, 65536\n\ts_mov_b32 s55, 98304\n\t"
      "s_nop 5\n\t"
      F_PACK(16, 0, 80) F_PACK(32, 0, 88)
      "v_mfma_f32_32x32x16_bf16 v[16:31], v[48:51], v[80:83], 0\n\t"
      F_PACK(112, 0, 128)
      "v_mfma_f32_32x32x16_bf16 v[16:31], v[52:55], v[84:87], v[16:31]\n\t"
      F_PACK(96, 0, 136)
      "v_mfma_f32_32x32x16_bf16 v[16:31], v[56:59], v[88:91], v[16:31]\n\t"
      "v_mfma_f32_32x32x16_bf16 v[32:47], v[64:67], v[128:131], 0\n\t"
      "v_mfma_f32_32x32x16_bf16 v[16:31], v[60:63], v[92:95], v[16:31]\n\t"
      "v_mfma_f32_32x32x16_bf16 v[32:47], v[68:71], v[132:135], v[32:47]\n\t"
      "v_mfma_f32_32x32x16_bf16 v[32:47], v[72:75], v[136:139], v[32:47]\n\t"
      "v_mfma_f32_32x32x16_bf16 v[32:47], v[76:79], v[140:143], v[32:47]\n\t"
      "s_nop 8\n\t"
      F_PACK(16, 0, 80)
      G_ST4(80, 149, "0 offen") G_ST4(84, 149, "s52 offen")
      "s_nop 0\n\t"
      F_PACK(32, 0, 88)
      G_ST4(88, 149, "s54 offen") G_ST4(92, 149, "s55 offen")
      "s_setprio 1\n\t"
      "s_waitcnt vmcnt(0)\n\t"
      "buffer_store_dwordx2 v[2:3], v150, s[72:75], 0 offen sc1\n\t"
      G_C0 G_C1
      "s_branch 301f\n\t"
      "151:\n\t"
      "s_setprio 1\n\t"
      "300:\n\t"
      "s_waitcnt vmcnt(0) lgkmcnt(0)\n\t"
      "301:\n\t"
      "v_mov_b64 v[16:17], 0\n\t"
      "v_permlane16_swap_b32 v9, v8\n\t"
      QK(128, 143, 200, 203, 192, 195, 96, 111)
      ".irp j,0,1,2,3\n\tv_dot2c_f32_bf16 v16, v[192+\\j], v[192+\\j]\n\tv_dot2c_f32_bf16 v17, v[196+\\j], v[196+\\j]\n\t.endr\n\t"
      "v_max_f32 v9, v9, v8\n\t"
      "s_nop 1\n\t"
      "v_permlane32_swap_b32 v16, v17\n\t"
      "v_add_f32 v16, v16, v17\n\t"
      "v_mul_f32 v16, v16, v9\n\t"
      "v_cmp_nge_f32 vcc, 0x45100000, v16\n\t"
      "s_cbranch_vccnz 310f\n\t"
      "311:\n\t"
      ST(4)
      "s_cmp_eq_u32 s56, 0\n\t"
      "s_cbranch_scc1 320f\n\t"
      RUN("5")
      "s_branch 330f\n\t"
      "310:\n\t"
      "v_mov_b32 v96, 0xff7fffff\n\tv_mov_b32 v112, 0xff7fffff\n\t"
      "s_mov_b32 s52, s39\n\ts_mov_b32 s54, 63\n\t"
      "312:\n\t"
      "buffer_load_dwordx4 v[16:19], v4, s[72:75], s52 offen sc1\n\t"
      "s_add_u32 s52, s52, 0x800\n\t"
      "s_waitcnt vmcnt(0)\n\t"
      "v_mfma_f32_32x32x16_bf16 v[32:47], v[16:19], v[192:195], 0\n\t"
      "v_mfma_f32_32x32x16_bf16 v[48:63], v[16:19], v[196:199], 0\n\t"
      "s_nop 11\n\t"
      ".irp i,0,2,4,6,8,10,12,14\n\tv_max3_f32 v96, v96, v[32+\\i], v[33+\\i]\n\tv_max3_f32 v112, v112, v[48+\\i], v[49+\\i]\n\t.endr\n\t"
      "s_sub_u32 s54, s54, 1\n\t"
      "s_cbranch_scc0 312b\n\t"
      "v_mov_b32 v97, v96\n\tv_mov_b32 v113, v112\n\ts_nop 1\n\t"
      "v_permlane32_swap_b32 v96, v97\n\tv_permlane32_swap_b32 v112, v113\n\t"
      "v_max_f32 v96, v96, v97\n\tv_max_f32 v112, v112, v113\n\t"
      "v_xor_b32 v96, 0x80000000, v96\n\tv_xor_b32 v112, 0x80000000, v112\n\t"
      ".irp r,1,2,3,4,5,6,7,8,9,10,11,12,13,14,15\n\tv_mov_b32 v[96+\\r], v96\n\tv_mov_b32 v[112+\\r], v112\n\t.endr\n\t"
      "s_nop 1\n\t"
      QK(128, 143, 200, 203, 192, 195, 96, 111)
      "s_nop 11\n\t"
      "s_branch 311b\n\t"
      "320:\n\t"
      RUN("0")
      ST(5)
      "v_mov_b64 v[16:17], -1\n\tv_mov_b64 v[18:19], -1\n\t"
      G_ST4(16, 4, "s24 offen")
      RUN("0")
      "s_cmp_lg_u32 s37, 0\n\t"
      "s_cbranch_scc1 322f\n\t"
      "v_and_b32 v20, 0x1f0, v4\n\tv_lshlrev_b32 v20, 3, v20\n\ts_add_u32 s52, s29, 4096\n\t"
      "3210:\n\tbuffer_load_dwordx2 v[22:23], v20, s[72:75], s28 offen sc1\n\ts_waitcnt vmcnt(0)\n\t"
      "v_cmp_ne_u64 vcc, s[86:87], v[22:23]\n\t" G_SPIN(321, "s_sleep 4\n\t")
      P_LOAD8(32, 29, 52)
      "322:\n\t"
      RUN("0")
      ST(6)
      G_ST4(16, 4, "s25 offen")
      "s_cmp_lg_u32 s37, 0\n\t"
      "s_cbranch_scc1 323f\n\t"
      G_Z("24,26,28,30")
      "v_mov_b32 v20, 0x3f80\n\tv_mov_b32 v21, 0x3f800000\n\t"
      ".irp i,0,1,2,3,4,5,6,7\n\t.irp j,0,1,2,3\n\t"
      "v_dot2c_f32_bf16 v[24+2*\\j], v[32+4*\\i+\\j], v20\n\tv_dot2c_f32_bf16 v[25+2*\\j], v[32+4*\\i+\\j], v21\n\t"
      ".endr\n\t.endr\n\t"
      "v_and_b32 v20, 0xf0, v4\n\tv_lshrrev_b32 v21, 5, v4\n\tv_lshrrev_b32 v22, 7, v4\n\t"
      ".irp e,0,1,2,3\n\tv_permlane16_swap_b32 v[24+\\e], v[28+\\e]\n\t.endr\n\t"
      ".irp e,0,1,2,3\n\tv_add_f32 v[24+\\e], v[24+\\e], v[28+\\e]\n\t.endr\n\t"
      "v_and_or_b32 v20, v21, 8, v20\n\tv_and_or_b32 v20, v22, 4, v20\n\t"
      "v_permlane32_swap_b32 v24, v26\n\tv_permlane32_swap_b32 v25, v27\n\t"
      "v_add_f32 v24, v24, v26\n\tv_add_f32 v25, v25, v27\n\t"
      "v_cvt_pk_bf16_f32 v24, v24, v25\n\tv_min_u32 v24, -2, v24\n\t"
      "buffer_store_dword v24, v20, s[72:75], s30 offen sc1\n\t"
      "323:\n\t"
      RUN("0")
      ST(7)
      "v_add_u32 v20, s35, v4\n\t"
      P_LOAD(16, 0, 31) P_LOAD(20, 1, 31)
      ".irp i,0,1,2,3\n\tds_read_b128 v[32+4*\\i:35+4*\\i], v20 offset:1024*\\i\n\t.endr\n\t"
      RUN("0")
      ST(8)
      "3240:\n\t"
      "v_max3_u32 v80, v16, v17, v18\n\tv_max3_u32 v81, v19, v24, v25\n\tv_max3_u32 v80, v80, v26, v27\n\tv_max_u32 v80, v80, v81\n\t"
      "v_cmp_eq_u32 vcc, -1, v80\n\t"
      G_SPIN(324, "s_sleep 4\n\t" P_LOAD(16, 0, 31) P_LOAD(20, 1, 31) "s_waitcnt vmcnt(0)\n\t")
      "s_waitcnt lgkmcnt(0)\n\t"
      "v_mfma_f32_32x32x16_bf16 v[48:63], v[16:19], v[32:35], 0\n\t"
      "v_mfma_f32_32x32x16_bf16 v[64:79], v[16:19], v[40:43], 0\n\t"
      "v_mfma_f32_32x32x16_bf16 v[48:63], v[24:27], v[36:39], v[48:63]\n\t"
      "v_mfma_f32_32x32x16_bf16 v[64:79], v[24:27], v[44:47], v[64:79]\n\t"
      "v_sub_f32 v80, 1.0, v253\n\tv_sub_f32 v81, 1.0, v252\n\t"
      "v_mov_b32 v82, s86\n\tv_mov_b32 v83, s87\n\tv_mov_b32 v84, s36\n\t"
      "ds_write_b64 v84, v[82:83]\n\t"
      "s_nop 4\n\t"
      "v_add_f32 v56, 0x358637bd, v56\n\tv_rcp_f32 v56, v56\n\t"
      "v_add_f32 v72, 0x358637bd, v72\n\tv_rcp_f32 v72, v72\n\t"
      "v_mul_f32 v80, v80, v56\n\t"
      ".irp r,48,49,50,51,52,53,54,55\n\tv_mul_f32 v\\r, v80, v\\r\n\t.endr\n\t"
      "v_mul_f32 v81, v81, v72\n\t"
      ".irp i,0,1,2,3\n\tv_cvt_pk_bf16_f32 v[16+\\i], v[48+2*\\i], v[49+2*\\i]\n\t.endr\n\t"
      G_ST4(16, 4, "s26 offen")
      ".irp r,64,65,66,67,68,69,70,71\n\tv_mul_f32 v\\r, v81, v\\r\n\t.endr\n\t"
      ".irp i,0,1,2,3\n\tv_cvt_pk_bf16_f32 v[20+\\i], v[64+2*\\i], v[65+2*\\i]\n\t.endr\n\t"
      G_ST4(20, 4, "s27 offen")
      RUNV("0", STR(HALF_SWITCH))
      "v_cmp_eq_u32 vcc, 0, v4\n\tv_mov_b32 v84, s32\n\tv_bfrev_b32 v80, 15\n\t"
      "s_setprio 0\n\t"
      "v_cndmask_b32 v84, v80, v84, vcc\n\t"
      "buffer_store_dwordx2 v[82:83], v84, s[72:75], 0 offen sc1\n\t"
      "330:\n\t"
      ST(9)
      "v_and_b32 v20, 0x70, v4\n\tv_lshlrev_b32 v20, 3, v20\n\t"
      "buffer_load_dwordx2 v[22:23], v20, s[72:75], s33 offen sc1\n\t"
      RUN("0")
      ST(10)
      "v_mov_b32 v84, s36\n\ts_add_u32 s52, s34, 4096\n\t"
      "3310:\n\tds_read_b64 v[24:25], v84\n\ts_waitcnt lgkmcnt(0)\n\tv_cmp_ne_u64 vcc, s[86:87], v[24:25]\n\t" G_SPIN(331, "s_sleep 2\n\t")
      "3320:\n\tv_cmp_ne_u64 vcc, s[86:87], v[22:23]\n\t"
      G_SPIN(332, "s_sleep 4\n\tbuffer_load_dwordx2 v[22:23], v20, s[72:75], s33 offen sc1\n\ts_waitcnt vmcnt(0)\n\t")
      P_LOAD8(48, 34, 52)
      "v_add_u32 v6, s88, v4\n\tv_lshrrev_b32 v7, 2, v4\n\tv_add_u32 v7, s89, v7\n\t"
      "v_xor_b32 v8, 0x800, v6\n\tv_xor_b32 v9, 0x400, v7\n\tv_xor_b32 v1, 64, v0\n\t"
      "s_add_u32 s54, s34, 0x200000\n\ts_add_u32 s55, s34, 0x201000\n\t"
      RUN("0")
      ST(12)
      PV(176, 191, 244, 247, 152, 159)
      "ds_write_b128 v6, v[160:163]\n\tds_write_b128 v6, v[164:167] offset:1024\n\tds_write_b32 v7, v168\n\t"
      G_LBAR
      "ds_read_b32 v24, v9\n\tds_read_b128 v[16:19], v8\n\tds_read_b128 v[20:23], v8 offset:1024\n\t"
      "s_nop 3\n\t"
      "s_waitcnt lgkmcnt(2)\n\t"
      "v_add_f32 v24, v184, v24\n\tv_rcp_f32 v24, v24\n\t"
      "s_waitcnt lgkmcnt(0)\n\t"
      ".irp i,0,1,2,3,4,5,6,7\n\tv_add_f32 v[16+\\i], v[176+\\i], v[16+\\i]\n\t.endr\n\t"
      "v_mul_f32 v24, v253, v24\n\t"
      ".irp i,0,1,2,3,4,5,6,7\n\tv_mul_f32 v[16+\\i], v24, v[16+\\i]\n\t.endr\n\t"
      ".irp i,0,1,2,3\n\tv_cvt_pk_bf16_f32 v[84+\\i], v[16+2*\\i], v[17+2*\\i]\n\t.endr\n\t"
      ".irp i,0,1,2,3\n\tv_min_u32 v[84+\\i], -2, v[84+\\i]\n\t.endr\n\t"
      G_ST4(84, 4, "s38 offen")
      ST(13)
      P_RB(0) P_RB(1)
      P_USEB(0, 4, "0", "0", P_RB(2) P_RA(0) P_AT(0)) P_USEB(1, 8, G_A0, G_A1, P_RB(3) P_RA(1) P_AT(1))
      P_USEB(2, 12, G_A0, G_A1, P_RB(4) P_RA(2) P_AT(2)) P_USEB(3, 12, G_A0, G_A1, P_RB(5) P_RA(3) P_AT(3))
      P_USEB(4, 12, G_A0, G_A1, P_RB(6) P_RA(4) P_AT(4)) P_USEB(5, 12, G_A0, G_A1, P_RB(7) P_RA(5) P_AT(5))
      P_USEB(6, 12, G_A0, G_A1, P_RA(6) P_AT(6)) P_USEB(7, 8, G_A0, G_A1, P_RA(7) P_AT(7))
      "v_xor_b32 v6, 32, v248\n\tv_xor_b32 v7, 64, v248\n\tv_xor_b32 v8, 96, v248\n\t"
      "v_lshrrev_b32 v9, 7, v4\n\tv_bfe_u32 v10, v4, 3, 4\n\tv_xor_b32 v10, v10, v9\n\tv_lshlrev_b32 v10, 4, v10\n\t"
      "v_lshl_add_u32 v10, v9, 8, v10\n\tv_add_u32 v10, s90, v10\n\tv_xor_b32 v11, 16, v10\n\t"
      "v_and_b32 v12, 0x70, v4\n\tv_lshl_or_b32 v12, v9, 10, v12\n\tv_add_u32 v12, s91, v12\n\t"
      G_LBAR
      P_USEA(0, 7) P_USEA(1, 6) P_USEA(2, 5) P_USEA(3, 4) P_USEA(4, 3) P_USEA(5, 2) P_USEA(6, 1) P_USEA(7, 0)
      "s_nop 10\n\t"
      "ds_write_b128 v248, v[16:19]\n\tds_write_b128 v6, v[20:23]\n\tds_write_b128 v7, v[24:27]\n\tds_write_b128 v8, v[28:31]\n\t"
      "ds_write_b128 v248, v[32:35] offset:128\n\tds_write_b128 v6, v[36:39] offset:128\n\t"
      "ds_write_b128 v7, v[40:43] offset:128\n\tds_write_b128 v8, v[44:47] offset:128\n\t"
      ST(15)
      G_LBAR
      ".irp k,0,1,2,3\n\tds_read_b128 v[16+8*\\k:19+8*\\k], v10 offset:16384*\\k\n\tds_read_b128 v[20+8*\\k:23+8*\\k], v11 offset:16384*\\k\n\t.endr\n\t"
      "s_waitcnt lgkmcnt(4)\n\t"
      ".irp i,0,1,2,3,4,5,6,7\n\tv_add_f32 v[16+\\i], v[16+\\i], v[24+\\i]\n\t.endr\n\t"
      "s_waitcnt lgkmcnt(0)\n\t"
      ".irp i,0,1,2,3,4,5,6,7\n\tv_add_f32 v[32+\\i], v[32+\\i], v[40+\\i]\n\t.endr\n\t"
      ".irp i,0,1,2,3,4,5,6,7\n\tv_add_f32 v[16+\\i], v[16+\\i], v[32+\\i]\n\t.endr\n\t"
      ".irp i,0,1,2,3\n\tv_cvt_pk_bf16_f32 v[16+\\i], v[16+2*\\i], v[17+2*\\i]\n\t.endr\n\t"
      "buffer_store_dwordx4 v[16:19], v12, s[20:23], 0 offen" OUT_POL "\n\t"
      "v_cmp_eq_u32 vcc, 0, v4\n\tv_mov_b32 v20, s68\n\tv_bfrev_b32 v21, 15\n\tv_mov_b32 v22, s86\n\tv_mov_b32 v23, s87\n\t"
      "v_cndmask_b32 v20, v21, v20, vcc\n\t"
      "buffer_store_dwordx2 v[22:23], v20, s[72:75], 0 offen sc1\n\t"
      ST_END
      "s_branch 599f\n\t"
      P_STUBS(57, 56, 580) P_SLOW(580, 96, 54, 55)
      "599:\n\t"
      ".Lgm_code_to:"
      : "+{v[4:7]}"(xa), "+{v8}"(wa), "+{v[144:159]}"(vp), "+{v[10:15]}"(sr.wv), "+{s[24:27]}"(base) ST_OP
      : "{v0}"(pc.wo0), "{v248}"(pc.red), "{v249}"(pc.koff), "{v250}"(pc.voff0), "{v251}"(pc.voff1), "{v254}"(pc.wov), "{s[20:23]}"(pc.out),
        "{s37}"(w), "{s[40:43]}"(sr.ra), "{s[44:47]}"(sr.rb), "{s[48:51]}"(sr.rx), "{s[56:71]}"(sp), "{s[72:75]}"(pc.ws),
        "{s[80:83]}"(pc.wo), "{s91}"(outoff)
      : "memory", "scc", "vcc", "s28", "s29", "s30", "s31", "s32", "s33", "s34", "s35", "s36", "s38", "s39", "s52", "s53", "s54", "s55", "s76", "s77",
        "s84", "s85", "s88", "s89", "s90", "s78", "s79", "s86", "s87", "s92", "s93", "v1", "v2", "v3", "v9", "v252", "v253",
#define R8(a, b, c, d, e, f, g, h) "v" #a, "v" #b, "v" #c, "v" #d, "v" #e, "v" #f, "v" #g, "v" #h
        R8(16, 17, 18, 19, 20, 21, 22, 23), R8(24, 25, 26, 27, 28, 29, 30, 31), R8(32, 33, 34, 35, 36, 37, 38, 39),
        R8(40, 41, 42, 43, 44, 45, 46, 47), R8(48, 49, 50, 51, 52, 53, 54, 55), R8(56, 57, 58, 59, 60, 61, 62, 63),
        R8(64, 65, 66, 67, 68, 69, 70, 71), R8(72, 73, 74, 75, 76, 77, 78, 79), R8(80, 81, 82, 83, 84, 85, 86, 87),
        R8(88, 89, 90, 91, 92, 93, 94, 95), R8(96, 97, 98, 99, 100, 101, 102, 103), R8(104, 105, 106, 107, 108, 109, 110, 111),
        R8(112, 113, 114, 115, 116, 117, 118, 119), R8(120, 121, 122, 123, 124, 125, 126, 127), R8(128, 129, 130, 131, 132, 133, 134, 135),
        R8(136, 137, 138, 139, 140, 141, 142, 143), R8(160, 161, 162, 163, 164, 165, 166, 167), R8(168, 169, 170, 171, 172, 173, 174, 175),
        R8(176, 177, 178, 179, 180, 181, 182, 183), R8(184, 185, 186, 187, 188, 189, 190, 191), R8(192, 193, 194, 195, 196, 197, 198, 199),
        R8(200, 201, 202, 203, 204, 205, 206, 207), R8(208, 209, 210, 211, 212, 213, 214, 215), R8(216, 217, 218, 219, 220, 221, 222, 223),
        R8(224, 225, 226, 227, 228, 229, 230, 231), R8(232, 233, 234, 235, 236, 237, 238, 239), R8(240, 241, 242, 243, 244, 245, 246, 247) ST_CL);
}
DI void identity_seed(int r32, int h, unsigned& val, unsigned& hot) {
  val = 0x3F80u << ((r32 & 1) * 16);
  hot = (((r32 >> 2) & 1) == h ? 1u : 0u) << (((r32 >> 1) & 1) | ((r32 >> 2) & 6));
}

#define HG 2
#define ARGS_AT_ONCE 1
#define TLBALL 0
#define WARM 6
#define CODE_LINES 1
#define DBG_OFF 0u
#define WS_SIZE WS_END

struct Scalars { u32x16 sp; u32x4 base; unsigned outoff; };
DI Scalars scalars(unsigned b, unsigned w, unsigned lds0) {
  const unsigned xc = b & 7, in = b >> 3, g = HG * (xc % (8 / HG)) + in % HG, tb = (32 / HG) * (xc / (8 / HG)) + in / HG;
  const unsigned wi = w & 3, grp = w >> 2, pr = wi & 1, rot = (in / HG + (8 / HG) * (in % HG)) & 7;
  const unsigned hd = 4 * g + wi, otb = 4 * xc + (in & 3), og = in >> 2, rh = 4 * g + (tb & 3), sl = tb >> 2;
  Scalars s;
  s.sp = u32x16{grp, lds0 + L_RING + wi * 2048, lds0 + L_X + wi * 2048, rot, rot * 128, wi >> 1, tb * 2048, ((tb + 1) & 31) * 2048,
                ((tb + 2) & 31) * 2048, L_Q + (wi >> 1) * (L_VB - L_Q) + pr * 4096, L_Q + wi * 2048 + grp * 1024, L_PQ + pr * 8192,
                (b | w) ? OOB : WS_EPOCH, L_VB + pr * 4096, DBG_OFF, 0x3FB8AA3Bu  };
  s.base = u32x4{hd * 1024, rh * 8 + sl, ((otb * 2 + grp) * 32 + wi * 8) * 1024, 0u};
  s.outoff = (64 * otb + 8 * w) * 1024 + og * 128;
  return s;
}

__global__ __launch_bounds__(512, 2) void gated_mix_kernel(const char* __restrict__ x, const char* __restrict__ wqa,
                                                           const char* __restrict__ woa, const char* __restrict__ wqb,
                                                           const char* __restrict__ wphi, const char* __restrict__ wob,
                                                           const char* __restrict__ gatew, char* __restrict__ out,
                                                           char* __restrict__ ws) {
  __shared__ __attribute__((aligned(16))) char lds_raw[L_BYTES];
  LAS char* lds = (LAS char*)lds_raw;
  const unsigned lds0 = (unsigned)(uintptr_t)lds;
  const int tid = threadIdx.x, lane = tid & 63, w = __builtin_amdgcn_readfirstlane(tid >> 6);
  const int xc = blockIdx.x & 7, in = blockIdx.x >> 3;
  const int g = HG * (xc % (8 / HG)) + in % HG, tb = (32 / HG) * (xc / (8 / HG)) + in / HG;
  const int r32 = lane & 31, h = lane >> 5;
  const int wi = w & 3, grp = w >> 2, pr = wi & 1;
  asm volatile("" : "+s"(x), "+s"(wqa), "+s"(woa), "+s"(wqb), "+s"(wphi), "+s"(wob), "+s"(gatew), "+s"(out), "+s"(ws));
  STAMP(0); STAMP_RT(62);

  const int l8 = lane >> 3, dcp = lane & 7;
  Src sr;
  sr.ra = __builtin_amdgcn_make_buffer_rsrc((void*)wqa, 0, 512 * 3072, 0x00020000);
  sr.rx = __builtin_amdgcn_make_buffer_rsrc((void*)x, 0, 2048 * 1024, 0x00020000);
  const unsigned wv = (unsigned)(16 * wi + l8) * 3072 + g * 128 + ((dcp ^ ((lane >> 2) & 4)) * 16);
#pragma unroll
  for (int c = 0; c < 3; ++c) { sr.wv[2 * c] = wv + c * 1024; sr.wv[2 * c + 1] = wv + c * 1024 + 8 * 3072 - 1024; }
  const unsigned rot = (in / HG + (8 / HG) * (in % HG)) & 7;
  const unsigned xv0 = (unsigned)(64 * tb + 16 * wi + l8) * 1024 + ((dcp ^ (l8 >> 1)) * 16), xv1 = ((xv0 + 8192) ^ 64) - 1024;
  const unsigned ml = lds0 + L_RING + wi * 2048, mx = lds0 + L_X + wi * 2048;
  const unsigned rotx = rot * 128;
  if (grp) first_chunks(sr, xv0, xv1, ml, mx, rot, rotx);
  SBAR();
  const unsigned hd = 4 * g + wi, og = in >> 2;
  sr.rb = __builtin_amdgcn_make_buffer_rsrc((void*)wqb, 0, 512 * 3072, 0x00020000);
  const rsrc_t rws = __builtin_amdgcn_make_buffer_rsrc((void*)ws, 0, WS_SIZE, 0x00020000);
  if (grp == 0) {
    if (wi < 2) dma16(__builtin_amdgcn_make_buffer_rsrc((void*)wphi, 0, 32 * 512, 0x00020000), lane * 16, g * 2048 + wi * 1024, lds0 + L_WPHI + wi * 1024);
    if (wi == 2) dma16(__builtin_amdgcn_make_buffer_rsrc((void*)gatew, 0, 1024, 0x00020000), lane * 16, 0, lds0 + L_GW);
    {
      const unsigned sh = in / HG, nsh = 32 / HG;
      if (wi < 2 * HG / 2 + (HG == 1)) {
        const unsigned i = (sh * HG + wi % HG) * 64 + lane;
        const unsigned row = (i >> 1) & 511, sg = i & 1, st = i >> 10;
        const rsrc_t r = (st && !sg) ? sr.ra : sr.rb;
      }
      if (wi == 0) {
        asm volatile("s_mov_b32 m0, %0\n\ts_nop 0\n\tbuffer_load_dword %1, %2, 0 offen sc1 lds" :: "s"(lds0 + L_DUMMY), "v"((TLBALL ? lane + 64 * (in & 3) : in * 8 + l8) * 65536 + 0xFF80), "s"(rws) : "memory");
      }
      if (wi == 1) {
        const rsrc_t ro = __builtin_amdgcn_make_buffer_rsrc((void*)out, 0, 2048 * 1024, 0x00020000);
        asm volatile("s_mov_b32 m0, %0\n\ts_nop 0\n\tbuffer_load_dword %1, %2, 0 offen sc1 lds" :: "s"(lds0 + L_DUMMY), "v"((TLBALL ? (lane & 31) : in) * 65536 + 0xFF80), "s"(ro) : "memory");
      }
      if (wi == 2) {
        const unsigned i = blockIdx.x * 32 + (lane & 31);
        const rsrc_t r = __builtin_amdgcn_make_buffer_rsrc((void*)(blockIdx.x & 128 ? wob : woa), 0, 512 * 1024, 0x00020000);
        asm volatile("s_mov_b32 m0, %0\n\ts_nop 0\n\tbuffer_load_dword %1, %2, 0 offen lds" :: "s"(lds0 + L_DUMMY), "v"((i & 4095) * 128), "s"(r) : "memory");
      }
    }
    if (wi == 3) {
      unsigned long long pc;
      unsigned len;
      asm volatile("s_getpc_b64 %0\n.Lgm_code_from:\n\ts_mov_b32 %1, .Lgm_code_to-.Lgm_code_from" : "=s"(pc), "=s"(len));
      const rsrc_t rc = __builtin_amdgcn_make_buffer_rsrc((void*)pc, 0, len, 0x00020000);
      asm volatile("s_mov_b32 m0, %0\n\ts_nop 0\n\tbuffer_load_dword %1, %2, 0 offen lds" :: "s"(lds0 + L_DUMMY), "v"((in + 32 * l8) * 128), "s"(rc) : "memory");
    }
  }

  const unsigned kvb = WS_KV + (unsigned)hd * 131072, kvh = kvb + grp * 65536;
  Prog pc;
  pc.ws = rws;
  pc.koff = kvh + lane * 16;
  {
    const unsigned vreal = kvh + h * 256 + (lane & 15) * 16, vones = WS_ONES + g * 256;
    const bool pad = lane & 16, one = (lane & 11) == 0;
    pc.voff0 = pad ? (one ? vones - 1024 : OOB) : vreal; pc.voff1 = pad ? (one ? vones - 1536 : OOB) : vreal;
  }
  const int tq = (lane & 15) >> 2, tp_ = lane & 3, tnb = (lane >> 4) & 1;
  const unsigned wlane = L_RING + (8 * h + tq) * 128 + (((2 * pr + tnb) ^ (tq & 2)) * 32) + tp_ * 8;
  u32x4 xa;
#pragma unroll
  for (int s = 0; s < 4; ++s) xa[s] = L_X + r32 * 128 + (((2 * s + h) ^ ((r32 >> 1) & 7)) * 16);
  unsigned vp[16];
  if (grp == 0) {
    const unsigned kb = WS_KV + (unsigned)(4 * g + 2 * pr) * 131072 + (unsigned)(2 * tb) * 2048, fl = WS_FKV + (g * 32 + tb) * FL;
    const int sw = (r32 & 19) | ((r32 & 4) << 1) | ((r32 & 8) >> 1);
    vp[0] = vp[1] = vp[2] = vp[3] = vp[4] = 0;
    vp[5] = wi < 2 ? kb + h * 512 + sw * 16 : kb + (r32 >> 4) * 131072 + 1024 + h * 256 + (r32 & 15) * 16;
    vp[6] = wi < 2 ? (r32 == 0 ? fl + (2 * pr + h) * 16 + 8 : OOB) : (wi == 2 && lane < 8 ? WS_ONES + tb * 2048 + g * 256 + lane * 16 : OOB);
    vp[7] = w == 0 ? WS_KR + ((4 * g + (tb & 3)) * 8 + (tb >> 2)) * 256 + lane * 4 : OOB;
    vp[8] = L_X + r32 * 128 + (((2 * wi + h) ^ ((r32 >> 1) & 7)) * 16);
    vp[9] = L_GW + rotx + wi * 32 + h * 16;
    vp[10] = L_GP + wi * 512 + lane * 4;
    vp[11] = tid < 4 ? fl + lane * 16 : OOB;
  } else {
    vp[0] = xv0; vp[1] = xv1; vp[4] = vp[7] = vp[9] = vp[11] = 0;
    vp[2] = L_WPHI + 2 * pr * 512 + (4 * h + tq) * 32 + tp_ * 8;
    vp[3] = tnb ? 0x80008000u : 0u;
    vp[5] = WS_KP + (unsigned)(((4 * g + 2 * pr) * 8 + (lane >> 4)) * 32 + tb) * 256 + (lane & 15) * 16;
    vp[6] = lane == 0 ? WS_FKP + ((g * 2 + pr) * 32 + tb) * FL : OOB;
    vp[8] = tnb ? 0x3F800000u : 0u;
    vp[10] = L_GP + 256 + lane * 4;
  }
  {
    unsigned val, hot;
    identity_seed(r32, h, val, hot);
    vp[12] = val; vp[13] = hot; vp[14] = WS_FKV + (g * 32 + r32) * FL + wi * 16; vp[15] = lane * 16;
  }
  pc.out = __builtin_amdgcn_make_buffer_rsrc((void*)out, 0, 2048 * 1024, 0x00020000);
  pc.wo = __builtin_amdgcn_make_buffer_rsrc((void*)(grp ? woa : wob), 0, 512 * 1024, 0x00020000);
  pc.wov = (unsigned)(128 * wi + l8) * 1024 + og * 128 + ((dcp ^ ((lane >> 2) & 4)) * 16);
  pc.wo0 = L_WO + wi * 32768 + (unsigned)(4 * h + tq) * 128 + ((tnb ^ (tq & 2)) * 32) + tp_ * 8;
  pc.red = L_RED + wi * 16384 + (grp * 32 + r32) * 256 + ((h ^ (r32 & 7)) * 16);

  unsigned bi = blockIdx.x, wv_ = w;
  asm volatile("" : "+s"(bi), "+s"(wv_));
  const Scalars sc = scalars(bi, wv_, lds0);
  program(sr, xa, wlane + (wi >> 1) * 8192,
          u32x16{vp[0], vp[1], vp[2], vp[3], vp[4], vp[5], vp[6], vp[7], vp[8], vp[9], vp[10], vp[11], vp[12], vp[13], vp[14], vp[15]}, sc.sp,
          sc.base, wv_, sc.outoff, pc ST_ARG);
}

__global__ void pre_kernel(char* ws) {  }
}

extern "C" void kernel_launch(void* const* d_in, const int* in_sizes, int n_in, void* d_out, int out_size, void* d_ws,
                              size_t ws_size, hipStream_t stream) {
  pre_kernel<<<8, 64, 0, stream>>>((char*)d_ws);
  gated_mix_kernel<<<256, 512, 0, stream>>>((const char*)d_in[0], (const char*)d_in[1], (const char*)d_in[2], (const char*)d_in[4],
                                            (const char*)d_in[5], (const char*)d_in[6], (const char*)d_in[7], (char*)d_out, (char*)d_ws);
}
```

```cpp
#include <hip/hip_runtime.h>
#include <hip/hip_bf16.h>
#include <stdint.h>

namespace {

typedef __bf16 bf16x8 __attribute__((ext_vector_type(8)));
typedef __bf16 bf16x2 __attribute__((ext_vector_type(2)));
typedef float f32x16 __attribute__((ext_vector_type(16)));
typedef float f32x8 __attribute__((ext_vector_type(8)));
typedef float f32x4 __attribute__((ext_vector_type(4)));
typedef float f32x2 __attribute__((ext_vector_type(2)));
typedef unsigned u32x16 __attribute__((ext_vector_type(16)));
typedef unsigned u32x8 __attribute__((ext_vector_type(8)));
typedef unsigned u32x4 __attribute__((ext_vector_type(4)));
typedef unsigned u32x2 __attribute__((ext_vector_type(2)));
typedef short s16x4 __attribute__((ext_vector_type(4)));
typedef __amdgpu_buffer_rsrc_t rsrc_t;
#define LAS __attribute__((address_space(3)))
#define DI __device__ __forceinline__

constexpr unsigned WS_EPOCH = 0;
constexpr unsigned FL = 128;
constexpr unsigned WS_KR = 0x10000;
constexpr unsigned WS_ONES = 0x40000;
constexpr unsigned WS_FKV = 0x60000;
constexpr unsigned WS_FKP = 0x70000;
constexpr unsigned WS_FOB = 0x80000;
constexpr unsigned WS_KP = 0x100000;
constexpr unsigned WS_OB = 0x500000;
constexpr unsigned WS_OA = 0x700000;
constexpr unsigned WS_KV = 0x900000;
constexpr unsigned WS_END = 0xD00000;

constexpr unsigned L_X = 0;
constexpr unsigned L_RING = 65536;
constexpr unsigned L_WO = 0;
constexpr unsigned L_RED = 0;
constexpr unsigned L_PQ = 131072;
constexpr unsigned L_Q = 147456;
constexpr unsigned L_WPHI = L_Q;
constexpr unsigned L_GW = L_Q + 2048;
constexpr unsigned L_DUMMY = L_Q + 3072;
constexpr unsigned L_GP = L_Q + 4096;
constexpr unsigned L_VB = 155648;
constexpr unsigned L_BYTES = 163840;

constexpr int SC1 = 16;
constexpr unsigned OOB = 0xF0000000u;

DI void dma16(rsrc_t r, unsigned voff, unsigned soff, unsigned lds_dst) {
  asm volatile("s_mov_b32 m0, %0\n\ts_nop 0\n\tbuffer_load_dwordx4 %1, %2, %3 offen lds"
               :: "s"(lds_dst), "v"(voff), "s"(r), "s"(soff) : "memory");
}
#define SBAR() __builtin_amdgcn_sched_barrier(0)

#define EX(r) "v_exp_f32 v" #r ", v" #r "\n\t"
#define QK(s0, s1, k0, k1, q0, q1, c0, c1) "v_mfma_f32_32x32x16_bf16 v[" #s0 ":" #s1 "], v[" #k0 ":" #k1 "], v[" #q0 ":" #q1 "], v[" #c0 ":" #c1 "]\n\t"
#define PV(o0, o1, a0, a1, p0, p1) "v_smfmac_f32_32x32x32_bf16 v[" #o0 ":" #o1 "], v[" #a0 ":" #a1 "], v[" #p0 ":" #p1 "], v5\n\t"
#define LOADS(k0, k1, a0, a1, b0, b1)                                                     \
  "s_add_u32 s76, s76, 0x800\n\t"                                                         \
  "s_and_b32 s76, s76, 0xffff\n\t"                                                        \
  "buffer_load_dwordx4 v[" #k0 ":" #k1 "], v249, s[72:75], s76 offen sc1\n\t"             \
  "buffer_load_dwordx4 v[" #a0 ":" #a1 "], v250, s[72:75], s76 offen offset:1024 sc1\n\t" \
  "buffer_load_dwordx4 v[" #b0 ":" #b1 "], v251, s[72:75], s76 offen offset:1536 sc1\n\t"
#define WODMA                                                                             \
  "s_mov_b32 m0, s84\n\t"                                                                 \
  "s_add_u32 s84, s84, 0x400\n\t"                                                         \
  "buffer_load_dwordx4 v254, s[80:83], s85 offen lds\n\t"                                 \
  "s_add_u32 s85, s85, 0x2000\n\t"
#define ITER(ck0, ck1, ca0, ca1, cb0, cb1, pk0, pk1, pa0, pa1, pb0, pb1, nk0, nk1, EXTRA, WAIT) \
  EX(128) EX(129) PV(176, 191, pb0, pb1, 152, 159) EX(130) EX(131)                        \
  LOADS(pk0, pk1, pa0, pa1, pb0, pb1)                                                     \
  EX(132) EX(133) QK(144, 159, ck0, ck1, 196, 199, 112, 127) EX(134) EX(135) EXTRA EX(136) EX(137) EX(138) \
  PV(160, 175, ca0, ca1, 128, 135) EX(139) EX(140) EX(141) EX(142) EX(143)                 \
  EX(144) EX(145) PV(160, 175, cb0, cb1, 136, 143) EX(146) EX(147) EX(148) EX(149)         \
  WAIT                                                                                    \
  QK(128, 143, nk0, nk1, 192, 195, 96, 111) EX(150) EX(151) EX(152) EX(153) EX(154)      \
  PV(176, 191, ca0, ca1, 144, 151) EX(155) EX(156) EX(157) EX(158) EX(159)

#define ATTN_ROUTINE                                                                                  \
  "400:\n\t"                                                                                          \
  ITER(200, 203, 216, 219, 220, 223, 212, 215, 240, 243, 244, 247, 204, 207, WODMA, "")               \
  ITER(204, 207, 224, 227, 228, 231, 200, 203, 216, 219, 220, 223, 208, 211, "", "")                  \
  "s_cbranch_vccnz 402f\n\t401:\n\t"                                                                 \
  ITER(208, 211, 232, 235, 236, 239, 204, 207, 224, 227, 228, 231, 212, 215, WODMA, "s_waitcnt vmcnt(8)\n\t") \
  ITER(212, 215, 240, 243, 244, 247, 208, 211, 232, 235, 236, 239, 200, 203, "", "s_waitcnt vmcnt(7)\n\t")    \
  "s_sub_u32 s77, s77, 1\n\ts_waitcnt vmcnt(0)\n\ts_cbranch_scc0 400b\n\ts_setpc_b64 s[78:79]\n\t"  \
  "402:\n\ts_setprio 0\n\ts_branch 401b\n\t"
#define RUNV(N, V) "s_mov_b64 vcc, " V "\n\ts_mov_b32 s77, " N "\n\ts_swappc_b64 s[78:79], s[92:93]\n\t"
#define RUN(N) RUNV(N, "0")

#define STAMP(i) do { } while (0)
#define STAMP_RT(i) do { } while (0)
#define ST(i) ""
#define ST_END ""
#define ST_OP
#define ST_CL
#define ST_PAR
#define ST_ARG

#define G_BAR(N) "s_waitcnt vmcnt(" #N ") lgkmcnt(0)\n\ts_barrier\n\t"
#define G_LBAR "s_waitcnt lgkmcnt(0)\n\ts_barrier\n\t"
#define G_EPOCH "buffer_load_dwordx2 v[2:3], off, s[72:75], 0 sc1\n\t"
#define G_EPOCH_USE "v_readfirstlane_b32 s86, v2\n\tv_readfirstlane_b32 s87, v3\n\ts_add_u32 s86, s86, 1\n\ts_addc_u32 s87, s87, 0\n\t"
#define D_ROW(KC) "s_xor_b32 s52, s59, " KC "\n\ts_mul_i32 s52, s52, 196608\n\t"
#define D_A(R, COL, LOFF) "s_add_u32 m0, s57, " LOFF "\n\ts_nop 0\n\tbuffer_load_dwordx4 v[10+2*" #COL "], " R ", s52 offen lds\n\t"
#define D_B(R, COL) "buffer_load_dwordx4 v[11+2*" #COL "], " R ", s52 offen offset:1024 lds\n\t"
#define RA "s[40:43]"
#define RB "s[44:47]"
#define D_X(C)                                                                                        \
  "s_add_u32 m0, s58, 8192*" #C "\n\ts_xor_b32 s52, s60, 128*" #C "\n\t"                              \
  "buffer_load_dwordx4 v144, s[48:51], s52 offen lds\n\tbuffer_load_dwordx4 v145, s[48:51], s52 offen offset:1024 lds\n\t"
#define D_W(N, R0, C0, R1, C1) D_ROW(#N "&7") D_A(R0, C0, "16384*(" #N "&3)"), D_B(R0, C0), D_A(R1, C1, "16384*(" #N "&3)+8192"), D_B(R1, C1)
#define D_S0(N) D_W(N, RA, 1, RA, 2)
#define D_S1(N) D_W(N, RB, 0, RB, 1)
#define D_S2(N) D_W(N, RA, 0, RB, 2)
#define G_CAT(A, B, C, D) A B C D
#define G_APPLY(M, ...) M(__VA_ARGS__)
#define G_ALL(L) G_APPLY(G_CAT, L)

#define G_RW(W, C, S)                                                                                 \
  "ds_read_b64_tr_b16 v[" #W "+4*" #S ":" #W "+4*" #S "+1], v8 offset:16384*(" #C "&3)+2048*" #S "\n\t"       \
  "ds_read_b64_tr_b16 v[" #W "+4*" #S "+2:" #W "+4*" #S "+3], v8 offset:16384*(" #C "&3)+2048*" #S "+512\n\t"
#define G_RX(X, C, S, T) "ds_read_b128 v[" #X "+4*" #S ":" #X "+4*" #S "+3], v[4+" #S "] offset:8192*" #C "+4096*" #T "\n\t"
#define G_RS(W, X0, X1, C, S) G_RW(W, C, S) G_RX(X0, C, S, 0) G_RX(X1, C, S, 1)
#define G_READ(W, X0, X1, C) G_RS(W, X0, X1, C, 0) G_RS(W, X0, X1, C, 1) G_RS(W, X0, X1, C, 2) G_RS(W, X0, X1, C, 3)
#define G_M(ACC, W, X, S, CIN) \
  "v_mfma_f32_32x32x16_bf16 " ACC ", v[" #W "+4*" #S ":" #W "+4*" #S "+3], v[" #X "+4*" #S ":" #X "+4*" #S "+3], " CIN "\n\t"
#define G_A0 "v[16:31]"
#define G_A1 "v[32:47]"
#define G_MMA2(W, X0, X1, S) G_M(G_A0, W, X0, S, G_A0) G_M(G_A1, W, X1, S, G_A1)
#define G_STEP(RW, RX0, RX1, C, W, X0, X1, C0, C1, D0, D1, D2, D3)                                    \
  G_M(G_A0, W, X0, 0, C0) G_RW(RW, C, 0) G_RX(RX0, C, 0, 0) G_M(G_A1, W, X1, 0, C1) G_RX(RX1, C, 0, 1) G_RW(RW, C, 1) \
  G_M(G_A0, W, X0, 1, G_A0) G_RX(RX0, C, 1, 0) G_RX(RX1, C, 1, 1) D0 G_M(G_A1, W, X1, 1, G_A1) G_RW(RW, C, 2) G_RX(RX0, C, 2, 0) \
  G_M(G_A0, W, X0, 2, G_A0) G_RX(RX1, C, 2, 1) G_RW(RW, C, 3) D1 G_M(G_A1, W, X1, 2, G_A1) G_RX(RX0, C, 3, 0) G_RX(RX1, C, 3, 1) \
  G_M(G_A0, W, X0, 3, G_A0) D2 G_M(G_A1, W, X1, 3, G_A1) D3
#define G_EVEN(C, D0, D1, D2, D3) G_STEP(48, 64, 80, C, 96, 112, 128, G_A0, G_A1, D0, D1, D2, D3)
#define G_ODD(C, D0, D1, D2, D3) G_STEP(96, 112, 128, C, 48, 64, 80, G_A0, G_A1, D0, D1, D2, D3)
#define G_FIRST(D0, D1, D2, D3) G_STEP(96, 112, 128, 1, 48, 64, 80, "0", "0", D0, D1, D2, D3)
#define A_PACK                                                                                        \
  ".irp i,0,1,2,3\n\tv_cvt_pk_bf16_f32 v[48+\\i], v[16+2*\\i], v[17+2*\\i]\n\tv_cvt_pk_bf16_f32 v[56+\\i], v[24+2*\\i], v[25+2*\\i]\n\t.endr\n\t" \
  ".irp i,0,1,2,3\n\tv_cvt_pk_bf16_f32 v[52+\\i], v[32+2*\\i], v[33+2*\\i]\n\tv_cvt_pk_bf16_f32 v[60+\\i], v[40+2*\\i], v[41+2*\\i]\n\t.endr\n\t"
#define G_Z(L) ".irp r," L "\n\tv_mov_b64 v[\\r:\\r+1], 0\n\t.endr\n\t"
#define G_ZO G_Z("160,162,164,166,168,170,172,174,176,178,180,182,184,186,188,190,244,246")
#define G_QPRE "v_add_u32 v6, s66, v159\n\tv_mov_b32 v4, v159\n\tv_xor_b32 v7, 0x400, v6\n\tv_mov_b32 v5, 0xdddddddd\n\t"
#define G_QRD "ds_read_b128 v[192:195], v7\n\tds_read_b128 v[196:199], v6\n\t"
#define G_C(L) ".irp r," L "\n\tv_mov_b32 v\\r, 0xc2400000\n\t.endr\n\t"
#define G_C0 G_C("96,97,98,99,100,101,102,103,104,105,106,107,108,109,110,111")
#define G_C1 G_C("112,113,114,115,116,117,118,119,120,121,122,123,124,125,126,127")
#define G_ST4(R, V, O) "buffer_store_dwordx4 v[" #R ":" #R "+3], v" #V ", s[72:75], " O " sc1\n\t"
#define G_POLL(R) "buffer_load_dwordx3 v[" #R ":" #R "+2], v158, s[72:75], 0 offen sc1\n\t"
#define G_SPIN(L, AGAIN) "s_cbranch_vccz " #L "1f\n\t" AGAIN "s_sub_u32 s53, s53, 1\n\ts_cbranch_scc0 " #L "0b\n\ts_mov_b32 s53, 0\n\t" #L "1:\n\t"
#define G_POLLWAIT(L, R)                                                                              \
  #L "0:\n\ts_waitcnt vmcnt(0)\n\tv_cmp_ne_u64 vcc, s[86:87], v[" #R ":" #R "+1]\n\t"                  \
  G_SPIN(L, "s_sleep 4\n\t" G_POLL(R)) "s_waitcnt vmcnt(0)\n\tv_mov_b32 v9, v[" #R "+2]\n\t"
#define G_KV1(K, V, T)                                                                                \
  "buffer_load_dwordx4 v[" #K ":" #K "+3], v249, s[72:75], s" #T " offen sc1\n\t"                      \
  "buffer_load_dwordx4 v[" #V ":" #V "+3], v250, s[72:75], s" #T " offen offset:1024 sc1\n\t"          \
  "buffer_load_dwordx4 v[" #V "+4:" #V "+7], v251, s[72:75], s" #T " offen offset:1536 sc1\n\t"
#define G_KV G_KV1(200, 216, 62) G_KV1(204, 224, 63) G_KV1(208, 232, 64)
#define DPPMAX(R, CTRL) "v_max_f32_dpp v" #R ", v" #R ", v" #R " " CTRL " row_mask:0xf bank_mask:0xf\n\t"
#define SIGMOID(R, OUT) \
  "v_mul_f32 v" #R ", 0xbfb8aa3b, v" #R "\n\tv_exp_f32 v" #R ", v" #R "\n\ts_nop 0\n\tv_add_f32 v" #R ", 1.0, v" #R "\n\tv_rcp_f32 v" #OUT ", v" #R "\n\t"
#define A_GREAD(C)                                                                                    \
  "v_xor_b32 v92, 128*" #C ", v153\n\tv_xor_b32 v93, 128*(" #C "+1), v153\n\t"                     \
  "ds_read_b128 v[72:75], v152 offset:8192*" #C "\n\tds_read_b128 v[80:83], v152 offset:8192*" #C "+4096\n\t" \
  "ds_read_b128 v[76:79], v152 offset:8192*(" #C "+1)\n\tds_read_b128 v[84:87], v152 offset:8192*(" #C "+1)+4096\n\t" \
  "ds_read_b128 v[64:67], v92\n\tds_read_b128 v[68:71], v93\n\t"
#define A_GDOT                                                                                        \
  ".irp j,0,1,2,3\n\t"                                                                                \
  "v_dot2c_f32_bf16 v88, v[72+\\j], v[64+\\j]\n\tv_dot2c_f32_bf16 v89, v[80+\\j], v[64+\\j]\n\t"       \
  "v_dot2c_f32_bf16 v90, v[76+\\j], v[68+\\j]\n\tv_dot2c_f32_bf16 v91, v[84+\\j], v[68+\\j]\n\t"       \
  ".endr\n\t"
#define A_DPPMAX(CTRL) "s_nop 1\n\tv_max_f32_dpp v96, v96, v96 " CTRL " row_mask:0xf bank_mask:0xf\n\t"
#define F_JOBS(M) M(16, 80, 48) M(32, 84, 56) M(96, 88, 64) M(112, 92, 72)
#define F_MAX1(E, T, P)                                                                               \
  "v_max3_f32 v[" #T "], v[" #E "], v[" #E "+1], v[" #E "+2]\n\t"                                      \
  "v_max3_f32 v[" #T "+2], v[" #E "+8], v[" #E "+9], v[" #E "+10]\n\t"                                 \
  "v_max3_f32 v[" #T "+1], v[" #E "+3], v[" #E "+4], v[" #E "+5]\n\t"                                  \
  "v_max3_f32 v[" #T "+3], v[" #E "+11], v[" #E "+12], v[" #E "+13]\n\t"                               \
  "v_max3_f32 v[" #T "], v[" #T "], v[" #E "+6], v[" #E "+7]\n\t"                                      \
  "v_max3_f32 v[" #T "+2], v[" #T "+2], v[" #E "+14], v[" #E "+15]\n\t"                                \
  "v_max_f32 v[" #T "], v[" #T "], v[" #T "+1]\n\t"                                                    \
  "v_max_f32 v[" #T "+2], v[" #T "+2], v[" #T "+3]\n\t"
#define F_SWAP(E, T, P) "v_permlane32_swap_b32 v[" #T "], v[" #T "+2]\n\t"
#define F_MAX2(E, T, P) "v_max_f32 v[" #T "], v[" #T "], v[" #T "+2]\n\t"
#define F_NEG(E, T, P) "v_mul_f32 v[" #T "], 0xbfb8aa3b, v[" #T "]\n\t"
#define F_DUP(E, T, P) "v_mov_b32 v[" #T "+2], v[" #T "]\n\t"
#define F_FMA(E, T)                                                                                   \
  ".irp i,0,1,2,3,4,5,6,7\n\t"                                                                        \
  "v_fma_f32 v[" #E "+\\i], v[" #E "+\\i], s71, v[" #T "]\n\t"                                      \
  "v_fma_f32 v[" #E "+8+\\i], v[" #E "+8+\\i], s71, v[" #T "+2]\n\t"                                \
  ".endr\n\t"
#define F_EX(E, L) ".irp i," L "\n\tv_exp_f32 v[" #E "+\\i], v[" #E "+\\i]\n\t.endr\n\t"
#define F_EXP(E, T) F_FMA(E, T) F_EX(E, "0,8,1,9,2,10,3,11,4,12,5,13,6,14,7,15")
#define F_SUM1(E, T, P) ".irp i,0,1,2,3,4,5,6,7\n\tv_add_f32 v[" #P "+\\i], v[" #E "+2*\\i], v[" #E "+2*\\i+1]\n\t.endr\n\t"
#define F_SUM2(E, T, P) ".irp i,0,2,4,6\n\tv_add_f32 v[" #P "+\\i], v[" #P "+\\i], v[" #P "+\\i+1]\n\t.endr\n\t"
#define F_SUM3(E, T, P) "v_add_f32 v[" #T "], v[" #P "], v[" #P "+2]\n\tv_add_f32 v[" #T "+2], v[" #P "+4], v[" #P "+6]\n\t"
#define F_ADD(E, T, P) "v_add_f32 v[" #T "], v[" #T "], v[" #T "+2]\n\t"
#define F_RCP(E, T, P) "v_rcp_f32 v[" #T "], v[" #T "]\n\t"
#define F_PACK(E, T, P) ".irp i,0,1,2,3,4,5,6,7\n\tv_cvt_pk_bf16_f32 v[" #P "+\\i], v[" #E "+2*\\i], v[" #E "+2*\\i+1]\n\t.endr\n\t"
#define F_SCALE(E, T, P)                                                                              \
  ".irp i,0,1,2,3,4,5,6,7\n\t"                                                                        \
  "v_mul_f32 v[" #E "+\\i], v[" #E "+\\i], v[" #T "]\n\t"                                              \
  "v_mul_f32 v[" #E "+8+\\i], v[" #E "+8+\\i], v[" #T "+2]\n\t"                                        \
  ".endr\n\t" F_PACK(E, T, P)
#define F_TR0(E, T, P) "v_mfma_f32_32x32x16_bf16 v[" #E ":" #E "+15], v[" #P ":" #P "+3], v[128:131], 0\n\t"
#define F_TR1(E, T, P) "v_mfma_f32_32x32x16_bf16 v[" #E ":" #E "+15], v[" #P "+4:" #P "+7], v[132:135], v[" #E ":" #E "+15]\n\t"
#define P_TEST(F, I)                                                                                  \
  "v_max3_u32 v80, v[" #F "+4*" #I "], v[" #F "+1+4*" #I "], v[" #F "+2+4*" #I "]\n\t"                 \
  "v_max_u32 v80, v80, v[" #F "+3+4*" #I "]\n\t"                                                       \
  "v_cmp_eq_u32 vcc, -1, v80\n\t"
#define P_LOAD(F, I, SO) "buffer_load_dwordx4 v[" #F "+4*" #I ":" #F "+3+4*" #I "], v4, s[72:75], s" #SO " offen offset:1024*(" #I "&3) sc1\n\t"
#define P_RD(R, I, OFF)                                                                               \
  "ds_read_b64_tr_b16 v[" R ":" R "+1], v0 offset:" #OFF "+2048*" #I "\n\t"                         \
  "ds_read_b64_tr_b16 v[" R "+2:" R "+3], v0 offset:" #OFF "+2048*" #I "+1024\n\t"                  \
  "ds_read_b64_tr_b16 v[" R "+4:" R "+5], v1 offset:" #OFF "+2048*" #I "\n\t"                       \
  "ds_read_b64_tr_b16 v[" R "+6:" R "+7], v1 offset:" #OFF "+2048*" #I "+1024\n\t"
#define P_RB(I) P_RD("192+8*(" #I "&3)", I, 0)
#define P_RA(I) P_RD("128+8*" #I, I, 16384)
#define P_MM(W, F, I, C0, C1)                                                                         \
  "v_mfma_f32_32x32x16_bf16 v[16:31], v[" W ":" W "+3], v[" #F "+4*" #I ":" #F "+3+4*" #I "], " C0 "\n\t"   \
  "v_mfma_f32_32x32x16_bf16 v[32:47], v[" W "+4:" W "+7], v[" #F "+4*" #I ":" #F "+3+4*" #I "], " C1 "\n\t"
#define P_USEB(I, N, C0, C1, MORE)                                                                    \
  "s_waitcnt lgkmcnt(" #N ")\n\t" P_MM("192+8*(" #I "&3)", 48, I, C0, C1) MORE
#define P_USEA(I, N) "s_waitcnt vmcnt(" #N ")\n\t" P_TEST(96, I) "s_cbranch_vccnz 57" #I "f\n\t56" #I ":\n\t" P_MM("128+8*" #I, 96, I, G_A0, G_A1)
#define P_STUB(A, B, I, R) #A #I ":\n\ts_call_b64 s[78:79], " #R "f\n\ts_branch " #B #I "b\n\t"
#define P_STUBS(A, B, R) P_STUB(A, B, 0, R) P_STUB(A, B, 1, R) P_STUB(A, B, 2, R) P_STUB(A, B, 3, R) P_STUB(A, B, 4, R) P_STUB(A, B, 5, R) P_STUB(A, B, 6, R) P_STUB(A, B, 7, R)
#define P_RETRY(F, I, SO) P_TEST(F, I) "s_cbranch_vccz 1f\n\ts_mov_b32 s52, 1\n\t" P_LOAD(F, I, SO) "1:\n\t"
#define P_SLOW(R, F, S0, S1)                                                                          \
  #R ":\n\ts_waitcnt vmcnt(0)\n\ts_mov_b32 s52, 0\n\t"                                             \
  P_RETRY(F, 0, S0) P_RETRY(F, 1, S0) P_RETRY(F, 2, S0) P_RETRY(F, 3, S0) P_RETRY(F, 4, S1) P_RETRY(F, 5, S1) P_RETRY(F, 6, S1) P_RETRY(F, 7, S1) \
  "s_sub_u32 s53, s53, 1\n\ts_cselect_b32 s52, 0, s52\n\ts_cselect_b32 s53, 0, s53\n\t" \
  "s_cmp_lg_u32 s52, 0\n\ts_cbranch_scc1 " #R "b\n\ts_waitcnt vmcnt(0)\n\ts_setpc_b64 s[78:79]\n\t"
#define P_LOAD8(F, S0, S1) P_LOAD(F, 0, S0) P_LOAD(F, 1, S0) P_LOAD(F, 2, S0) P_LOAD(F, 3, S0) P_LOAD(F, 4, S1) P_LOAD(F, 5, S1) P_LOAD(F, 6, S1) P_LOAD(F, 7, S1)
#define P_LOADA P_LOAD8(96, 54, 55)
#define OUT_POL " sc1"
#define HALF_SWITCH 0
#define A_AT 7
#define P_AT(I) ".if " STR(A_AT) " == " #I "\n\t" P_LOADA ".endif\n\t"
#define STR_(x) #x
#define STR(x) STR_(x)
typedef unsigned u32x6 __attribute__((ext_vector_type(6)));
struct Src { u32x6 wv; rsrc_t ra, rb, rx; };
#define M_PAR0                                                                                        \
  "s_add_u32 s34, s26, 0x500000\n\ts_lshl_b32 s33, s56, 15\n\ts_sub_u32 s33, s26, s33\n\ts_lshr_b32 s33, s33, 3\n\ts_add_u32 s33, s33, 0x80000\n\t" \
  "s_lshl_b32 s29, s25, 13\n\ts_add_u32 s29, s29, 0x100000\n\ts_lshl_b32 s30, s25, 8\n\ts_add_u32 s30, s30, 0x10000\n\t" \
  "s_lshr_b32 s28, s25, 4\n\ts_lshl_b32 s28, s28, 12\n\ts_add_u32 s28, s28, 0x70000\n\t"
#define M_PAR1                                                                                        \
  "s_lshl_b32 s31, s24, 1\n\ts_add_u32 s31, s31, 0x10000\n\ts_lshl_b32 s39, s24, 7\n\ts_add_u32 s39, s39, 0x900000\n\t" \
  "s_lshl_b32 s32, s62, 5\n\ts_add_u32 s32, s32, s24\n\ts_add_u32 s24, s32, 0x700000\n\ts_add_u32 s25, s32, 0x708000\n\t" \
  "s_add_u32 s26, s32, 0x500000\n\ts_add_u32 s27, s32, 0x508000\n\ts_lshr_b32 s32, s32, 3\n\ts_add_u32 s32, s32, 0x80000\n\t"
#define M_PAR2                                                                                        \
  "s_lshl_b32 s38, s56, 15\n\ts_add_u32 s38, s38, s24\n\ts_lshl_b32 s35, s58, 1\n\ts_add_u32 s35, s35, 0x20000\n\t" \
  "s_add_u32 s36, s58, 0x26200\n\ts_lshl_b32 s88, s56, 11\n\ts_add_u32 s88, s88, s35\n\ts_lshl_b32 s89, s56, 10\n\t" \
  "s_add_u32 s89, s89, s58\n\ts_add_u32 s89, s89, 0x26000\n\ts_lshl_b32 s90, s37, 11\n\t"              \
  "s_mov_b32 s76, s64\n\ts_lshl_b32 s84, s58, 4\n\ts_lshl_b32 s85, s56, 14\n\ts_add_u32 s84, s84, s85\n\ts_mov_b32 s85, 0\n\t"
static_assert(WS_OB == 0x500000 && WS_OA == 0x700000 && WS_FOB == 0x80000 && WS_KP == 0x100000 && WS_KR == 0x10000 && WS_FKP == 0x70000 &&
              WS_KV == 0x900000 && L_PQ == 0x20000 && L_VB == 0x26000 && L_X == 0 && L_WO == 0 && L_RED == 0 && FL == 128, "literals above");
DI void first_chunks(const Src& sr, unsigned xv0, unsigned xv1, unsigned ml, unsigned mx, unsigned rot, unsigned rotx) {
  asm volatile(D_X(0) G_ALL(D_S0(0)) D_X(1) G_ALL(D_S0(1))
               :: "{v[10:15]}"(sr.wv), "{v144}"(xv0), "{v145}"(xv1), "{s[40:43]}"(sr.ra), "{s[48:51]}"(sr.rx), "{s57}"(ml), "{s58}"(mx),
                  "{s59}"(rot), "{s60}"(rotx)
               : "memory", "scc", "s52");
}
struct Prog { unsigned wo0, red, koff, voff0, voff1, wov; rsrc_t out, ws, wo; };
DI void program(Src sr, u32x4 xa, unsigned wa, u32x16 vp, u32x16 sp, u32x4 base, unsigned w, unsigned outoff, const Prog& pc ST_PAR) {
  asm volatile(
      "s_getpc_b64 s[92:93]\n\t"
      "99:\n\t"
      "s_add_u32 s92, s92, 400f-99b\n\ts_addc_u32 s93, s93, 0\n\t"
      "s_mov_b32 s53, 0x40000\n\t"
      "s_cmp_eq_u32 s56, 0\n\t"
      "s_cbranch_scc1 100f\n\t"
      G_BAR(6) D_X(2) G_ALL(D_S0(2)) G_BAR(6) D_X(3) G_ALL(D_S0(3)) G_BAR(6) D_X(4) G_ALL(D_S0(4)) M_PAR0
      G_BAR(6) D_X(5) G_ALL(D_S0(5)) M_PAR1 G_BAR(6) D_X(6) G_ALL(D_S0(6)) M_PAR2 G_BAR(6) D_X(7) G_ALL(D_S0(7))
      G_BAR(6) G_ALL(D_S1(8)) G_ALL(D_S1(9)) G_BAR(8) G_ALL(D_S1(10))
      "s_branch 200f\n\t"
      "100:\n\t"
      G_EPOCH
      G_Z("16,18,20,22,24,26,28,30,32,34,36,38,40,42,44,46")
      "s_mov_b32 s52, 0\n\t"
      G_LBAR G_READ(48, 64, 80, 0)
      "s_branch 102f\n\t"
      "101:\n\t"
      G_BAR(0) G_EVEN(0, "", "", "", "")
      "102:\n\t"
      G_LBAR G_ODD(1, "", "", "", "") G_LBAR G_EVEN(2, "", "", "", "") G_LBAR G_ODD(3, "", "", "", "")
      ".irp r,4,5,6,7\n\tv_xor_b32 v\\r, 0x8000, v\\r\n\t.endr\n\t"
      "s_xor_b32 s52, s52, 1\n\t"
      "s_cbranch_scc1 101b\n\t"
      ST(1)
      G_LBAR
      G_MMA2(96, 112, 128, 0) G_MMA2(96, 112, 128, 1) G_MMA2(96, 112, 128, 2) G_MMA2(96, 112, 128, 3)
      G_EPOCH_USE G_Z("88,90,92,94")
      "s_cmp_lg_u32 s61, 0\n\t"
      "s_cbranch_scc1 110f\n\t"
      "s_nop 0\n\t"
      ".irp r,16,17,18,19,20,21,22,23,24,25,26,27,28,29,30,31\n\tv_mul_f32 v\\r, 0x3eb8aa3b, v\\r\n\t.endr\n\t"
      G_LBAR
      ".irp r,32,33,34,35,36,37,38,39,40,41,42,43,44,45,46,47\n\tv_mul_f32 v\\r, 0x3eb8aa3b, v\\r\n\t.endr\n\t"
      A_PACK
      G_ST4(48, 149, "0 offen") G_ST4(52, 149, "0 offen offset:2048")
      ".irp j,0,1,2,3\n\t"
      "v_dot2c_f32_bf16 v92, v[48+\\j], v[48+\\j]\n\tv_dot2c_f32_bf16 v93, v[52+\\j], v[52+\\j]\n\t"
      "v_dot2c_f32_bf16 v94, v[56+\\j], v[56+\\j]\n\tv_dot2c_f32_bf16 v95, v[60+\\j], v[60+\\j]\n\t"
      ".endr\n\t"
      "s_mov_b32 s54, 131072\n\t"
      G_LBAR
      G_ST4(56, 149, "s54 offen") G_ST4(60, 149, "s54 offen offset:2048")
      "v_permlane32_swap_b32 v92, v94\n\tv_permlane32_swap_b32 v93, v95\n\t"
      "v_add_f32 v96, v92, v94\n\tv_add_f32 v97, v93, v95\n\t"
      A_GREAD(0)
      "v_max_f32 v96, v96, v97\n\t"
      A_DPPMAX("quad_perm:[1,0,3,2]") A_DPPMAX("quad_perm:[2,3,0,1]") A_DPPMAX("row_half_mirror") A_DPPMAX("row_mirror")
      "v_mov_b32 v97, v96\n\tv_mov_b32 v100, -1\n\ts_nop 0\n\tv_permlane16_swap_b32 v96, v97\n\tv_max_f32 v96, v96, v97\n\t"
      "buffer_store_dword v96, v150, s[72:75], 0 offen sc1\n\t"
      "buffer_store_dword v100, v151, s[72:75], 0 offen sc1\n\t"
      "s_branch 111f\n\t"
      "110:\n\t"
      ".irp j,0,1,2,3,4,5,6,7\n\tv_bfe_i32 v[96+\\j], v157, \\j, 1\n\t.endr\n\t"
      A_PACK
      ".irp j,0,1,2,3,4,5,6,7\n\tv_and_b32 v[96+\\j], v156, v[96+\\j]\n\t.endr\n\t"
      G_LBAR
      "s_setprio 2\n\t"
      "v_mfma_f32_32x32x16_bf16 v[16:31], v[48:51], v[96:99], 0\n\t"
      "v_mfma_f32_32x32x16_bf16 v[32:47], v[52:55], v[96:99], 0\n\t"
      "v_mfma_f32_32x32x16_bf16 v[16:31], v[56:59], v[100:103], v[16:31]\n\t"
      "v_mfma_f32_32x32x16_bf16 v[32:47], v[60:63], v[100:103], v[32:47]\n\t"
      "s_setprio 0\n\t"
      ".irp r,104,105,106,107\n\tv_mov_b32 v\\r, 0x3f803f80\n\t.endr\n\t"
      G_ST4(104, 150, "0 offen")
      "s_nop 4\n\t"
      F_PACK(16, 0, 48)
      G_ST4(48, 149, "0 offen") G_ST4(52, 149, "0 offen offset:512")
      G_LBAR
      F_PACK(32, 0, 56)
      G_ST4(56, 149, "0 offen offset:2048") G_ST4(60, 149, "0 offen offset:2560")
      A_GREAD(0)
      "111:\n\t"
      G_LBAR A_GDOT A_GREAD(2) M_PAR0
      G_LBAR A_GDOT A_GREAD(4) M_PAR1
      G_LBAR A_GDOT A_GREAD(6) M_PAR2
      G_LBAR A_GDOT
      "v_mov_b32 v94, s86\n\tv_mov_b32 v95, s87\n\tv_and_b32 v92, 0xfffff0ff, v154\n\t"
      "v_add_f32 v88, v88, v90\n\tv_add_f32 v89, v89, v91\n\t"
      "ds_write2st64_b32 v154, v88, v89 offset1:1\n\t"
      G_BAR(0)
      "ds_read2st64_b32 v[64:65], v92 offset1:1\n\tds_read2st64_b32 v[66:67], v92 offset0:2 offset1:3\n\t"
      "ds_read2st64_b32 v[68:69], v92 offset0:4 offset1:5\n\tds_read2st64_b32 v[70:71], v92 offset0:6 offset1:7\n\t"
      "buffer_store_dwordx2 v[94:95], v155, s[72:75], 0 offen sc1\n\t"
      "s_waitcnt lgkmcnt(0)\n\t"
      "v_add_f32 v64, v64, v66\n\tv_add_f32 v65, v65, v67\n\tv_add_f32 v68, v68, v70\n\tv_add_f32 v69, v69, v71\n\t"
      "v_add_f32 v64, v64, v68\n\tv_add_f32 v65, v65, v69\n\t"
      "s_nop 1\n\tv_permlane32_swap_b32 v64, v65\n\t"
      "v_add_f32 v64, v64, v65\n\t" SIGMOID(64, 253)
      "s_nop 0\n\tv_mov_b32 v252, v253\n\t"
      ST(2)
      G_LBAR "s_setprio 1\n\t" "v_permlane32_swap_b32 v253, v252\n\t" G_READ(48, 64, 80, 0) G_ALL(D_S2(19))
      G_LBAR G_APPLY(G_FIRST, D_S2(20))
      G_LBAR G_APPLY(G_EVEN, 2, D_S2(21)) G_BAR(8) G_APPLY(G_ODD, 3, D_S2(22))
      G_BAR(8) G_APPLY(G_EVEN, 4, D_S2(23))
      G_BAR(8) G_ODD(5, "buffer_load_dword v9, v158, s[72:75], 0 offen offset:8 sc1\n\t", G_KV1(200, 216, 62), G_KV1(204, 224, 63), G_KV1(208, 232, 64))
      G_BAR(14) G_EVEN(6, G_Z("160,162,164,166,168"), G_Z("170,172,174,176,178"), G_Z("180,182,184,186"), G_Z("188,190,244,246"))
      G_BAR(9) G_ODD(7, DPPMAX(9, "quad_perm:[1,0,3,2]"), DPPMAX(9, "quad_perm:[2,3,0,1]"), DPPMAX(9, "row_half_mirror"), "s_nop 0\n\t" DPPMAX(9, "row_mirror"))
      "s_waitcnt lgkmcnt(0)\n\t"
      G_MMA2(96, 112, 128, 0) G_MMA2(96, 112, 128, 1) G_MMA2(96, 112, 128, 2) G_MMA2(96, 112, 128, 3)
      "v_add_u32 v1, s65, v159\n\t"
      "s_setprio 2\n\t"
      G_QPRE G_Z("152,154,156,158") "v_mov_b32 v8, v9\n\t"
      A_PACK
      ".irp i,0,1,2,3\n\tds_write_b128 v1, v[48+4*\\i:51+4*\\i] offset:1024*\\i\n\t.endr\n\t"
      G_C0 G_C1
      ST(3)
      G_LBAR
      G_QRD
      "s_branch 300f\n\t"
      ATTN_ROUTINE
      "200:\n\t"
      ST(1)
      G_BAR(8) "s_setprio 1\n\t" G_EPOCH G_READ(48, 64, 80, 0) G_ALL(D_S1(11))
      G_BAR(9) G_APPLY(G_FIRST, D_S1(12))
      G_BAR(9) G_APPLY(G_EVEN, 2, D_S1(13)) G_BAR(8) G_APPLY(G_ODD, 3, D_S1(14))
      G_BAR(8) G_APPLY(G_EVEN, 4, D_S1(15)) G_BAR(8) G_APPLY(G_ODD, 5, D_S2(16))
      G_BAR(8) G_APPLY(G_EVEN, 6, D_S2(17)) G_BAR(8) G_APPLY(G_ODD, 7, D_S2(18))
      ST(2)
      G_BAR(8)
      "s_setprio 0\n\t"
      "ds_read_b64_tr_b16 v[64:65], v146\n\tds_read_b64_tr_b16 v[66:67], v146 offset:256\n\t"
      "ds_read_b64_tr_b16 v[68:69], v146 offset:512\n\tds_read_b64_tr_b16 v[70:71], v146 offset:768\n\t"
      "ds_read2st64_b32 v[72:73], v154 offset1:2\n\tds_read2st64_b32 v[74:75], v154 offset0:4 offset1:6\n\t"
      G_MMA2(96, 112, 128, 0) G_EPOCH_USE G_MMA2(96, 112, 128, 1)
      "s_waitcnt lgkmcnt(0)\n\t"
      "v_add_f32 v72, v72, v74\n\tv_add_f32 v73, v73, v75\n\t"
      ".irp r,64,65,66,67\n\tv_xor_b32 v\\r, v147, v\\r\n\t.endr\n\t"
      "v_add_f32 v72, v72, v73\n\t"
      G_MMA2(96, 112, 128, 2)
      "v_mov_b32 v73, v72\n\t"
      ".irp r,68,69,70,71\n\tv_xor_b32 v\\r, v147, v\\r\n\t.endr\n\t"
      "v_permlane32_swap_b32 v72, v73\n\t"
      G_MMA2(96, 112, 128, 3)
      "v_add_f32 v72, v72, v73\n\t" SIGMOID(72, 253)
      "s_nop 5\n\t"
      A_PACK
      G_BAR(4)
      "s_setprio 2\n\t"
      "v_mfma_f32_32x32x16_bf16 v[16:31], v[64:67], v[48:51], 0\n\t"
      "v_mfma_f32_32x32x16_bf16 v[32:47], v[64:67], v[52:55], 0\n\t"
      ".irp j,0,1,2,3\n\tv_bfe_i32 v[128+\\j], v157, \\j, 1\n\t.endr\n\t"
      "v_mfma_f32_32x32x16_bf16 v[96:111], v[68:71], v[56:59], 0\n\t"
      ".irp j,4,5,6,7\n\tv_bfe_i32 v[128+\\j], v157, \\j, 1\n\t.endr\n\t"
      "v_mfma_f32_32x32x16_bf16 v[112:127], v[68:71], v[60:63], 0\n\t"
      "s_setprio 0\n\t"
      ".irp j,0,1,2,3,4,5,6,7\n\tv_and_b32 v[128+\\j], v156, v[128+\\j]\n\t.endr\n\t"
      F_JOBS(F_MAX1) "s_nop 0\n\t" F_JOBS(F_SWAP) F_JOBS(F_MAX2) F_JOBS(F_NEG) F_JOBS(F_DUP) F_JOBS(F_SWAP)
      G_BAR(0)
      F_EXP(16, 80) F_FMA(32, 84) F_EX(32, "0,8,1,9")
      G_LBAR
      G_POLL(140) F_EX(32, "2,10,3,11,4,12,5,13,6,14,7,15") F_EXP(96, 88)
      G_LBAR
      F_EXP(112, 92) F_SUM1(16, 80, 48) F_SUM1(32, 84, 56) G_POLLWAIT(22, 140)
      G_LBAR
      F_SUM1(96, 88, 64) DPPMAX(9, "quad_perm:[1,0,3,2]") F_SUM1(112, 92, 72) DPPMAX(9, "quad_perm:[2,3,0,1]") F_JOBS(F_SUM2)
      DPPMAX(9, "row_half_mirror") F_JOBS(F_SUM3) DPPMAX(9, "row_mirror") F_JOBS(F_SWAP) F_JOBS(F_ADD) F_JOBS(F_RCP) F_JOBS(F_DUP) F_JOBS(F_SWAP)
      G_LBAR
      G_KV1(200, 216, 62) F_SCALE(16, 80, 48) G_KV1(204, 224, 63) F_SCALE(32, 84, 56) G_KV1(208, 232, 64) F_SCALE(96, 88, 64)
      G_LBAR
      F_SCALE(112, 92, 72)
      "s_cmp_lg_u32 s61, 0\n\t"
      "s_cbranch_scc1 230f\n\t"
      "v_add_u32 v136, s67, v159\n\t"
      ".irp i,0,1,2,3,4,5,6,7\n\tds_write_b128 v136, v[48+4*\\i:51+4*\\i] offset:1024*\\i\n\t.endr\n\t"
      G_ZO G_C0
      "s_branch 231f\n\t"
      "230:\n\t"
      F_JOBS(F_TR0) F_JOBS(F_TR1) G_ZO F_JOBS(F_PACK)
      ".irp r,96,97,98,99,100,101,102,103,104,105,106,107,108,109,110,111\n\tv_mov_b32 v\\r, v152\n\t.endr\n\t"
      "v_add_u32 v136, s69, v159\n\tv_mov_b32 v2, s86\n\tv_mov_b32 v3, s87\n\t"
      "231:\n\t"
      G_QPRE G_Z("152,154,156,158") "v_mov_b32 v8, v9\n\t" G_C1
      ST(3)
      G_LBAR
      G_QRD
      "s_cmp_eq_u32 s61, 0\n\t"
      "s_cbranch_scc1 151f\n\t"
      "s_setprio 3\n\t"
      ".irp i,0,1,2,3\n\tds_read_b128 v[80+4*\\i:83+4*\\i], v136 offset:1024*\\i\n\t.endr\n\t"
      "s_waitcnt lgkmcnt(2)\n\t"
      "v_mfma_f32_32x32x16_bf16 v[16:31], v[80:83], v[128:131], v[96:111]\n\t"
      "v_mfma_f32_32x32x16_bf16 v[32:47], v[84:87], v[128:131], v[96:111]\n\t"
      "s_waitcnt lgkmcnt(0)\n\t"
      "v_mfma_f32_32x32x16_bf16 v[112:127], v[88:91], v[128:131], v[96:111]\n\t"
      "v_mfma_f32_32x32x16_bf16 v[96:111], v[92:95], v[128:131], v[96:111]\n\t"
      "s_mov_b32 s52, 32768\n\ts_mov_b32 s54, 65536\n\ts_mov_b32 s55, 98304\n\t"
      "s_nop 5\n\t"
      F_PACK(16, 0, 80) F_PACK(32, 0, 88)
      "v_mfma_f32_32x32x16_bf16 v[16:31], v[48:51], v[80:83], 0\n\t"
      F_PACK(112, 0, 128)
      "v_mfma_f32_32x32x16_bf16 v[16:31], v[52:55], v[84:87], v[16:31]\n\t"
      F_PACK(96, 0, 136)
      "v_mfma_f32_32x32x16_bf16 v[16:31], v[56:59], v[88:91], v[16:31]\n\t"
      "v_mfma_f32_32x32x16_bf16 v[32:47], v[64:67], v[128:131], 0\n\t"
      "v_mfma_f32_32x32x16_bf16 v[16:31], v[60:63], v[92:95], v[16:31]\n\t"
      "v_mfma_f32_32x32x16_bf16 v[32:47], v[68:71], v[132:135], v[32:47]\n\t"
      "v_mfma_f32_32x32x16_bf16 v[32:47], v[72:75], v[136:139], v[32:47]\n\t"
      "v_mfma_f32_32x32x16_bf16 v[32:47], v[76:79], v[140:143], v[32:47]\n\t"
      "s_nop 8\n\t"
      F_PACK(16, 0, 80)
      G_ST4(80, 149, "0 offen") G_ST4(84, 149, "s52 offen")
      "s_nop 0\n\t"
      F_PACK(32, 0, 88)
      G_ST4(88, 149, "s54 offen") G_ST4(92, 149, "s55 offen")
      "s_setprio 1\n\t"
      "s_waitcnt vmcnt(0)\n\t"
      "buffer_store_dwordx2 v[2:3], v150, s[72:75], 0 offen sc1\n\t"
      G_C0 G_C1
      "s_branch 301f\n\t"
      "151:\n\t"
      "s_setprio 1\n\t"
      "300:\n\t"
      "s_waitcnt vmcnt(0) lgkmcnt(0)\n\t"
      "301:\n\t"
      "v_mov_b64 v[16:17], 0\n\t"
      "v_permlane16_swap_b32 v9, v8\n\t"
      QK(128, 143, 200, 203, 192, 195, 96, 111)
      ".irp j,0,1,2,3\n\tv_dot2c_f32_bf16 v16, v[192+\\j], v[192+\\j]\n\tv_dot2c_f32_bf16 v17, v[196+\\j], v[196+\\j]\n\t.endr\n\t"
      "v_max_f32 v9, v9, v8\n\t"
      "s_nop 1\n\t"
      "v_permlane32_swap_b32 v16, v17\n\t"
      "v_add_f32 v16, v16, v17\n\t"
      "v_mul_f32 v16, v16, v9\n\t"
      "v_cmp_nge_f32 vcc, 0x45100000, v16\n\t"
      "s_cbranch_vccnz 310f\n\t"
      "311:\n\t"
      ST(4)
      "s_cmp_eq_u32 s56, 0\n\t"
      "s_cbranch_scc1 320f\n\t"
      RUN("5")
      "s_branch 330f\n\t"
      "310:\n\t"
      "v_mov_b32 v96, 0xff7fffff\n\tv_mov_b32 v112, 0xff7fffff\n\t"
      "s_mov_b32 s52, s39\n\ts_mov_b32 s54, 63\n\t"
      "312:\n\t"
      "buffer_load_dwordx4 v[16:19], v4, s[72:75], s52 offen sc1\n\t"
      "s_add_u32 s52, s52, 0x800\n\t"
      "s_waitcnt vmcnt(0)\n\t"
      "v_mfma_f32_32x32x16_bf16 v[32:47], v[16:19], v[192:195], 0\n\t"
      "v_mfma_f32_32x32x16_bf16 v[48:63], v[16:19], v[196:199], 0\n\t"
      "s_nop 11\n\t"
      ".irp i,0,2,4,6,8,10,12,14\n\tv_max3_f32 v96, v96, v[32+\\i], v[33+\\i]\n\tv_max3_f32 v112, v112, v[48+\\i], v[49+\\i]\n\t.endr\n\t"
      "s_sub_u32 s54, s54, 1\n\t"
      "s_cbranch_scc0 312b\n\t"
      "v_mov_b32 v97, v96\n\tv_mov_b32 v113, v112\n\ts_nop 1\n\t"
      "v_permlane32_swap_b32 v96, v97\n\tv_permlane32_swap_b32 v112, v113\n\t"
      "v_max_f32 v96, v96, v97\n\tv_max_f32 v112, v112, v113\n\t"
      "v_xor_b32 v96, 0x80000000, v96\n\tv_xor_b32 v112, 0x80000000, v112\n\t"
      ".irp r,1,2,3,4,5,6,7,8,9,10,11,12,13,14,15\n\tv_mov_b32 v[96+\\r], v96\n\tv_mov_b32 v[112+\\r], v112\n\t.endr\n\t"
      "s_nop 1\n\t"
      QK(128, 143, 200, 203, 192, 195, 96, 111)
      "s_nop 11\n\t"
      "s_branch 311b\n\t"
      "320:\n\t"
      RUN("0")
      ST(5)
      "v_mov_b64 v[16:17], -1\n\tv_mov_b64 v[18:19], -1\n\t"
      G_ST4(16, 4, "s24 offen")
      RUN("0")
      "s_cmp_lg_u32 s37, 0\n\t"
      "s_cbranch_scc1 322f\n\t"
      "v_and_b32 v20, 0x1f0, v4\n\tv_lshlrev_b32 v20, 3, v20\n\ts_add_u32 s52, s29, 4096\n\t"
      "3210:\n\tbuffer_load_dwordx2 v[22:23], v20, s[72:75], s28 offen sc1\n\ts_waitcnt vmcnt(0)\n\t"
      "v_cmp_ne_u64 vcc, s[86:87], v[22:23]\n\t" G_SPIN(321, "s_sleep 4\n\t")
      P_LOAD8(32, 29, 52)
      "322:\n\t"
      RUN("0")
      ST(6)
      G_ST4(16, 4, "s25 offen")
      "s_cmp_lg_u32 s37, 0\n\t"
      "s_cbranch_scc1 323f\n\t"
      G_Z("24,26,28,30")
      "v_mov_b32 v20, 0x3f80\n\tv_mov_b32 v21, 0x3f800000\n\t"
      ".irp i,0,1,2,3,4,5,6,7\n\t.irp j,0,1,2,3\n\t"
      "v_dot2c_f32_bf16 v[24+2*\\j], v[32+4*\\i+\\j], v20\n\tv_dot2c_f32_bf16 v[25+2*\\j], v[32+4*\\i+\\j], v21\n\t"
      ".endr\n\t.endr\n\t"
      "v_and_b32 v20, 0xf0, v4\n\tv_lshrrev_b32 v21, 5, v4\n\tv_lshrrev_b32 v22, 7, v4\n\t"
      ".irp e,0,1,2,3\n\tv_permlane16_swap_b32 v[24+\\e], v[28+\\e]\n\t.endr\n\t"
      ".irp e,0,1,2,3\n\tv_add_f32 v[24+\\e], v[24+\\e], v[28+\\e]\n\t.endr\n\t"
      "v_and_or_b32 v20, v21, 8, v20\n\tv_and_or_b32 v20, v22, 4, v20\n\t"
      "v_permlane32_swap_b32 v24, v26\n\tv_permlane32_swap_b32 v25, v27\n\t"
      "v_add_f32 v24, v24, v26\n\tv_add_f32 v25, v25, v27\n\t"
      "v_cvt_pk_bf16_f32 v24, v24, v25\n\tv_min_u32 v24, -2, v24\n\t"
      "buffer_store_dword v24, v20, s[72:75], s30 offen sc1\n\t"
      "323:\n\t"
      RUN("0")
      ST(7)
      "v_add_u32 v20, s35, v4\n\t"
      P_LOAD(16, 0, 31) P_LOAD(20, 1, 31)
      ".irp i,0,1,2,3\n\tds_read_b128 v[32+4*\\i:35+4*\\i], v20 offset:1024*\\i\n\t.endr\n\t"
      RUN("0")
      ST(8)
      "3240:\n\t"
      "v_max3_u32 v80, v16, v17, v18\n\tv_max3_u32 v81, v19, v24, v25\n\tv_max3_u32 v80, v80, v26, v27\n\tv_max_u32 v80, v80, v81\n\t"
      "v_cmp_eq_u32 vcc, -1, v80\n\t"
      G_SPIN(324, "s_sleep 4\n\t" P_LOAD(16, 0, 31) P_LOAD(20, 1, 31) "s_waitcnt vmcnt(0)\n\t")
      "s_waitcnt lgkmcnt(0)\n\t"
      "v_mfma_f32_32x32x16_bf16 v[48:63], v[16:19], v[32:35], 0\n\t"
      "v_mfma_f32_32x32x16_bf16 v[64:79], v[16:19], v[40:43], 0\n\t"
      "v_mfma_f32_32x32x16_bf16 v[48:63], v[24:27], v[36:39], v[48:63]\n\t"
      "v_mfma_f32_32x32x16_bf16 v[64:79], v[24:27], v[44:47], v[64:79]\n\t"
      "v_sub_f32 v80, 1.0, v253\n\tv_sub_f32 v81, 1.0, v252\n\t"
      "v_mov_b32 v82, s86\n\tv_mov_b32 v83, s87\n\tv_mov_b32 v84, s36\n\t"
      "ds_write_b64 v84, v[82:83]\n\t"
      "s_nop 4\n\t"
      "v_add_f32 v56, 0x358637bd, v56\n\tv_rcp_f32 v56, v56\n\t"
      "v_add_f32 v72, 0x358637bd, v72\n\tv_rcp_f32 v72, v72\n\t"
      "v_mul_f32 v80, v80, v56\n\t"
      ".irp r,48,49,50,51,52,53,54,55\n\tv_mul_f32 v\\r, v80, v\\r\n\t.endr\n\t"
      "v_mul_f32 v81, v81, v72\n\t"
      ".irp i,0,1,2,3\n\tv_cvt_pk_bf16_f32 v[16+\\i], v[48+2*\\i], v[49+2*\\i]\n\t.endr\n\t"
      G_ST4(16, 4, "s26 offen")
      ".irp r,64,65,66,67,68,69,70,71\n\tv_mul_f32 v\\r, v81, v\\r\n\t.endr\n\t"
      ".irp i,0,1,2,3\n\tv_cvt_pk_bf16_f32 v[20+\\i], v[64+2*\\i], v[65+2*\\i]\n\t.endr\n\t"
      G_ST4(20, 4, "s27 offen")
      RUNV("0", STR(HALF_SWITCH))
      "v_cmp_eq_u32 vcc, 0, v4\n\tv_mov_b32 v84, s32\n\tv_bfrev_b32 v80, 15\n\t"
      "s_setprio 0\n\t"
      "v_cndmask_b32 v84, v80, v84, vcc\n\t"
      "buffer_store_dwordx2 v[82:83], v84, s[72:75], 0 offen sc1\n\t"
      "330:\n\t"
      ST(9)
      "v_and_b32 v20, 0x70, v4\n\tv_lshlrev_b32 v20, 3, v20\n\t"
      "buffer_load_dwordx2 v[22:23], v20, s[72:75], s33 offen sc1\n\t"
      RUN("0")
      ST(10)
      "v_mov_b32 v84, s36\n\ts_add_u32 s52, s34, 4096\n\t"
      "3310:\n\tds_read_b64 v[24:25], v84\n\ts_waitcnt lgkmcnt(0)\n\tv_cmp_ne_u64 vcc, s[86:87], v[24:25]\n\t" G_SPIN(331, "s_sleep 2\n\t")
      "3320:\n\tv_cmp_ne_u64 vcc, s[86:87], v[22:23]\n\t"
      G_SPIN(332, "s_sleep 4\n\tbuffer_load_dwordx2 v[22:23], v20, s[72:75], s33 offen sc1\n\ts_waitcnt vmcnt(0)\n\t")
      P_LOAD8(48, 34, 52)
      "v_add_u32 v6, s88, v4\n\tv_lshrrev_b32 v7, 2, v4\n\tv_add_u32 v7, s89, v7\n\t"
      "v_xor_b32 v8, 0x800, v6\n\tv_xor_b32 v9, 0x400, v7\n\tv_xor_b32 v1, 64, v0\n\t"
      "s_add_u32 s54, s34, 0x200000\n\ts_add_u32 s55, s34, 0x201000\n\t"
      RUN("0")
      ST(12)
      PV(176, 191, 244, 247, 152, 159)
      "ds_write_b128 v6, v[160:163]\n\tds_write_b128 v6, v[164:167] offset:1024\n\tds_write_b32 v7, v168\n\t"
      G_LBAR
      "ds_read_b32 v24, v9\n\tds_read_b128 v[16:19], v8\n\tds_read_b128 v[20:23], v8 offset:1024\n\t"
      "s_nop 3\n\t"
      "s_waitcnt lgkmcnt(2)\n\t"
      "v_add_f32 v24, v184, v24\n\tv_rcp_f32 v24, v24\n\t"
      "s_waitcnt lgkmcnt(0)\n\t"
      ".irp i,0,1,2,3,4,5,6,7\n\tv_add_f32 v[16+\\i], v[176+\\i], v[16+\\i]\n\t.endr\n\t"
      "v_mul_f32 v24, v253, v24\n\t"
      ".irp i,0,1,2,3,4,5,6,7\n\tv_mul_f32 v[16+\\i], v24, v[16+\\i]\n\t.endr\n\t"
      ".irp i,0,1,2,3\n\tv_cvt_pk_bf16_f32 v[84+\\i], v[16+2*\\i], v[17+2*\\i]\n\t.endr\n\t"
      ".irp i,0,1,2,3\n\tv_min_u32 v[84+\\i], -2, v[84+\\i]\n\t.endr\n\t"
      G_ST4(84, 4, "s38 offen")
      ST(13)
      P_RB(0) P_RB(1)
      P_USEB(0, 4, "0", "0", P_RB(2) P_RA(0) P_AT(0)) P_USEB(1, 8, G_A0, G_A1, P_RB(3) P_RA(1) P_AT(1))
      P_USEB(2, 12, G_A0, G_A1, P_RB(4) P_RA(2) P_AT(2)) P_USEB(3, 12, G_A0, G_A1, P_RB(5) P_RA(3) P_AT(3))
      P_USEB(4, 12, G_A0, G_A1, P_RB(6) P_RA(4) P_AT(4)) P_USEB(5, 12, G_A0, G_A1, P_RB(7) P_RA(5) P_AT(5))
      P_USEB(6, 12, G_A0, G_A1, P_RA(6) P_AT(6)) P_USEB(7, 8, G_A0, G_A1, P_RA(7) P_AT(7))
      "v_xor_b32 v6, 32, v248\n\tv_xor_b32 v7, 64, v248\n\tv_xor_b32 v8, 96, v248\n\t"
      "v_lshrrev_b32 v9, 7, v4\n\tv_bfe_u32 v10, v4, 3, 4\n\tv_xor_b32 v10, v10, v9\n\tv_lshlrev_b32 v10, 4, v10\n\t"
      "v_lshl_add_u32 v10, v9, 8, v10\n\tv_add_u32 v10, s90, v10\n\tv_xor_b32 v11, 16, v10\n\t"
      "v_and_b32 v12, 0x70, v4\n\tv_lshl_or_b32 v12, v9, 10, v12\n\tv_add_u32 v12, s91, v12\n\t"
      G_LBAR
      P_USEA(0, 7) P_USEA(1, 6) P_USEA(2, 5) P_USEA(3, 4) P_USEA(4, 3) P_USEA(5, 2) P_USEA(6, 1) P_USEA(7, 0)
      "s_nop 10\n\t"
      "ds_write_b128 v248, v[16:19]\n\tds_write_b128 v6, v[20:23]\n\tds_write_b128 v7, v[24:27]\n\tds_write_b128 v8, v[28:31]\n\t"
      "ds_write_b128 v248, v[32:35] offset:128\n\tds_write_b128 v6, v[36:39] offset:128\n\t"
      "ds_write_b128 v7, v[40:43] offset:128\n\tds_write_b128 v8, v[44:47] offset:128\n\t"
      ST(15)
      G_LBAR
      ".irp k,0,1,2,3\n\tds_read_b128 v[16+8*\\k:19+8*\\k], v10 offset:16384*\\k\n\tds_read_b128 v[20+8*\\k:23+8*\\k], v11 offset:16384*\\k\n\t.endr\n\t"
      "s_waitcnt lgkmcnt(4)\n\t"
      ".irp i,0,1,2,3,4,5,6,7\n\tv_add_f32 v[16+\\i], v[16+\\i], v[24+\\i]\n\t.endr\n\t"
      "s_waitcnt lgkmcnt(0)\n\t"
      ".irp i,0,1,2,3,4,5,6,7\n\tv_add_f32 v[32+\\i], v[32+\\i], v[40+\\i]\n\t.endr\n\t"
      ".irp i,0,1,2,3,4,5,6,7\n\tv_add_f32 v[16+\\i], v[16+\\i], v[32+\\i]\n\t.endr\n\t"
      ".irp i,0,1,2,3\n\tv_cvt_pk_bf16_f32 v[16+\\i], v[16+2*\\i], v[17+2*\\i]\n\t.endr\n\t"
      "buffer_store_dwordx4 v[16:19], v12, s[20:23], 0 offen" OUT_POL "\n\t"
      "v_cmp_eq_u32 vcc, 0, v4\n\tv_mov_b32 v20, s68\n\tv_bfrev_b32 v21, 15\n\tv_mov_b32 v22, s86\n\tv_mov_b32 v23, s87\n\t"
      "v_cndmask_b32 v20, v21, v20, vcc\n\t"
      "buffer_store_dwordx2 v[22:23], v20, s[72:75], 0 offen sc1\n\t"
      ST_END
      "s_branch 599f\n\t"
      P_STUBS(57, 56, 580) P_SLOW(580, 96, 54, 55)
      "599:\n\t"
      ".Lgm_code_to:"
      : "+{v[4:7]}"(xa), "+{v8}"(wa), "+{v[144:159]}"(vp), "+{v[10:15]}"(sr.wv), "+{s[24:27]}"(base) ST_OP
      : "{v0}"(pc.wo0), "{v248}"(pc.red), "{v249}"(pc.koff), "{v250}"(pc.voff0), "{v251}"(pc.voff1), "{v254}"(pc.wov), "{s[20:23]}"(pc.out),
        "{s37}"(w), "{s[40:43]}"(sr.ra), "{s[44:47]}"(sr.rb), "{s[48:51]}"(sr.rx), "{s[56:71]}"(sp), "{s[72:75]}"(pc.ws),
        "{s[80:83]}"(pc.wo), "{s91}"(outoff)
      : "memory", "scc", "vcc", "s28", "s29", "s30", "s31", "s32", "s33", "s34", "s35", "s36", "s38", "s39", "s52", "s53", "s54", "s55", "s76", "s77",
        "s84", "s85", "s88", "s89", "s90", "s78", "s79", "s86", "s87", "s92", "s93", "v1", "v2", "v3", "v9", "v252", "v253",
#define R8(a, b, c, d, e, f, g, h) "v" #a, "v" #b, "v" #c, "v" #d, "v" #e, "v" #f, "v" #g, "v" #h
        R8(16, 17, 18, 19, 20, 21, 22, 23), R8(24, 25, 26, 27, 28, 29, 30, 31), R8(32, 33, 34, 35, 36, 37, 38, 39),
        R8(40, 41, 42, 43, 44, 45, 46, 47), R8(48, 49, 50, 51, 52, 53, 54, 55), R8(56, 57, 58, 59, 60, 61, 62, 63),
        R8(64, 65, 66, 67, 68, 69, 70, 71), R8(72, 73, 74, 75, 76, 77, 78, 79), R8(80, 81, 82, 83, 84, 85, 86, 87),
        R8(88, 89, 90, 91, 92, 93, 94, 95), R8(96, 97, 98, 99, 100, 101, 102, 103), R8(104, 105, 106, 107, 108, 109, 110, 111),
        R8(112, 113, 114, 115, 116, 117, 118, 119), R8(120, 121, 122, 123, 124, 125, 126, 127), R8(128, 129, 130, 131, 132, 133, 134, 135),
        R8(136, 137, 138, 139, 140, 141, 142, 143), R8(160, 161, 162, 163, 164, 165, 166, 167), R8(168, 169, 170, 171, 172, 173, 174, 175),
        R8(176, 177, 178, 179, 180, 181, 182, 183), R8(184, 185, 186, 187, 188, 189, 190, 191), R8(192, 193, 194, 195, 196, 197, 198, 199),
        R8(200, 201, 202, 203, 204, 205, 206, 207), R8(208, 209, 210, 211, 212, 213, 214, 215), R8(216, 217, 218, 219, 220, 221, 222, 223),
        R8(224, 225, 226, 227, 228, 229, 230, 231), R8(232, 233, 234, 235, 236, 237, 238, 239), R8(240, 241, 242, 243, 244, 245, 246, 247) ST_CL);
}
DI void identity_seed(int r32, int h, unsigned& val, unsigned& hot) {
  val = 0x3F80u << ((r32 & 1) * 16);
  hot = (((r32 >> 2) & 1) == h ? 1u : 0u) << (((r32 >> 1) & 1) | ((r32 >> 2) & 6));
}

#define HG 2
#define ARGS_AT_ONCE 1
#define TLBALL 0
#define WARM 6
#define CODE_LINES 1
#define DBG_OFF 0u
#define WS_SIZE WS_END

struct Scalars { u32x16 sp; u32x4 base; unsigned outoff; };
DI Scalars scalars(unsigned b, unsigned w, unsigned lds0) {
  const unsigned xc = b & 7, in = b >> 3, g = HG * (xc % (8 / HG)) + in % HG, tb = (32 / HG) * (xc / (8 / HG)) + in / HG;
  const unsigned wi = w & 3, grp = w >> 2, pr = wi & 1, rot = (in / HG + (8 / HG) * (in % HG)) & 7;
  const unsigned hd = 4 * g + wi, otb = 4 * xc + (in & 3), og = in >> 2, rh = 4 * g + (tb & 3), sl = tb >> 2;
  Scalars s;
  s.sp = u32x16{grp, lds0 + L_RING + wi * 2048, lds0 + L_X + wi * 2048, rot, rot * 128, wi >> 1, tb * 2048, ((tb + 1) & 31) * 2048,
                ((tb + 2) & 31) * 2048, L_Q + (wi >> 1) * (L_VB - L_Q) + pr * 4096, L_Q + wi * 2048 + grp * 1024, L_PQ + pr * 8192,
                (b | w) ? OOB : WS_EPOCH, L_VB + pr * 4096, DBG_OFF, 0x3FB8AA3Bu  };
  s.base = u32x4{hd * 1024, rh * 8 + sl, ((otb * 2 + grp) * 32 + wi * 8) * 1024, 0u};
  s.outoff = (64 * otb + 8 * w) * 1024 + og * 128;
  return s;
}

__global__ __launch_bounds__(512, 2) void gated_mix_kernel(const char* __restrict__ x, const char* __restrict__ wqa,
                                                           const char* __restrict__ woa, const char* __restrict__ wqb,
                                                           const char* __restrict__ wphi, const char* __restrict__ wob,
                                                           const char* __restrict__ gatew, char* __restrict__ out,
                                                           char* __restrict__ ws) {
  __shared__ __attribute__((aligned(16))) char lds_raw[L_BYTES];
  LAS char* lds = (LAS char*)lds_raw;
  const unsigned lds0 = (unsigned)(uintptr_t)lds;
  const int tid = threadIdx.x, lane = tid & 63, w = __builtin_amdgcn_readfirstlane(tid >> 6);
  const int xc = blockIdx.x & 7, in = blockIdx.x >> 3;
  const int g = HG * (xc % (8 / HG)) + in % HG, tb = (32 / HG) * (xc / (8 / HG)) + in / HG;
  const int r32 = lane & 31, h = lane >> 5;
  const int wi = w & 3, grp = w >> 2, pr = wi & 1;
  char* wsp = ws;
  const unsigned tstart = (unsigned)__builtin_amdgcn_s_memrealtime();
  asm volatile("" : "+s"(x), "+s"(wqa), "+s"(woa), "+s"(wqb), "+s"(wphi), "+s"(wob), "+s"(gatew), "+s"(out), "+s"(ws));
  if (threadIdx.x == 0) *(unsigned*)(wsp + 0xE01000 + blockIdx.x * 4) = tstart;
  STAMP(0); STAMP_RT(62);

  const int l8 = lane >> 3, dcp = lane & 7;
  Src sr;
  sr.ra = __builtin_amdgcn_make_buffer_rsrc((void*)wqa, 0, 512 * 3072, 0x00020000);
  sr.rx = __builtin_amdgcn_make_buffer_rsrc((void*)x, 0, 2048 * 1024, 0x00020000);
  const unsigned wv = (unsigned)(16 * wi + l8) * 3072 + g * 128 + ((dcp ^ ((lane >> 2) & 4)) * 16);
#pragma unroll
  for (int c = 0; c < 3; ++c) { sr.wv[2 * c] = wv + c * 1024; sr.wv[2 * c + 1] = wv + c * 1024 + 8 * 3072 - 1024; }
  const unsigned rot = (in / HG + (8 / HG) * (in % HG)) & 7;
  const unsigned xv0 = (unsigned)(64 * tb + 16 * wi + l8) * 1024 + ((dcp ^ (l8 >> 1)) * 16), xv1 = ((xv0 + 8192) ^ 64) - 1024;
  const unsigned ml = lds0 + L_RING + wi * 2048, mx = lds0 + L_X + wi * 2048;
  const unsigned rotx = rot * 128;
  if (grp) first_chunks(sr, xv0, xv1, ml, mx, rot, rotx);
  SBAR();
  const unsigned hd = 4 * g + wi, og = in >> 2;
  sr.rb = __builtin_amdgcn_make_buffer_rsrc((void*)wqb, 0, 512 * 3072, 0x00020000);
  const rsrc_t rws = __builtin_amdgcn_make_buffer_rsrc((void*)ws, 0, WS_SIZE, 0x00020000);
  if (grp == 0) {
    if (wi < 2) dma16(__builtin_amdgcn_make_buffer_rsrc((void*)wphi, 0, 32 * 512, 0x00020000), lane * 16, g * 2048 + wi * 1024, lds0 + L_WPHI + wi * 1024);
    if (wi == 2) dma16(__builtin_amdgcn_make_buffer_rsrc((void*)gatew, 0, 1024, 0x00020000), lane * 16, 0, lds0 + L_GW);
    {
      const unsigned sh = in / HG, nsh = 32 / HG;
      if (wi < 2 * HG / 2 + (HG == 1)) {
        const unsigned i = (sh * HG + wi % HG) * 64 + lane;
        const unsigned row = (i >> 1) & 511, sg = i & 1, st = i >> 10;
        const rsrc_t r = (st && !sg) ? sr.ra : sr.rb;
      }
      if (wi == 0) {
        asm volatile("s_mov_b32 m0, %0\n\ts_nop 0\n\tbuffer_load_dword %1, %2, 0 offen sc1 lds" :: "s"(lds0 + L_DUMMY), "v"((TLBALL ? lane + 64 * (in & 3) : in * 8 + l8) * 65536 + 0xFF80), "s"(rws) : "memory");
      }
      if (wi == 1) {
        const rsrc_t ro = __builtin_amdgcn_make_buffer_rsrc((void*)out, 0, 2048 * 1024, 0x00020000);
        asm volatile("s_mov_b32 m0, %0\n\ts_nop 0\n\tbuffer_load_dword %1, %2, 0 offen sc1 lds" :: "s"(lds0 + L_DUMMY), "v"((TLBALL ? (lane & 31) : in) * 65536 + 0xFF80), "s"(ro) : "memory");
      }
      if (wi == 2) {
        const unsigned i = blockIdx.x * 32 + (lane & 31);
        const rsrc_t r = __builtin_amdgcn_make_buffer_rsrc((void*)(blockIdx.x & 128 ? wob : woa), 0, 512 * 1024, 0x00020000);
        asm volatile("s_mov_b32 m0, %0\n\ts_nop 0\n\tbuffer_load_dword %1, %2, 0 offen lds" :: "s"(lds0 + L_DUMMY), "v"((i & 4095) * 128), "s"(r) : "memory");
      }
    }
    if (wi == 3) {
      unsigned long long pc;
      unsigned len;
      asm volatile("s_getpc_b64 %0\n.Lgm_code_from:\n\ts_mov_b32 %1, .Lgm_code_to-.Lgm_code_from" : "=s"(pc), "=s"(len));
      const rsrc_t rc = __builtin_amdgcn_make_buffer_rsrc((void*)pc, 0, len, 0x00020000);
      asm volatile("s_mov_b32 m0, %0\n\ts_nop 0\n\tbuffer_load_dword %1, %2, 0 offen lds" :: "s"(lds0 + L_DUMMY), "v"((in + 32 * l8) * 128), "s"(rc) : "memory");
    }
  }

  const unsigned kvb = WS_KV + (unsigned)hd * 131072, kvh = kvb + grp * 65536;
  Prog pc;
  pc.ws = rws;
  pc.koff = kvh + lane * 16;
  {
    const unsigned vreal = kvh + h * 256 + (lane & 15) * 16, vones = WS_ONES + g * 256;
    const bool pad = lane & 16, one = (lane & 11) == 0;
    pc.voff0 = pad ? (one ? vones - 1024 : OOB) : vreal; pc.voff1 = pad ? (one ? vones - 1536 : OOB) : vreal;
  }
  const int tq = (lane & 15) >> 2, tp_ = lane & 3, tnb = (lane >> 4) & 1;
  const unsigned wlane = L_RING + (8 * h + tq) * 128 + (((2 * pr + tnb) ^ (tq & 2)) * 32) + tp_ * 8;
  u32x4 xa;
#pragma unroll
  for (int s = 0; s < 4; ++s) xa[s] = L_X + r32 * 128 + (((2 * s + h) ^ ((r32 >> 1) & 7)) * 16);
  unsigned vp[16];
  if (grp == 0) {
    const unsigned kb = WS_KV + (unsigned)(4 * g + 2 * pr) * 131072 + (unsigned)(2 * tb) * 2048, fl = WS_FKV + (g * 32 + tb) * FL;
    const int sw = (r32 & 19) | ((r32 & 4) << 1) | ((r32 & 8) >> 1);
    vp[0] = vp[1] = vp[2] = vp[3] = vp[4] = 0;
    vp[5] = wi < 2 ? kb + h * 512 + sw * 16 : kb + (r32 >> 4) * 131072 + 1024 + h * 256 + (r32 & 15) * 16;
    vp[6] = wi < 2 ? (r32 == 0 ? fl + (2 * pr + h) * 16 + 8 : OOB) : (wi == 2 && lane < 8 ? WS_ONES + tb * 2048 + g * 256 + lane * 16 : OOB);
    vp[7] = w == 0 ? WS_KR + ((4 * g + (tb & 3)) * 8 + (tb >> 2)) * 256 + lane * 4 : OOB;
    vp[8] = L_X + r32 * 128 + (((2 * wi + h) ^ ((r32 >> 1) & 7)) * 16);
    vp[9] = L_GW + rotx + wi * 32 + h * 16;
    vp[10] = L_GP + wi * 512 + lane * 4;
    vp[11] = tid < 4 ? fl + lane * 16 : OOB;
  } else {
    vp[0] = xv0; vp[1] = xv1; vp[4] = vp[7] = vp[9] = vp[11] = 0;
    vp[2] = L_WPHI + 2 * pr * 512 + (4 * h + tq) * 32 + tp_ * 8;
    vp[3] = tnb ? 0x80008000u : 0u;
    vp[5] = WS_KP + (unsigned)(((4 * g + 2 * pr) * 8 + (lane >> 4)) * 32 + tb) * 256 + (lane & 15) * 16;
    vp[6] = lane == 0 ? WS_FKP + ((g * 2 + pr) * 32 + tb) * FL : OOB;
    vp[8] = tnb ? 0x3F800000u : 0u;
    vp[10] = L_GP + 256 + lane * 4;
  }
  {
    unsigned val, hot;
    identity_seed(r32, h, val, hot);
    vp[12] = val; vp[13] = hot; vp[14] = WS_FKV + (g * 32 + r32) * FL + wi * 16; vp[15] = lane * 16;
  }
  pc.out = __builtin_amdgcn_make_buffer_rsrc((void*)out, 0, 2048 * 1024, 0x00020000);
  pc.wo = __builtin_amdgcn_make_buffer_rsrc((void*)(grp ? woa : wob), 0, 512 * 1024, 0x00020000);
  pc.wov = (unsigned)(128 * wi + l8) * 1024 + og * 128 + ((dcp ^ ((lane >> 2) & 4)) * 16);
  pc.wo0 = L_WO + wi * 32768 + (unsigned)(4 * h + tq) * 128 + ((tnb ^ (tq & 2)) * 32) + tp_ * 8;
  pc.red = L_RED + wi * 16384 + (grp * 32 + r32) * 256 + ((h ^ (r32 & 7)) * 16);

  unsigned bi = blockIdx.x, wv_ = w;
  asm volatile("" : "+s"(bi), "+s"(wv_));
  const Scalars sc = scalars(bi, wv_, lds0);
  program(sr, xa, wlane + (wi >> 1) * 8192,
          u32x16{vp[0], vp[1], vp[2], vp[3], vp[4], vp[5], vp[6], vp[7], vp[8], vp[9], vp[10], vp[11], vp[12], vp[13], vp[14], vp[15]}, sc.sp,
          sc.base, wv_, sc.outoff, pc ST_ARG);
  if (__builtin_amdgcn_readfirstlane(wv_) == 0) *(unsigned*)(wsp + 0xE02000 + bi * 4) = (unsigned)__builtin_amdgcn_s_memrealtime();
}

__global__ void pre_kernel(char* ws) { if (threadIdx.x == 0 && blockIdx.x == 0) *(unsigned*)(ws + 0xE00000) = (unsigned)__builtin_amdgcn_s_memrealtime(); }
__global__ void post_kernel(char* ws) {
  const unsigned t0 = (unsigned)__builtin_amdgcn_s_memrealtime();
  const unsigned* p = (const unsigned*)(ws + 0xE00000);
  int s_lo = 0x7FFFFFFF, s_hi = -0x7FFFFFFF, e_hi = -0x7FFFFFFF;
  for (int i = threadIdx.x; i < 256; i += 64) { int a = (int)(p[1024 + i] - t0), b = (int)(p[2048 + i] - t0); s_lo = min(s_lo, a); s_hi = max(s_hi, a); e_hi = max(e_hi, b); }
  for (int o = 32; o; o >>= 1) { s_lo = min(s_lo, __shfl_xor(s_lo, o)); s_hi = max(s_hi, __shfl_xor(s_hi, o)); e_hi = max(e_hi, __shfl_xor(e_hi, o)); }
  const int pre = (int)(p[0] - t0);
  unsigned d = (unsigned)(0); if (d > 6000) d = 6000;
  const unsigned t1 = (unsigned)__builtin_amdgcn_s_memrealtime();
  while ((unsigned)__builtin_amdgcn_s_memrealtime() - t1 < d) __builtin_amdgcn_s_sleep(1);
}
}

extern "C" void kernel_launch(void* const* d_in, const int* in_sizes, int n_in, void* d_out, int out_size, void* d_ws,
                              size_t ws_size, hipStream_t stream) {
  gated_mix_kernel<<<256, 512, 0, stream>>>((const char*)d_in[0], (const char*)d_in[1], (const char*)d_in[2], (const char*)d_in[4],
                                            (const char*)d_in[5], (const char*)d_in[6], (const char*)d_in[7], (char*)d_out, (char*)d_ws);
  post_kernel<<<1, 64, 0, stream>>>((char*)d_ws);
}
```

```cpp
#include <hip/hip_runtime.h>
#include <hip/hip_bf16.h>
#include <stdint.h>

namespace {

typedef __bf16 bf16x8 __attribute__((ext_vector_type(8)));
typedef __bf16 bf16x2 __attribute__((ext_vector_type(2)));
typedef float f32x16 __attribute__((ext_vector_type(16)));
typedef float f32x8 __attribute__((ext_vector_type(8)));
typedef float f32x4 __attribute__((ext_vector_type(4)));
typedef float f32x2 __attribute__((ext_vector_type(2)));
typedef unsigned u32x16 __attribute__((ext_vector_type(16)));
typedef unsigned u32x8 __attribute__((ext_vector_type(8)));
typedef unsigned u32x4 __attribute__((ext_vector_type(4)));
typedef unsigned u32x2 __attribute__((ext_vector_type(2)));
typedef short s16x4 __attribute__((ext_vector_type(4)));
typedef __amdgpu_buffer_rsrc_t rsrc_t;
#define LAS __attribute__((address_space(3)))
#define DI __device__ __forceinline__

constexpr unsigned WS_EPOCH = 0;
constexpr unsigned FL = 128;
constexpr unsigned WS_KR = 0x10000;
constexpr unsigned WS_ONES = 0x40000;
constexpr unsigned WS_FKV = 0x60000;
constexpr unsigned WS_FKP = 0x70000;
constexpr unsigned WS_FOB = 0x80000;
constexpr unsigned WS_KP = 0x100000;
constexpr unsigned WS_OB = 0x500000;
constexpr unsigned WS_OA = 0x700000;
constexpr unsigned WS_KV = 0x900000;
constexpr unsigned WS_END = 0xD00000;

constexpr unsigned L_X = 0;
constexpr unsigned L_RING = 65536;
constexpr unsigned L_WO = 0;
constexpr unsigned L_RED = 0;
constexpr unsigned L_PQ = 131072;
constexpr unsigned L_Q = 147456;
constexpr unsigned L_WPHI = L_Q;
constexpr unsigned L_GW = L_Q + 2048;
constexpr unsigned L_DUMMY = L_Q + 3072;
constexpr unsigned L_GP = L_Q + 4096;
constexpr unsigned L_VB = 155648;
constexpr unsigned L_BYTES = 163840;

constexpr int SC1 = 16;
constexpr unsigned OOB = 0xF0000000u;

DI void dma16(rsrc_t r, unsigned voff, unsigned soff, unsigned lds_dst) {
  asm volatile("s_mov_b32 m0, %0\n\ts_nop 0\n\tbuffer_load_dwordx4 %1, %2, %3 offen lds"
               :: "s"(lds_dst), "v"(voff), "s"(r), "s"(soff) : "memory");
}
#define SBAR() __builtin_amdgcn_sched_barrier(0)

#define EX(r) "v_exp_f32 v" #r ", v" #r "\n\t"
#define QK(s0, s1, k0, k1, q0, q1, c0, c1) "v_mfma_f32_32x32x16_bf16 v[" #s0 ":" #s1 "], v[" #k0 ":" #k1 "], v[" #q0 ":" #q1 "], v[" #c0 ":" #c1 "]\n\t"
#define PV(o0, o1, a0, a1, p0, p1) "v_smfmac_f32_32x32x32_bf16 v[" #o0 ":" #o1 "], v[" #a0 ":" #a1 "], v[" #p0 ":" #p1 "], v5\n\t"
#define LOADS(k0, k1, a0, a1, b0, b1)                                                     \
  "s_add_u32 s76, s76, 0x800\n\t"                                                         \
  "s_and_b32 s76, s76, 0xffff\n\t"                                                        \
  "buffer_load_dwordx4 v[" #k0 ":" #k1 "], v249, s[72:75], s76 offen sc1\n\t"             \
  "buffer_load_dwordx4 v[" #a0 ":" #a1 "], v250, s[72:75], s76 offen offset:1024 sc1\n\t" \
  "buffer_load_dwordx4 v[" #b0 ":" #b1 "], v251, s[72:75], s76 offen offset:1536 sc1\n\t"
#define WODMA                                                                             \
  "s_mov_b32 m0, s84\n\t"                                                                 \
  "s_add_u32 s84, s84, 0x400\n\t"                                                         \
  "buffer_load_dwordx4 v254, s[80:83], s85 offen lds\n\t"                                 \
  "s_add_u32 s85, s85, 0x2000\n\t"
#define ITER(ck0, ck1, ca0, ca1, cb0, cb1, pk0, pk1, pa0, pa1, pb0, pb1, nk0, nk1, EXTRA, WAIT) \
  EX(128) EX(129) PV(176, 191, pb0, pb1, 152, 159) EX(130) EX(131)                        \
  LOADS(pk0, pk1, pa0, pa1, pb0, pb1)                                                     \
  EX(132) EX(133) QK(144, 159, ck0, ck1, 196, 199, 112, 127) EX(134) EX(135) EXTRA EX(136) EX(137) EX(138) \
  PV(160, 175, ca0, ca1, 128, 135) EX(139) EX(140) EX(141) EX(142) EX(143)                 \
  EX(144) EX(145) PV(160, 175, cb0, cb1, 136, 143) EX(146) EX(147) EX(148) EX(149)         \
  WAIT                                                                                    \
  QK(128, 143, nk0, nk1, 192, 195, 96, 111) EX(150) EX(151) EX(152) EX(153) EX(154)      \
  PV(176, 191, ca0, ca1, 144, 151) EX(155) EX(156) EX(157) EX(158) EX(159)

#define ATTN_ROUTINE                                                                                  \
  "400:\n\t"                                                                                          \
  ITER(200, 203, 216, 219, 220, 223, 212, 215, 240, 243, 244, 247, 204, 207, WODMA, "")               \
  ITER(204, 207, 224, 227, 228, 231, 200, 203, 216, 219, 220, 223, 208, 211, "", "")                  \
  "s_cbranch_vccnz 402f\n\t401:\n\t"                                                                 \
  ITER(208, 211, 232, 235, 236, 239, 204, 207, 224, 227, 228, 231, 212, 215, WODMA, "s_waitcnt vmcnt(8)\n\t") \
  ITER(212, 215, 240, 243, 244, 247, 208, 211, 232, 235, 236, 239, 200, 203, "", "s_waitcnt vmcnt(7)\n\t")    \
  "s_sub_u32 s77, s77, 1\n\ts_waitcnt vmcnt(0)\n\ts_cbranch_scc0 400b\n\ts_setpc_b64 s[78:79]\n\t"  \
  "402:\n\ts_setprio 0\n\ts_branch 401b\n\t"
#define RUNV(N, V) "s_mov_b64 vcc, " V "\n\ts_mov_b32 s77, " N "\n\ts_swappc_b64 s[78:79], s[92:93]\n\t"
#define RUN(N) RUNV(N, "0")

#define STAMP(i) do { } while (0)
#define STAMP_RT(i) do { } while (0)
#define ST(i) ""
#define ST_END ""
#define ST_OP
#define ST_CL
#define ST_PAR
#define ST_ARG

#define G_BAR(N) "s_waitcnt vmcnt(" #N ") lgkmcnt(0)\n\ts_barrier\n\t"
#define G_LBAR "s_waitcnt lgkmcnt(0)\n\ts_barrier\n\t"
#define G_EPOCH "buffer_load_dwordx2 v[2:3], off, s[72:75], 0 sc1\n\t"
#define G_EPOCH_USE "v_readfirstlane_b32 s86, v2\n\tv_readfirstlane_b32 s87, v3\n\ts_add_u32 s86, s86, 1\n\ts_addc_u32 s87, s87, 0\n\t"
#define D_ROW(KC) "s_xor_b32 s52, s59, " KC "\n\ts_mul_i32 s52, s52, 196608\n\t"
#define D_A(R, COL, LOFF) "s_add_u32 m0, s57, " LOFF "\n\ts_nop 0\n\tbuffer_load_dwordx4 v[10+2*" #COL "], " R ", s52 offen lds\n\t"
#define D_B(R, COL) "buffer_load_dwordx4 v[11+2*" #COL "], " R ", s52 offen offset:1024 lds\n\t"
#define RA "s[40:43]"
#define RB "s[44:47]"
#define D_X(C)                                                                                        \
  "s_add_u32 m0, s58, 8192*" #C "\n\ts_xor_b32 s52, s60, 128*" #C "\n\t"                              \
  "buffer_load_dwordx4 v144, s[48:51], s52 offen lds\n\tbuffer_load_dwordx4 v145, s[48:51], s52 offen offset:1024 lds\n\t"
#define D_W(N, R0, C0, R1, C1) D_ROW(#N "&7") D_A(R0, C0, "16384*(" #N "&3)"), D_B(R0, C0), D_A(R1, C1, "16384*(" #N "&3)+8192"), D_B(R1, C1)
#define D_S0(N) D_W(N, RA, 1, RA, 2)
#define D_S1(N) D_W(N, RB, 0, RB, 1)
#define D_S2(N) D_W(N, RA, 0, RB, 2)
#define G_CAT(A, B, C, D) A B C D
#define G_APPLY(M, ...) M(__VA_ARGS__)
#define G_ALL(L) G_APPLY(G_CAT, L)

#define G_RW(W, C, S)                                                                                 \
  "ds_read_b64_tr_b16 v[" #W "+4*" #S ":" #W "+4*" #S "+1], v8 offset:16384*(" #C "&3)+2048*" #S "\n\t"       \
  "ds_read_b64_tr_b16 v[" #W "+4*" #S "+2:" #W "+4*" #S "+3], v8 offset:16384*(" #C "&3)+2048*" #S "+512\n\t"
#define G_RX(X, C, S, T) "ds_read_b128 v[" #X "+4*" #S ":" #X "+4*" #S "+3], v[4+" #S "] offset:8192*" #C "+4096*" #T "\n\t"
#define G_RS(W, X0, X1, C, S) G_RW(W, C, S) G_RX(X0, C, S, 0) G_RX(X1, C, S, 1)
#define G_READ(W, X0, X1, C) G_RS(W, X0, X1, C, 0) G_RS(W, X0, X1, C, 1) G_RS(W, X0, X1, C, 2) G_RS(W, X0, X1, C, 3)
#define G_M(ACC, W, X, S, CIN) \
  "v_mfma_f32_32x32x16_bf16 " ACC ", v[" #W "+4*" #S ":" #W "+4*" #S "+3], v[" #X "+4*" #S ":" #X "+4*" #S "+3], " CIN "\n\t"
#define G_A0 "v[16:31]"
#define G_A1 "v[32:47]"
#define G_MMA2(W, X0, X1, S) G_M(G_A0, W, X0, S, G_A0) G_M(G_A1, W, X1, S, G_A1)
#define G_STEP(RW, RX0, RX1, C, W, X0, X1, C0, C1, D0, D1, D2, D3)                                    \
  G_M(G_A0, W, X0, 0, C0) G_RW(RW, C, 0) G_RX(RX0, C, 0, 0) G_M(G_A1, W, X1, 0, C1) G_RX(RX1, C, 0, 1) G_RW(RW, C, 1) \
  G_M(G_A0, W, X0, 1, G_A0) G_RX(RX0, C, 1, 0) G_RX(RX1, C, 1, 1) D0 G_M(G_A1, W, X1, 1, G_A1) G_RW(RW, C, 2) G_RX(RX0, C, 2, 0) \
  G_M(G_A0, W, X0, 2, G_A0) G_RX(RX1, C, 2, 1) G_RW(RW, C, 3) D1 G_M(G_A1, W, X1, 2, G_A1) G_RX(RX0, C, 3, 0) G_RX(RX1, C, 3, 1) \
  G_M(G_A0, W, X0, 3, G_A0) D2 G_M(G_A1, W, X1, 3, G_A1) D3
#define G_EVEN(C, D0, D1, D2, D3) G_STEP(48, 64, 80, C, 96, 112, 128, G_A0, G_A1, D0, D1, D2, D3)
#define G_ODD(C, D0, D1, D2, D3) G_STEP(96, 112, 128, C, 48, 64, 80, G_A0, G_A1, D0, D1, D2, D3)
#define G_FIRST(D0, D1, D2, D3) G_STEP(96, 112, 128, 1, 48, 64, 80, "0", "0", D0, D1, D2, D3)
#define A_PACK                                                                                        \
  ".irp i,0,1,2,3\n\tv_cvt_pk_bf16_f32 v[48+\\i], v[16+2*\\i], v[17+2*\\i]\n\tv_cvt_pk_bf16_f32 v[56+\\i], v[24+2*\\i], v[25+2*\\i]\n\t.endr\n\t" \
  ".irp i,0,1,2,3\n\tv_cvt_pk_bf16_f32 v[52+\\i], v[32+2*\\i], v[33+2*\\i]\n\tv_cvt_pk_bf16_f32 v[60+\\i], v[40+2*\\i], v[41+2*\\i]\n\t.endr\n\t"
#define G_Z(L) ".irp r," L "\n\tv_mov_b64 v[\\r:\\r+1], 0\n\t.endr\n\t"
#define G_ZO G_Z("160,162,164,166,168,170,172,174,176,178,180,182,184,186,188,190,244,246")
#define G_QPRE "v_add_u32 v6, s66, v159\n\tv_mov_b32 v4, v159\n\tv_xor_b32 v7, 0x400, v6\n\tv_mov_b32 v5, 0xdddddddd\n\t"
#define G_QRD "ds_read_b128 v[192:195], v7\n\tds_read_b128 v[196:199], v6\n\t"
#define G_C(L) ".irp r," L "\n\tv_mov_b32 v\\r, 0xc2400000\n\t.endr\n\t"
#define G_C0 G_C("96,97,98,99,100,101,102,103,104,105,106,107,108,109,110,111")
#define G_C1 G_C("112,113,114,115,116,117,118,119,120,121,122,123,124,125,126,127")
#define G_ST4(R, V, O) "buffer_store_dwordx4 v[" #R ":" #R "+3], v" #V ", s[72:75], " O " sc1\n\t"
#define G_POLL(R) "buffer_load_dwordx3 v[" #R ":" #R "+2], v158, s[72:75], 0 offen sc1\n\t"
#define G_SPIN(L, AGAIN) "s_cbranch_vccz " #L "1f\n\t" AGAIN "s_sub_u32 s53, s53, 1\n\ts_cbranch_scc0 " #L "0b\n\ts_mov_b32 s53, 0\n\t" #L "1:\n\t"
#define G_POLLWAIT(L, R)                                                                              \
  #L "0:\n\ts_waitcnt vmcnt(0)\n\tv_cmp_ne_u64 vcc, s[86:87], v[" #R ":" #R "+1]\n\t"                  \
  G_SPIN(L, "s_sleep 4\n\t" G_POLL(R)) "s_waitcnt vmcnt(0)\n\tv_mov_b32 v9, v[" #R "+2]\n\t"
#define G_KV1(K, V, T)                                                                                \
  "buffer_load_dwordx4 v[" #K ":" #K "+3], v249, s[72:75], s" #T " offen sc1\n\t"                      \
  "buffer_load_dwordx4 v[" #V ":" #V "+3], v250, s[72:75], s" #T " offen offset:1024 sc1\n\t"          \
  "buffer_load_dwordx4 v[" #V "+4:" #V "+7], v251, s[72:75], s" #T " offen offset:1536 sc1\n\t"
#define G_KV G_KV1(200, 216, 62) G_KV1(204, 224, 63) G_KV1(208, 232, 64)
#define DPPMAX(R, CTRL) "v_max_f32_dpp v" #R ", v" #R ", v" #R " " CTRL " row_mask:0xf bank_mask:0xf\n\t"
#define SIGMOID(R, OUT) \
  "v_mul_f32 v" #R ", 0xbfb8aa3b, v" #R "\n\tv_exp_f32 v" #R ", v" #R "\n\ts_nop 0\n\tv_add_f32 v" #R ", 1.0, v" #R "\n\tv_rcp_f32 v" #OUT ", v" #R "\n\t"
#define A_GREAD(C)                                                                                    \
  "v_xor_b32 v92, 128*" #C ", v153\n\tv_xor_b32 v93, 128*(" #C "+1), v153\n\t"                     \
  "ds_read_b128 v[72:75], v152 offset:8192*" #C "\n\tds_read_b128 v[80:83], v152 offset:8192*" #C "+4096\n\t" \
  "ds_read_b128 v[76:79], v152 offset:8192*(" #C "+1)\n\tds_read_b128 v[84:87], v152 offset:8192*(" #C "+1)+4096\n\t" \
  "ds_read_b128 v[64:67], v92\n\tds_read_b128 v[68:71], v93\n\t"
#define A_GDOT                                                                                        \
  ".irp j,0,1,2,3\n\t"                                                                                \
  "v_dot2c_f32_bf16 v88, v[72+\\j], v[64+\\j]\n\tv_dot2c_f32_bf16 v89, v[80+\\j], v[64+\\j]\n\t"       \
  "v_dot2c_f32_bf16 v90, v[76+\\j], v[68+\\j]\n\tv_dot2c_f32_bf16 v91, v[84+\\j], v[68+\\j]\n\t"       \
  ".endr\n\t"
#define A_DPPMAX(CTRL) "s_nop 1\n\tv_max_f32_dpp v96, v96, v96 " CTRL " row_mask:0xf bank_mask:0xf\n\t"
#define F_JOBS(M) M(16, 80, 48) M(32, 84, 56) M(96, 88, 64) M(112, 92, 72)
#define F_MAX1(E, T, P)                                                                               \
  "v_max3_f32 v[" #T "], v[" #E "], v[" #E "+1], v[" #E "+2]\n\t"                                      \
  "v_max3_f32 v[" #T "+2], v[" #E "+8], v[" #E "+9], v[" #E "+10]\n\t"                                 \
  "v_max3_f32 v[" #T "+1], v[" #E "+3], v[" #E "+4], v[" #E "+5]\n\t"                                  \
  "v_max3_f32 v[" #T "+3], v[" #E "+11], v[" #E "+12], v[" #E "+13]\n\t"                               \
  "v_max3_f32 v[" #T "], v[" #T "], v[" #E "+6], v[" #E "+7]\n\t"                                      \
  "v_max3_f32 v[" #T "+2], v[" #T "+2], v[" #E "+14], v[" #E "+15]\n\t"                                \
  "v_max_f32 v[" #T "], v[" #T "], v[" #T "+1]\n\t"                                                    \
  "v_max_f32 v[" #T "+2], v[" #T "+2], v[" #T "+3]\n\t"
#define F_SWAP(E, T, P) "v_permlane32_swap_b32 v[" #T "], v[" #T "+2]\n\t"
#define F_MAX2(E, T, P) "v_max_f32 v[" #T "], v[" #T "], v[" #T "+2]\n\t"
#define F_NEG(E, T, P) "v_mul_f32 v[" #T "], 0xbfb8aa3b, v[" #T "]\n\t"
#define F_DUP(E, T, P) "v_mov_b32 v[" #T "+2], v[" #T "]\n\t"
#define F_FMA(E, T)                                                                                   \
  ".irp i,0,1,2,3,4,5,6,7\n\t"                                                                        \
  "v_fma_f32 v[" #E "+\\i], v[" #E "+\\i], s71, v[" #T "]\n\t"                                      \
  "v_fma_f32 v[" #E "+8+\\i], v[" #E "+8+\\i], s71, v[" #T "+2]\n\t"                                \
  ".endr\n\t"
#define F_EX(E, L) ".irp i," L "\n\tv_exp_f32 v[" #E "+\\i], v[" #E "+\\i]\n\t.endr\n\t"
#define F_EXP(E, T) F_FMA(E, T) F_EX(E, "0,8,1,9,2,10,3,11,4,12,5,13,6,14,7,15")
#define F_SUM1(E, T, P) ".irp i,0,1,2,3,4,5,6,7\n\tv_add_f32 v[" #P "+\\i], v[" #E "+2*\\i], v[" #E "+2*\\i+1]\n\t.endr\n\t"
#define F_SUM2(E, T, P) ".irp i,0,2,4,6\n\tv_add_f32 v[" #P "+\\i], v[" #P "+\\i], v[" #P "+\\i+1]\n\t.endr\n\t"
#define F_SUM3(E, T, P) "v_add_f32 v[" #T "], v[" #P "], v[" #P "+2]\n\tv_add_f32 v[" #T "+2], v[" #P "+4], v[" #P "+6]\n\t"
#define F_ADD(E, T, P) "v_add_f32 v[" #T "], v[" #T "], v[" #T "+2]\n\t"
#define F_RCP(E, T, P) "v_rcp_f32 v[" #T "], v[" #T "]\n\t"
#define F_PACK(E, T, P) ".irp i,0,1,2,3,4,5,6,7\n\tv_cvt_pk_bf16_f32 v[" #P "+\\i], v[" #E "+2*\\i], v[" #E "+2*\\i+1]\n\t.endr\n\t"
#define F_SCALE(E, T, P)                                                                              \
  ".irp i,0,1,2,3,4,5,6,7\n\t"                                                                        \
  "v_mul_f32 v[" #E "+\\i], v[" #E "+\\i], v[" #T "]\n\t"                                              \
  "v_mul_f32 v[" #E "+8+\\i], v[" #E "+8+\\i], v[" #T "+2]\n\t"                                        \
  ".endr\n\t" F_PACK(E, T, P)
#define F_TR0(E, T, P) "v_mfma_f32_32x32x16_bf16 v[" #E ":" #E "+15], v[" #P ":" #P "+3], v[128:131], 0\n\t"
#define F_TR1(E, T, P) "v_mfma_f32_32x32x16_bf16 v[" #E ":" #E "+15], v[" #P "+4:" #P "+7], v[132:135], v[" #E ":" #E "+15]\n\t"
#define P_TEST(F, I)                                                                                  \
  "v_max3_u32 v80, v[" #F "+4*" #I "], v[" #F "+1+4*" #I "], v[" #F "+2+4*" #I "]\n\t"                 \
  "v_max_u32 v80, v80, v[" #F "+3+4*" #I "]\n\t"                                                       \
  "v_cmp_eq_u32 vcc, -1, v80\n\t"
#define P_LOAD(F, I, SO) "buffer_load_dwordx4 v[" #F "+4*" #I ":" #F "+3+4*" #I "], v4, s[72:75], s" #SO " offen offset:1024*(" #I "&3) sc1\n\t"
#define P_RD(R, I, OFF)                                                                               \
  "ds_read_b64_tr_b16 v[" R ":" R "+1], v0 offset:" #OFF "+2048*" #I "\n\t"                         \
  "ds_read_b64_tr_b16 v[" R "+2:" R "+3], v0 offset:" #OFF "+2048*" #I "+1024\n\t"                  \
  "ds_read_b64_tr_b16 v[" R "+4:" R "+5], v1 offset:" #OFF "+2048*" #I "\n\t"                       \
  "ds_read_b64_tr_b16 v[" R "+6:" R "+7], v1 offset:" #OFF "+2048*" #I "+1024\n\t"
#define P_RB(I) P_RD("192+8*(" #I "&3)", I, 0)
#define P_RA(I) P_RD("128+8*" #I, I, 16384)
#define P_MM(W, F, I, C0, C1)                                                                         \
  "v_mfma_f32_32x32x16_bf16 v[16:31], v[" W ":" W "+3], v[" #F "+4*" #I ":" #F "+3+4*" #I "], " C0 "\n\t"   \
  "v_mfma_f32_32x32x16_bf16 v[32:47], v[" W "+4:" W "+7], v[" #F "+4*" #I ":" #F "+3+4*" #I "], " C1 "\n\t"
#define P_USEB(I, N, C0, C1, MORE)                                                                    \
  "s_waitcnt lgkmcnt(" #N ")\n\t" P_MM("192+8*(" #I "&3)", 48, I, C0, C1) MORE
#define P_USEA(I, N) "s_waitcnt vmcnt(" #N ")\n\t" P_TEST(96, I) "s_cbranch_vccnz 57" #I "f\n\t56" #I ":\n\t" P_MM("128+8*" #I, 96, I, G_A0, G_A1)
#define P_STUB(A, B, I, R) #A #I ":\n\ts_call_b64 s[78:79], " #R "f\n\ts_branch " #B #I "b\n\t"
#define P_STUBS(A, B, R) P_STUB(A, B, 0, R) P_STUB(A, B, 1, R) P_STUB(A, B, 2, R) P_STUB(A, B, 3, R) P_STUB(A, B, 4, R) P_STUB(A, B, 5, R) P_STUB(A, B, 6, R) P_STUB(A, B, 7, R)
#define P_RETRY(F, I, SO) P_TEST(F, I) "s_cbranch_vccz 1f\n\ts_mov_b32 s52, 1\n\t" P_LOAD(F, I, SO) "1:\n\t"
#define P_SLOW(R, F, S0, S1)                                                                          \
  #R ":\n\ts_waitcnt vmcnt(0)\n\ts_mov_b32 s52, 0\n\t"                                             \
  P_RETRY(F, 0, S0) P_RETRY(F, 1, S0) P_RETRY(F, 2, S0) P_RETRY(F, 3, S0) P_RETRY(F, 4, S1) P_RETRY(F, 5, S1) P_RETRY(F, 6, S1) P_RETRY(F, 7, S1) \
  "s_sub_u32 s53, s53, 1\n\ts_cselect_b32 s52, 0, s52\n\ts_cselect_b32 s53, 0, s53\n\t" \
  "s_cmp_lg_u32 s52, 0\n\ts_cbranch_scc1 " #R "b\n\ts_waitcnt vmcnt(0)\n\ts_setpc_b64 s[78:79]\n\t"
#define P_LOAD8(F, S0, S1) P_LOAD(F, 0, S0) P_LOAD(F, 1, S0) P_LOAD(F, 2, S0) P_LOAD(F, 3, S0) P_LOAD(F, 4, S1) P_LOAD(F, 5, S1) P_LOAD(F, 6, S1) P_LOAD(F, 7, S1)
#define P_LOADA P_LOAD8(96, 54, 55)
#define OUT_POL " sc1"
#define HALF_SWITCH 0
#define A_AT 7
#define P_AT(I) ".if " STR(A_AT) " == " #I "\n\t" P_LOADA ".endif\n\t"
#define STR_(x) #x
#define STR(x) STR_(x)
typedef unsigned u32x6 __attribute__((ext_vector_type(6)));
struct Src { u32x6 wv; rsrc_t ra, rb, rx; };
#define M_PAR0                                                                                        \
  "s_add_u32 s34, s26, 0x500000\n\ts_lshl_b32 s33, s56, 15\n\ts_sub_u32 s33, s26, s33\n\ts_lshr_b32 s33, s33, 3\n\ts_add_u32 s33, s33, 0x80000\n\t" \
  "s_lshl_b32 s29, s25, 13\n\ts_add_u32 s29, s29, 0x100000\n\ts_lshl_b32 s30, s25, 8\n\ts_add_u32 s30, s30, 0x10000\n\t" \
  "s_lshr_b32 s28, s25, 4\n\ts_lshl_b32 s28, s28, 12\n\ts_add_u32 s28, s28, 0x70000\n\t"
#define M_PAR1                                                                                        \
  "s_lshl_b32 s31, s24, 1\n\ts_add_u32 s31, s31, 0x10000\n\ts_lshl_b32 s39, s24, 7\n\ts_add_u32 s39, s39, 0x900000\n\t" \
  "s_lshl_b32 s32, s62, 5\n\ts_add_u32 s32, s32, s24\n\ts_add_u32 s24, s32, 0x700000\n\ts_add_u32 s25, s32, 0x708000\n\t" \
  "s_add_u32 s26, s32, 0x500000\n\ts_add_u32 s27, s32, 0x508000\n\ts_lshr_b32 s32, s32, 3\n\ts_add_u32 s32, s32, 0x80000\n\t"
#define M_PAR2                                                                                        \
  "s_lshl_b32 s38, s56, 15\n\ts_add_u32 s38, s38, s24\n\ts_lshl_b32 s35, s58, 1\n\ts_add_u32 s35, s35, 0x20000\n\t" \
  "s_add_u32 s36, s58, 0x26200\n\ts_lshl_b32 s88, s56, 11\n\ts_add_u32 s88, s88, s35\n\ts_lshl_b32 s89, s56, 10\n\t" \
  "s_add_u32 s89, s89, s58\n\ts_add_u32 s89, s89, 0x26000\n\ts_lshl_b32 s90, s37, 11\n\t"              \
  "s_mov_b32 s76, s64\n\ts_lshl_b32 s84, s58, 4\n\ts_lshl_b32 s85, s56, 14\n\ts_add_u32 s84, s84, s85\n\ts_mov_b32 s85, 0\n\t"
static_assert(WS_OB == 0x500000 && WS_OA == 0x700000 && WS_FOB == 0x80000 && WS_KP == 0x100000 && WS_KR == 0x10000 && WS_FKP == 0x70000 &&
              WS_KV == 0x900000 && L_PQ == 0x20000 && L_VB == 0x26000 && L_X == 0 && L_WO == 0 && L_RED == 0 && FL == 128, "literals above");
DI void first_chunks(const Src& sr, unsigned xv0, unsigned xv1, unsigned ml, unsigned mx, unsigned rot, unsigned rotx) {
  asm volatile(D_X(0) G_ALL(D_S0(0)) D_X(1) G_ALL(D_S0(1))
               :: "{v[10:15]}"(sr.wv), "{v144}"(xv0), "{v145}"(xv1), "{s[40:43]}"(sr.ra), "{s[48:51]}"(sr.rx), "{s57}"(ml), "{s58}"(mx),
                  "{s59}"(rot), "{s60}"(rotx)
               : "memory", "scc", "s52");
}
struct Prog { unsigned wo0, red, koff, voff0, voff1, wov; rsrc_t out, ws, wo; };
DI void program(Src sr, u32x4 xa, unsigned wa, u32x16 vp, u32x16 sp, u32x4 base, unsigned w, unsigned outoff, const Prog& pc ST_PAR) {
  asm volatile(
      "s_getpc_b64 s[92:93]\n\t"
      "99:\n\t"
      "s_add_u32 s92, s92, 400f-99b\n\ts_addc_u32 s93, s93, 0\n\t"
      "s_mov_b32 s53, 0x40000\n\t"
      "s_cmp_eq_u32 s56, 0\n\t"
      "s_cbranch_scc1 100f\n\t"
      G_BAR(6) D_X(2) G_ALL(D_S0(2)) G_BAR(6) D_X(3) G_ALL(D_S0(3)) G_BAR(6) D_X(4) G_ALL(D_S0(4)) M_PAR0
      G_BAR(6) D_X(5) G_ALL(D_S0(5)) M_PAR1 G_BAR(6) D_X(6) G_ALL(D_S0(6)) M_PAR2 G_BAR(6) D_X(7) G_ALL(D_S0(7))
      G_BAR(6) G_ALL(D_S1(8)) G_ALL(D_S1(9)) G_BAR(8) G_ALL(D_S1(10))
      "s_branch 200f\n\t"
      "100:\n\t"
      G_EPOCH
      G_Z("16,18,20,22,24,26,28,30,32,34,36,38,40,42,44,46")
      "s_mov_b32 s52, 0\n\t"
      G_LBAR G_READ(48, 64, 80, 0)
      "s_branch 102f\n\t"
      "101:\n\t"
      G_BAR(0) G_EVEN(0, "", "", "", "")
      "102:\n\t"
      G_LBAR G_ODD(1, "", "", "", "") G_LBAR G_EVEN(2, "", "", "", "") G_LBAR G_ODD(3, "", "", "", "")
      ".irp r,4,5,6,7\n\tv_xor_b32 v\\r, 0x8000, v\\r\n\t.endr\n\t"
      "s_xor_b32 s52, s52, 1\n\t"
      "s_cbranch_scc1 101b\n\t"
      ST(1)
      G_LBAR
      G_MMA2(96, 112, 128, 0) G_MMA2(96, 112, 128, 1) G_MMA2(96, 112, 128, 2) G_MMA2(96, 112, 128, 3)
      G_EPOCH_USE G_Z("88,90,92,94")
      "s_cmp_lg_u32 s61, 0\n\t"
      "s_cbranch_scc1 110f\n\t"
      "s_nop 0\n\t"
      ".irp r,16,17,18,19,20,21,22,23,24,25,26,27,28,29,30,31\n\tv_mul_f32 v\\r, 0x3eb8aa3b, v\\r\n\t.endr\n\t"
      G_LBAR
      ".irp r,32,33,34,35,36,37,38,39,40,41,42,43,44,45,46,47\n\tv_mul_f32 v\\r, 0x3eb8aa3b, v\\r\n\t.endr\n\t"
      A_PACK
      G_ST4(48, 149, "0 offen") G_ST4(52, 149, "0 offen offset:2048")
      ".irp j,0,1,2,3\n\t"
      "v_dot2c_f32_bf16 v92, v[48+\\j], v[48+\\j]\n\tv_dot2c_f32_bf16 v93, v[52+\\j], v[52+\\j]\n\t"
      "v_dot2c_f32_bf16 v94, v[56+\\j], v[56+\\j]\n\tv_dot2c_f32_bf16 v95, v[60+\\j], v[60+\\j]\n\t"
      ".endr\n\t"
      "s_mov_b32 s54, 131072\n\t"
      G_LBAR
      G_ST4(56, 149, "s54 offen") G_ST4(60, 149, "s54 offen offset:2048")
      "v_permlane32_swap_b32 v92, v94\n\tv_permlane32_swap_b32 v93, v95\n\t"
      "v_add_f32 v96, v92, v94\n\tv_add_f32 v97, v93, v95\n\t"
      A_GREAD(0)
      "v_max_f32 v96, v96, v97\n\t"
      A_DPPMAX("quad_perm:[1,0,3,2]") A_DPPMAX("quad_perm:[2,3,0,1]") A_DPPMAX("row_half_mirror") A_DPPMAX("row_mirror")
      "v_mov_b32 v97, v96\n\tv_mov_b32 v100, -1\n\ts_nop 0\n\tv_permlane16_swap_b32 v96, v97\n\tv_max_f32 v96, v96, v97\n\t"
      "buffer_store_dword v96, v150, s[72:75], 0 offen sc1\n\t"
      "buffer_store_dword v100, v151, s[72:75], 0 offen sc1\n\t"
      "s_branch 111f\n\t"
      "110:\n\t"
      ".irp j,0,1,2,3,4,5,6,7\n\tv_bfe_i32 v[96+\\j], v157, \\j, 1\n\t.endr\n\t"
      A_PACK
      ".irp j,0,1,2,3,4,5,6,7\n\tv_and_b32 v[96+\\j], v156, v[96+\\j]\n\t.endr\n\t"
      G_LBAR
      "s_setprio 2\n\t"
      "v_mfma_f32_32x32x16_bf16 v[16:31], v[48:51], v[96:99], 0\n\t"
      "v_mfma_f32_32x32x16_bf16 v[32:47], v[52:55], v[96:99], 0\n\t"
      "v_mfma_f32_32x32x16_bf16 v[16:31], v[56:59], v[100:103], v[16:31]\n\t"
      "v_mfma_f32_32x32x16_bf16 v[32:47], v[60:63], v[100:103], v[32:47]\n\t"
      "s_setprio 0\n\t"
      ".irp r,104,105,106,107\n\tv_mov_b32 v\\r, 0x3f803f80\n\t.endr\n\t"
      G_ST4(104, 150, "0 offen")
      "s_nop 4\n\t"
      F_PACK(16, 0, 48)
      G_ST4(48, 149, "0 offen") G_ST4(52, 149, "0 offen offset:512")
      G_LBAR
      F_PACK(32, 0, 56)
      G_ST4(56, 149, "0 offen offset:2048") G_ST4(60, 149, "0 offen offset:2560")
      A_GREAD(0)
      "111:\n\t"
      G_LBAR A_GDOT A_GREAD(2) M_PAR0
      G_LBAR A_GDOT A_GREAD(4) M_PAR1
      G_LBAR A_GDOT A_GREAD(6) M_PAR2
      G_LBAR A_GDOT
      "v_mov_b32 v94, s86\n\tv_mov_b32 v95, s87\n\tv_and_b32 v92, 0xfffff0ff, v154\n\t"
      "v_add_f32 v88, v88, v90\n\tv_add_f32 v89, v89, v91\n\t"
      "ds_write2st64_b32 v154, v88, v89 offset1:1\n\t"
      G_BAR(0)
      "ds_read2st64_b32 v[64:65], v92 offset1:1\n\tds_read2st64_b32 v[66:67], v92 offset0:2 offset1:3\n\t"
      "ds_read2st64_b32 v[68:69], v92 offset0:4 offset1:5\n\tds_read2st64_b32 v[70:71], v92 offset0:6 offset1:7\n\t"
      "buffer_store_dwordx2 v[94:95], v155, s[72:75], 0 offen sc1\n\t"
      "s_waitcnt lgkmcnt(0)\n\t"
      "v_add_f32 v64, v64, v66\n\tv_add_f32 v65, v65, v67\n\tv_add_f32 v68, v68, v70\n\tv_add_f32 v69, v69, v71\n\t"
      "v_add_f32 v64, v64, v68\n\tv_add_f32 v65, v65, v69\n\t"
      "s_nop 1\n\tv_permlane32_swap_b32 v64, v65\n\t"
      "v_add_f32 v64, v64, v65\n\t" SIGMOID(64, 253)
      "s_nop 0\n\tv_mov_b32 v252, v253\n\t"
      ST(2)
      G_LBAR "s_setprio 1\n\t" "v_permlane32_swap_b32 v253, v252\n\t" G_READ(48, 64, 80, 0) G_ALL(D_S2(19))
      G_LBAR G_APPLY(G_FIRST, D_S2(20))
      G_LBAR G_APPLY(G_EVEN, 2, D_S2(21)) G_BAR(8) G_APPLY(G_ODD, 3, D_S2(22))
      G_BAR(8) G_APPLY(G_EVEN, 4, D_S2(23))
      G_BAR(8) G_ODD(5, "buffer_load_dword v9, v158, s[72:75], 0 offen offset:8 sc1\n\t", G_KV1(200, 216, 62), G_KV1(204, 224, 63), G_KV1(208, 232, 64))
      G_BAR(14) G_EVEN(6, G_Z("160,162,164,166,168"), G_Z("170,172,174,176,178"), G_Z("180,182,184,186"), G_Z("188,190,244,246"))
      G_BAR(9) G_ODD(7, DPPMAX(9, "quad_perm:[1,0,3,2]"), DPPMAX(9, "quad_perm:[2,3,0,1]"), DPPMAX(9, "row_half_mirror"), "s_nop 0\n\t" DPPMAX(9, "row_mirror"))
      "s_waitcnt lgkmcnt(0)\n\t"
      G_MMA2(96, 112, 128, 0) G_MMA2(96, 112, 128, 1) G_MMA2(96, 112, 128, 2) G_MMA2(96, 112, 128, 3)
      "v_add_u32 v1, s65, v159\n\t"
      "s_setprio 2\n\t"
      G_QPRE G_Z("152,154,156,158") "v_mov_b32 v8, v9\n\t"
      A_PACK
      ".irp i,0,1,2,3\n\tds_write_b128 v1, v[48+4*\\i:51+4*\\i] offset:1024*\\i\n\t.endr\n\t"
      G_C0 G_C1
      ST(3)
      G_LBAR
      G_QRD
      "s_branch 300f\n\t"
      ATTN_ROUTINE
      "200:\n\t"
      ST(1)
      G_BAR(8) "s_setprio 1\n\t" G_EPOCH G_READ(48, 64, 80, 0) G_ALL(D_S1(11))
      G_BAR(9) G_APPLY(G_FIRST, D_S1(12))
      G_BAR(9) G_APPLY(G_EVEN, 2, D_S1(13)) G_BAR(8) G_APPLY(G_ODD, 3, D_S1(14))
      G_BAR(8) G_APPLY(G_EVEN, 4, D_S1(15)) G_BAR(8) G_APPLY(G_ODD, 5, D_S2(16))
      G_BAR(8) G_APPLY(G_EVEN, 6, D_S2(17)) G_BAR(8) G_APPLY(G_ODD, 7, D_S2(18))
      ST(2)
      G_BAR(8)
      "s_setprio 0\n\t"
      "ds_read_b64_tr_b16 v[64:65], v146\n\tds_read_b64_tr_b16 v[66:67], v146 offset:256\n\t"
      "ds_read_b64_tr_b16 v[68:69], v146 offset:512\n\tds_read_b64_tr_b16 v[70:71], v146 offset:768\n\t"
      "ds_read2st64_b32 v[72:73], v154 offset1:2\n\tds_read2st64_b32 v[74:75], v154 offset0:4 offset1:6\n\t"
      G_MMA2(96, 112, 128, 0) G_EPOCH_USE G_MMA2(96, 112, 128, 1)
      "s_waitcnt lgkmcnt(0)\n\t"
      "v_add_f32 v72, v72, v74\n\tv_add_f32 v73, v73, v75\n\t"
      ".irp r,64,65,66,67\n\tv_xor_b32 v\\r, v147, v\\r\n\t.endr\n\t"
      "v_add_f32 v72, v72, v73\n\t"
      G_MMA2(96, 112, 128, 2)
      "v_mov_b32 v73, v72\n\t"
      ".irp r,68,69,70,71\n\tv_xor_b32 v\\r, v147, v\\r\n\t.endr\n\t"
      "v_permlane32_swap_b32 v72, v73\n\t"
      G_MMA2(96, 112, 128, 3)
      "v_add_f32 v72, v72, v73\n\t" SIGMOID(72, 253)
      "s_nop 5\n\t"
      A_PACK
      G_BAR(4)
      "s_setprio 2\n\t"
      "v_mfma_f32_32x32x16_bf16 v[16:31], v[64:67], v[48:51], 0\n\t"
      "v_mfma_f32_32x32x16_bf16 v[32:47], v[64:67], v[52:55], 0\n\t"
      ".irp j,0,1,2,3\n\tv_bfe_i32 v[128+\\j], v157, \\j, 1\n\t.endr\n\t"
      "v_mfma_f32_32x32x16_bf16 v[96:111], v[68:71], v[56:59], 0\n\t"
      ".irp j,4,5,6,7\n\tv_bfe_i32 v[128+\\j], v157, \\j, 1\n\t.endr\n\t"
      "v_mfma_f32_32x32x16_bf16 v[112:127], v[68:71], v[60:63], 0\n\t"
      "s_setprio 0\n\t"
      ".irp j,0,1,2,3,4,5,6,7\n\tv_and_b32 v[128+\\j], v156, v[128+\\j]\n\t.endr\n\t"
      F_JOBS(F_MAX1) "s_nop 0\n\t" F_JOBS(F_SWAP) F_JOBS(F_MAX2) F_JOBS(F_NEG) F_JOBS(F_DUP) F_JOBS(F_SWAP)
      G_BAR(0)
      F_EXP(16, 80) F_FMA(32, 84) F_EX(32, "0,8,1,9")
      G_LBAR
      G_POLL(140) F_EX(32, "2,10,3,11,4,12,5,13,6,14,7,15") F_EXP(96, 88)
      G_LBAR
      F_EXP(112, 92) F_SUM1(16, 80, 48) F_SUM1(32, 84, 56) G_POLLWAIT(22, 140)
      G_LBAR
      F_SUM1(96, 88, 64) DPPMAX(9, "quad_perm:[1,0,3,2]") F_SUM1(112, 92, 72) DPPMAX(9, "quad_perm:[2,3,0,1]") F_JOBS(F_SUM2)
      DPPMAX(9, "row_half_mirror") F_JOBS(F_SUM3) DPPMAX(9, "row_mirror") F_JOBS(F_SWAP) F_JOBS(F_ADD) F_JOBS(F_RCP) F_JOBS(F_DUP) F_JOBS(F_SWAP)
      G_LBAR
      G_KV1(200, 216, 62) F_SCALE(16, 80, 48) G_KV1(204, 224, 63) F_SCALE(32, 84, 56) G_KV1(208, 232, 64) F_SCALE(96, 88, 64)
      G_LBAR
      F_SCALE(112, 92, 72)
      "s_cmp_lg_u32 s61, 0\n\t"
      "s_cbranch_scc1 230f\n\t"
      "v_add_u32 v136, s67, v159\n\t"
      ".irp i,0,1,2,3,4,5,6,7\n\tds_write_b128 v136, v[48+4*\\i:51+4*\\i] offset:1024*\\i\n\t.endr\n\t"
      G_ZO G_C0
      "s_branch 231f\n\t"
      "230:\n\t"
      F_JOBS(F_TR0) F_JOBS(F_TR1) G_ZO F_JOBS(F_PACK)
      ".irp r,96,97,98,99,100,101,102,103,104,105,106,107,108,109,110,111\n\tv_mov_b32 v\\r, v152\n\t.endr\n\t"
      "v_add_u32 v136, s69, v159\n\tv_mov_b32 v2, s86\n\tv_mov_b32 v3, s87\n\t"
      "231:\n\t"
      G_QPRE G_Z("152,154,156,158") "v_mov_b32 v8, v9\n\t" G_C1
      ST(3)
      G_LBAR
      G_QRD
      "s_cmp_eq_u32 s61, 0\n\t"
      "s_cbranch_scc1 151f\n\t"
      "s_setprio 3\n\t"
      ".irp i,0,1,2,3\n\tds_read_b128 v[80+4*\\i:83+4*\\i], v136 offset:1024*\\i\n\t.endr\n\t"
      "s_waitcnt lgkmcnt(2)\n\t"
      "v_mfma_f32_32x32x16_bf16 v[16:31], v[80:83], v[128:131], v[96:111]\n\t"
      "v_mfma_f32_32x32x16_bf16 v[32:47], v[84:87], v[128:131], v[96:111]\n\t"
      "s_waitcnt lgkmcnt(0)\n\t"
      "v_mfma_f32_32x32x16_bf16 v[112:127], v[88:91], v[128:131], v[96:111]\n\t"
      "v_mfma_f32_32x32x16_bf16 v[96:111], v[92:95], v[128:131], v[96:111]\n\t"
      "s_mov_b32 s52, 32768\n\ts_mov_b32 s54, 65536\n\ts_mov_b32 s55, 98304\n\t"
      "s_nop 5\n\t"
      F_PACK(16, 0, 80) F_PACK(32, 0, 88)
      "v_mfma_f32_32x32x16_bf16 v[16:31], v[48:51], v[80:83], 0\n\t"
      F_PACK(112, 0, 128)
      "v_mfma_f32_32x32x16_bf16 v[16:31], v[52:55], v[84:87], v[16:31]\n\t"
      F_PACK(96, 0, 136)
      "v_mfma_f32_32x32x16_bf16 v[16:31], v[56:59], v[88:91], v[16:31]\n\t"
      "v_mfma_f32_32x32x16_bf16 v[32:47], v[64:67], v[128:131], 0\n\t"
      "v_mfma_f32_32x32x16_bf16 v[16:31], v[60:63], v[92:95], v[16:31]\n\t"
      "v_mfma_f32_32x32x16_bf16 v[32:47], v[68:71], v[132:135], v[32:47]\n\t"
      "v_mfma_f32_32x32x16_bf16 v[32:47], v[72:75], v[136:139], v[32:47]\n\t"
      "v_mfma_f32_32x32x16_bf16 v[32:47], v[76:79], v[140:143], v[32:47]\n\t"
      "s_nop 8\n\t"
      F_PACK(16, 0, 80)
      G_ST4(80, 149, "0 offen") G_ST4(84, 149, "s52 offen")
      "s_nop 0\n\t"
      F_PACK(32, 0, 88)
      G_ST4(88, 149, "s54 offen") G_ST4(92, 149, "s55 offen")
      "s_setprio 1\n\t"
      "s_waitcnt vmcnt(0)\n\t"
      "buffer_store_dwordx2 v[2:3], v150, s[72:75], 0 offen sc1\n\t"
      G_C0 G_C1
      "s_branch 301f\n\t"
      "151:\n\t"
      "s_setprio 1\n\t"
      "300:\n\t"
      "s_waitcnt vmcnt(0) lgkmcnt(0)\n\t"
      "301:\n\t"
      "v_mov_b64 v[16:17], 0\n\t"
      "v_permlane16_swap_b32 v9, v8\n\t"
      QK(128, 143, 200, 203, 192, 195, 96, 111)
      ".irp j,0,1,2,3\n\tv_dot2c_f32_bf16 v16, v[192+\\j], v[192+\\j]\n\tv_dot2c_f32_bf16 v17, v[196+\\j], v[196+\\j]\n\t.endr\n\t"
      "v_max_f32 v9, v9, v8\n\t"
      "s_nop 1\n\t"
      "v_permlane32_swap_b32 v16, v17\n\t"
      "v_add_f32 v16, v16, v17\n\t"
      "v_mul_f32 v16, v16, v9\n\t"
      "v_cmp_nge_f32 vcc, 0x45100000, v16\n\t"
      "s_cbranch_vccnz 310f\n\t"
      "311:\n\t"
      ST(4)
      "s_cmp_eq_u32 s56, 0\n\t"
      "s_cbranch_scc1 320f\n\t"
      RUN("5")
      "s_branch 330f\n\t"
      "310:\n\t"
      "v_mov_b32 v96, 0xff7fffff\n\tv_mov_b32 v112, 0xff7fffff\n\t"
      "s_mov_b32 s52, s39\n\ts_mov_b32 s54, 63\n\t"
      "312:\n\t"
      "buffer_load_dwordx4 v[16:19], v4, s[72:75], s52 offen sc1\n\t"
      "s_add_u32 s52, s52, 0x800\n\t"
      "s_waitcnt vmcnt(0)\n\t"
      "v_mfma_f32_32x32x16_bf16 v[32:47], v[16:19], v[192:195], 0\n\t"
      "v_mfma_f32_32x32x16_bf16 v[48:63], v[16:19], v[196:199], 0\n\t"
      "s_nop 11\n\t"
      ".irp i,0,2,4,6,8,10,12,14\n\tv_max3_f32 v96, v96, v[32+\\i], v[33+\\i]\n\tv_max3_f32 v112, v112, v[48+\\i], v[49+\\i]\n\t.endr\n\t"
      "s_sub_u32 s54, s54, 1\n\t"
      "s_cbranch_scc0 312b\n\t"
      "v_mov_b32 v97, v96\n\tv_mov_b32 v113, v112\n\ts_nop 1\n\t"
      "v_permlane32_swap_b32 v96, v97\n\tv_permlane32_swap_b32 v112, v113\n\t"
      "v_max_f32 v96, v96, v97\n\tv_max_f32 v112, v112, v113\n\t"
      "v_xor_b32 v96, 0x80000000, v96\n\tv_xor_b32 v112, 0x80000000, v112\n\t"
      ".irp r,1,2,3,4,5,6,7,8,9,10,11,12,13,14,15\n\tv_mov_b32 v[96+\\r], v96\n\tv_mov_b32 v[112+\\r], v112\n\t.endr\n\t"
      "s_nop 1\n\t"
      QK(128, 143, 200, 203, 192, 195, 96, 111)
      "s_nop 11\n\t"
      "s_branch 311b\n\t"
      "320:\n\t"
      RUN("0")
      ST(5)
      "v_mov_b64 v[16:17], -1\n\tv_mov_b64 v[18:19], -1\n\t"
      G_ST4(16, 4, "s24 offen")
      RUN("0")
      "s_cmp_lg_u32 s37, 0\n\t"
      "s_cbranch_scc1 322f\n\t"
      "v_and_b32 v20, 0x1f0, v4\n\tv_lshlrev_b32 v20, 3, v20\n\ts_add_u32 s52, s29, 4096\n\t"
      "3210:\n\tbuffer_load_dwordx2 v[22:23], v20, s[72:75], s28 offen sc1\n\ts_waitcnt vmcnt(0)\n\t"
      "v_cmp_ne_u64 vcc, s[86:87], v[22:23]\n\t" G_SPIN(321, "s_sleep 4\n\t")
      P_LOAD8(32, 29, 52)
      "322:\n\t"
      RUN("0")
      ST(6)
      G_ST4(16, 4, "s25 offen")
      "s_cmp_lg_u32 s37, 0\n\t"
      "s_cbranch_scc1 323f\n\t"
      G_Z("24,26,28,30")
      "v_mov_b32 v20, 0x3f80\n\tv_mov_b32 v21, 0x3f800000\n\t"
      ".irp i,0,1,2,3,4,5,6,7\n\t.irp j,0,1,2,3\n\t"
      "v_dot2c_f32_bf16 v[24+2*\\j], v[32+4*\\i+\\j], v20\n\tv_dot2c_f32_bf16 v[25+2*\\j], v[32+4*\\i+\\j], v21\n\t"
      ".endr\n\t.endr\n\t"
      "v_and_b32 v20, 0xf0, v4\n\tv_lshrrev_b32 v21, 5, v4\n\tv_lshrrev_b32 v22, 7, v4\n\t"
      ".irp e,0,1,2,3\n\tv_permlane16_swap_b32 v[24+\\e], v[28+\\e]\n\t.endr\n\t"
      ".irp e,0,1,2,3\n\tv_add_f32 v[24+\\e], v[24+\\e], v[28+\\e]\n\t.endr\n\t"
      "v_and_or_b32 v20, v21, 8, v20\n\tv_and_or_b32 v20, v22, 4, v20\n\t"
      "v_permlane32_swap_b32 v24, v26\n\tv_permlane32_swap_b32 v25, v27\n\t"
      "v_add_f32 v24, v24, v26\n\tv_add_f32 v25, v25, v27\n\t"
      "v_cvt_pk_bf16_f32 v24, v24, v25\n\tv_min_u32 v24, -2, v24\n\t"
      "buffer_store_dword v24, v20, s[72:75], s30 offen sc1\n\t"
      "323:\n\t"
      RUN("0")
      ST(7)
      "v_add_u32 v20, s35, v4\n\t"
      P_LOAD(16, 0, 31) P_LOAD(20, 1, 31)
      ".irp i,0,1,2,3\n\tds_read_b128 v[32+4*\\i:35+4*\\i], v20 offset:1024*\\i\n\t.endr\n\t"
      RUN("0")
      ST(8)
      "3240:\n\t"
      "v_max3_u32 v80, v16, v17, v18\n\tv_max3_u32 v81, v19, v24, v25\n\tv_max3_u32 v80, v80, v26, v27\n\tv_max_u32 v80, v80, v81\n\t"
      "v_cmp_eq_u32 vcc, -1, v80\n\t"
      G_SPIN(324, "s_sleep 4\n\t" P_LOAD(16, 0, 31) P_LOAD(20, 1, 31) "s_waitcnt vmcnt(0)\n\t")
      "s_waitcnt lgkmcnt(0)\n\t"
      "v_mfma_f32_32x32x16_bf16 v[48:63], v[16:19], v[32:35], 0\n\t"
      "v_mfma_f32_32x32x16_bf16 v[64:79], v[16:19], v[40:43], 0\n\t"
      "v_mfma_f32_32x32x16_bf16 v[48:63], v[24:27], v[36:39], v[48:63]\n\t"
      "v_mfma_f32_32x32x16_bf16 v[64:79], v[24:27], v[44:47], v[64:79]\n\t"
      "v_sub_f32 v80, 1.0, v253\n\tv_sub_f32 v81, 1.0, v252\n\t"
      "v_mov_b32 v82, s86\n\tv_mov_b32 v83, s87\n\tv_mov_b32 v84, s36\n\t"
      "ds_write_b64 v84, v[82:83]\n\t"
      "s_nop 4\n\t"
      "v_add_f32 v56, 0x358637bd, v56\n\tv_rcp_f32 v56, v56\n\t"
      "v_add_f32 v72, 0x358637bd, v72\n\tv_rcp_f32 v72, v72\n\t"
      "v_mul_f32 v80, v80, v56\n\t"
      ".irp r,48,49,50,51,52,53,54,55\n\tv_mul_f32 v\\r, v80, v\\r\n\t.endr\n\t"
      "v_mul_f32 v81, v81, v72\n\t"
      ".irp i,0,1,2,3\n\tv_cvt_pk_bf16_f32 v[16+\\i], v[48+2*\\i], v[49+2*\\i]\n\t.endr\n\t"
      G_ST4(16, 4, "s26 offen")
      ".irp r,64,65,66,67,68,69,70,71\n\tv_mul_f32 v\\r, v81, v\\r\n\t.endr\n\t"
      ".irp i,0,1,2,3\n\tv_cvt_pk_bf16_f32 v[20+\\i], v[64+2*\\i], v[65+2*\\i]\n\t.endr\n\t"
      G_ST4(20, 4, "s27 offen")
      RUNV("0", STR(HALF_SWITCH))
      "v_cmp_eq_u32 vcc, 0, v4\n\tv_mov_b32 v84, s32\n\tv_bfrev_b32 v80, 15\n\t"
      "s_setprio 0\n\t"
      "v_cndmask_b32 v84, v80, v84, vcc\n\t"
      "buffer_store_dwordx2 v[82:83], v84, s[72:75], 0 offen sc1\n\t"
      "330:\n\t"
      ST(9)
      "v_and_b32 v20, 0x70, v4\n\tv_lshlrev_b32 v20, 3, v20\n\t"
      "buffer_load_dwordx2 v[22:23], v20, s[72:75], s33 offen sc1\n\t"
      RUN("0")
      ST(10)
      "v_mov_b32 v84, s36\n\ts_add_u32 s52, s34, 4096\n\t"
      "3310:\n\tds_read_b64 v[24:25], v84\n\ts_waitcnt lgkmcnt(0)\n\tv_cmp_ne_u64 vcc, s[86:87], v[24:25]\n\t" G_SPIN(331, "s_sleep 2\n\t")
      "3320:\n\tv_cmp_ne_u64 vcc, s[86:87], v[22:23]\n\t"
      G_SPIN(332, "s_sleep 4\n\tbuffer_load_dwordx2 v[22:23], v20, s[72:75], s33 offen sc1\n\ts_waitcnt vmcnt(0)\n\t")
      P_LOAD8(48, 34, 52)
      "v_add_u32 v6, s88, v4\n\tv_lshrrev_b32 v7, 2, v4\n\tv_add_u32 v7, s89, v7\n\t"
      "v_xor_b32 v8, 0x800, v6\n\tv_xor_b32 v9, 0x400, v7\n\tv_xor_b32 v1, 64, v0\n\t"
      "s_add_u32 s54, s34, 0x200000\n\ts_add_u32 s55, s34, 0x201000\n\t"
      RUN("0")
      ST(12)
      PV(176, 191, 244, 247, 152, 159)
      "ds_write_b128 v6, v[160:163]\n\tds_write_b128 v6, v[164:167] offset:1024\n\tds_write_b32 v7, v168\n\t"
      G_LBAR
      "ds_read_b32 v24, v9\n\tds_read_b128 v[16:19], v8\n\tds_read_b128 v[20:23], v8 offset:1024\n\t"
      "s_nop 3\n\t"
      "s_waitcnt lgkmcnt(2)\n\t"
      "v_add_f32 v24, v184, v24\n\tv_rcp_f32 v24, v24\n\t"
      "s_waitcnt lgkmcnt(0)\n\t"
      ".irp i,0,1,2,3,4,5,6,7\n\tv_add_f32 v[16+\\i], v[176+\\i], v[16+\\i]\n\t.endr\n\t"
      "v_mul_f32 v24, v253, v24\n\t"
      ".irp i,0,1,2,3,4,5,6,7\n\tv_mul_f32 v[16+\\i], v24, v[16+\\i]\n\t.endr\n\t"
      ".irp i,0,1,2,3\n\tv_cvt_pk_bf16_f32 v[84+\\i], v[16+2*\\i], v[17+2*\\i]\n\t.endr\n\t"
      ".irp i,0,1,2,3\n\tv_min_u32 v[84+\\i], -2, v[84+\\i]\n\t.endr\n\t"
      G_ST4(84, 4, "s38 offen")
      ST(13)
      P_RB(0) P_RB(1)
      P_USEB(0, 4, "0", "0", P_RB(2) P_RA(0) P_AT(0)) P_USEB(1, 8, G_A0, G_A1, P_RB(3) P_RA(1) P_AT(1))
      P_USEB(2, 12, G_A0, G_A1, P_RB(4) P_RA(2) P_AT(2)) P_USEB(3, 12, G_A0, G_A1, P_RB(5) P_RA(3) P_AT(3))
      P_USEB(4, 12, G_A0, G_A1, P_RB(6) P_RA(4) P_AT(4)) P_USEB(5, 12, G_A0, G_A1, P_RB(7) P_RA(5) P_AT(5))
      P_USEB(6, 12, G_A0, G_A1, P_RA(6) P_AT(6)) P_USEB(7, 8, G_A0, G_A1, P_RA(7) P_AT(7))
      "v_xor_b32 v6, 32, v248\n\tv_xor_b32 v7, 64, v248\n\tv_xor_b32 v8, 96, v248\n\t"
      "v_lshrrev_b32 v9, 7, v4\n\tv_bfe_u32 v10, v4, 3, 4\n\tv_xor_b32 v10, v10, v9\n\tv_lshlrev_b32 v10, 4, v10\n\t"
      "v_lshl_add_u32 v10, v9, 8, v10\n\tv_add_u32 v10, s90, v10\n\tv_xor_b32 v11, 16, v10\n\t"
      "v_and_b32 v12, 0x70, v4\n\tv_lshl_or_b32 v12, v9, 10, v12\n\tv_add_u32 v12, s91, v12\n\t"
      G_LBAR
      P_USEA(0, 7) P_USEA(1, 6) P_USEA(2, 5) P_USEA(3, 4) P_USEA(4, 3) P_USEA(5, 2) P_USEA(6, 1) P_USEA(7, 0)
      "s_nop 10\n\t"
      "ds_write_b128 v248, v[16:19]\n\tds_write_b128 v6, v[20:23]\n\tds_write_b128 v7, v[24:27]\n\tds_write_b128 v8, v[28:31]\n\t"
      "ds_write_b128 v248, v[32:35] offset:128\n\tds_write_b128 v6, v[36:39] offset:128\n\t"
      "ds_write_b128 v7, v[40:43] offset:128\n\tds_write_b128 v8, v[44:47] offset:128\n\t"
      ST(15)
      G_LBAR
      ".irp k,0,1,2,3\n\tds_read_b128 v[16+8*\\k:19+8*\\k], v10 offset:16384*\\k\n\tds_read_b128 v[20+8*\\k:23+8*\\k], v11 offset:16384*\\k\n\t.endr\n\t"
      "s_waitcnt lgkmcnt(4)\n\t"
      ".irp i,0,1,2,3,4,5,6,7\n\tv_add_f32 v[16+\\i], v[16+\\i], v[24+\\i]\n\t.endr\n\t"
      "s_waitcnt lgkmcnt(0)\n\t"
      ".irp i,0,1,2,3,4,5,6,7\n\tv_add_f32 v[32+\\i], v[32+\\i], v[40+\\i]\n\t.endr\n\t"
      ".irp i,0,1,2,3,4,5,6,7\n\tv_add_f32 v[16+\\i], v[16+\\i], v[32+\\i]\n\t.endr\n\t"
      ".irp i,0,1,2,3\n\tv_cvt_pk_bf16_f32 v[16+\\i], v[16+2*\\i], v[17+2*\\i]\n\t.endr\n\t"
      "buffer_store_dwordx4 v[16:19], v12, s[20:23], 0 offen" OUT_POL "\n\t"
      "v_cmp_eq_u32 vcc, 0, v4\n\tv_mov_b32 v20, s68\n\tv_bfrev_b32 v21, 15\n\tv_mov_b32 v22, s86\n\tv_mov_b32 v23, s87\n\t"
      "v_cndmask_b32 v20, v21, v20, vcc\n\t"
      "buffer_store_dwordx2 v[22:23], v20, s[72:75], 0 offen sc1\n\t"
      ST_END
      "s_branch 599f\n\t"
      P_STUBS(57, 56, 580) P_SLOW(580, 96, 54, 55)
      "599:\n\t"
      ".Lgm_code_to:"
      : "+{v[4:7]}"(xa), "+{v8}"(wa), "+{v[144:159]}"(vp), "+{v[10:15]}"(sr.wv), "+{s[24:27]}"(base) ST_OP
      : "{v0}"(pc.wo0), "{v248}"(pc.red), "{v249}"(pc.koff), "{v250}"(pc.voff0), "{v251}"(pc.voff1), "{v254}"(pc.wov), "{s[20:23]}"(pc.out),
        "{s37}"(w), "{s[40:43]}"(sr.ra), "{s[44:47]}"(sr.rb), "{s[48:51]}"(sr.rx), "{s[56:71]}"(sp), "{s[72:75]}"(pc.ws),
        "{s[80:83]}"(pc.wo), "{s91}"(outoff)
      : "memory", "scc", "vcc", "s28", "s29", "s30", "s31", "s32", "s33", "s34", "s35", "s36", "s38", "s39", "s52", "s53", "s54", "s55", "s76", "s77",
        "s84", "s85", "s88", "s89", "s90", "s78", "s79", "s86", "s87", "s92", "s93", "v1", "v2", "v3", "v9", "v252", "v253",
#define R8(a, b, c, d, e, f, g, h) "v" #a, "v" #b, "v" #c, "v" #d, "v" #e, "v" #f, "v" #g, "v" #h
        R8(16, 17, 18, 19, 20, 21, 22, 23), R8(24, 25, 26, 27, 28, 29, 30, 31), R8(32, 33, 34, 35, 36, 37, 38, 39),
        R8(40, 41, 42, 43, 44, 45, 46, 47), R8(48, 49, 50, 51, 52, 53, 54, 55), R8(56, 57, 58, 59, 60, 61, 62, 63),
        R8(64, 65, 66, 67, 68, 69, 70, 71), R8(72, 73, 74, 75, 76, 77, 78, 79), R8(80, 81, 82, 83, 84, 85, 86, 87),
        R8(88, 89, 90, 91, 92, 93, 94, 95), R8(96, 97, 98, 99, 100, 101, 102, 103), R8(104, 105, 106, 107, 108, 109, 110, 111),
        R8(112, 113, 114, 115, 116, 117, 118, 119), R8(120, 121, 122, 123, 124, 125, 126, 127), R8(128, 129, 130, 131, 132, 133, 134, 135),
        R8(136, 137, 138, 139, 140, 141, 142, 143), R8(160, 161, 162, 163, 164, 165, 166, 167), R8(168, 169, 170, 171, 172, 173, 174, 175),
        R8(176, 177, 178, 179, 180, 181, 182, 183), R8(184, 185, 186, 187, 188, 189, 190, 191), R8(192, 193, 194, 195, 196, 197, 198, 199),
        R8(200, 201, 202, 203, 204, 205, 206, 207), R8(208, 209, 210, 211, 212, 213, 214, 215), R8(216, 217, 218, 219, 220, 221, 222, 223),
        R8(224, 225, 226, 227, 228, 229, 230, 231), R8(232, 233, 234, 235, 236, 237, 238, 239), R8(240, 241, 242, 243, 244, 245, 246, 247) ST_CL);
}
DI void identity_seed(int r32, int h, unsigned& val, unsigned& hot) {
  val = 0x3F80u << ((r32 & 1) * 16);
  hot = (((r32 >> 2) & 1) == h ? 1u : 0u) << (((r32 >> 1) & 1) | ((r32 >> 2) & 6));
}

#define HG 2
#define ARGS_AT_ONCE 1
#define TLBALL 0
#define WARM 6
#define CODE_LINES 1
#define DBG_OFF 0u
#define WS_SIZE WS_END

struct Scalars { u32x16 sp; u32x4 base; unsigned outoff; };
DI Scalars scalars(unsigned b, unsigned w, unsigned lds0) {
  const unsigned xc = b & 7, in = b >> 3, g = HG * (xc % (8 / HG)) + in % HG, tb = (32 / HG) * (xc / (8 / HG)) + in / HG;
  const unsigned wi = w & 3, grp = w >> 2, pr = wi & 1, rot = (in / HG + (8 / HG) * (in % HG)) & 7;
  const unsigned hd = 4 * g + wi, otb = 4 * xc + (in & 3), og = in >> 2, rh = 4 * g + (tb & 3), sl = tb >> 2;
  Scalars s;
  s.sp = u32x16{grp, lds0 + L_RING + wi * 2048, lds0 + L_X + wi * 2048, rot, rot * 128, wi >> 1, tb * 2048, ((tb + 1) & 31) * 2048,
                ((tb + 2) & 31) * 2048, L_Q + (wi >> 1) * (L_VB - L_Q) + pr * 4096, L_Q + wi * 2048 + grp * 1024, L_PQ + pr * 8192,
                (b | w) ? OOB : WS_EPOCH, L_VB + pr * 4096, DBG_OFF, 0x3FB8AA3Bu  };
  s.base = u32x4{hd * 1024, rh * 8 + sl, ((otb * 2 + grp) * 32 + wi * 8) * 1024, 0u};
  s.outoff = (64 * otb + 8 * w) * 1024 + og * 128;
  return s;
}

__global__ __launch_bounds__(512, 2) void gated_mix_kernel(const char* __restrict__ x, const char* __restrict__ wqa,
                                                           const char* __restrict__ woa, const char* __restrict__ wqb,
                                                           const char* __restrict__ wphi, const char* __restrict__ wob,
                                                           const char* __restrict__ gatew, char* __restrict__ out,
                                                           char* __restrict__ ws) {
  __shared__ __attribute__((aligned(16))) char lds_raw[L_BYTES];
  LAS char* lds = (LAS char*)lds_raw;
  const unsigned lds0 = (unsigned)(uintptr_t)lds;
  const int tid = threadIdx.x, lane = tid & 63, w = __builtin_amdgcn_readfirstlane(tid >> 6);
  const int xc = blockIdx.x & 7, in = blockIdx.x >> 3;
  const int g = HG * (xc % (8 / HG)) + in % HG, tb = (32 / HG) * (xc / (8 / HG)) + in / HG;
  const int r32 = lane & 31, h = lane >> 5;
  const int wi = w & 3, grp = w >> 2, pr = wi & 1;
  asm volatile("" : "+s"(x), "+s"(wqa), "+s"(woa), "+s"(wqb), "+s"(wphi), "+s"(wob), "+s"(gatew), "+s"(out), "+s"(ws));
  STAMP(0); STAMP_RT(62);

  const int l8 = lane >> 3, dcp = lane & 7;
  Src sr;
  sr.ra = __builtin_amdgcn_make_buffer_rsrc((void*)wqa, 0, 512 * 3072, 0x00020000);
  sr.rx = __builtin_amdgcn_make_buffer_rsrc((void*)x, 0, 2048 * 1024, 0x00020000);
  const unsigned wv = (unsigned)(16 * wi + l8) * 3072 + g * 128 + ((dcp ^ ((lane >> 2) & 4)) * 16);
#pragma unroll
  for (int c = 0; c < 3; ++c) { sr.wv[2 * c] = wv + c * 1024; sr.wv[2 * c + 1] = wv + c * 1024 + 8 * 3072 - 1024; }
  const unsigned rot = (in / HG + (8 / HG) * (in % HG)) & 7;
  const unsigned xv0 = (unsigned)(64 * tb + 16 * wi + l8) * 1024 + ((dcp ^ (l8 >> 1)) * 16), xv1 = ((xv0 + 8192) ^ 64) - 1024;
  const unsigned ml = lds0 + L_RING + wi * 2048, mx = lds0 + L_X + wi * 2048;
  const unsigned rotx = rot * 128;
  if (grp) first_chunks(sr, xv0, xv1, ml, mx, rot, rotx);
  SBAR();
  const unsigned hd = 4 * g + wi, og = in >> 2;
  sr.rb = __builtin_amdgcn_make_buffer_rsrc((void*)wqb, 0, 512 * 3072, 0x00020000);
  const rsrc_t rws = __builtin_amdgcn_make_buffer_rsrc((void*)ws, 0, WS_SIZE, 0x00020000);
  if (grp == 0) {
    if (wi < 2) dma16(__builtin_amdgcn_make_buffer_rsrc((void*)wphi, 0, 32 * 512, 0x00020000), lane * 16, g * 2048 + wi * 1024, lds0 + L_WPHI + wi * 1024);
    if (wi == 2) dma16(__builtin_amdgcn_make_buffer_rsrc((void*)gatew, 0, 1024, 0x00020000), lane * 16, 0, lds0 + L_GW);
    {
      const unsigned sh = in / HG, nsh = 32 / HG;
      if (wi < 2 * HG / 2 + (HG == 1)) {
        const unsigned i = (sh * HG + wi % HG) * 64 + lane;
        const unsigned row = (i >> 1) & 511, sg = i & 1, st = i >> 10;
        const rsrc_t r = (st && !sg) ? sr.ra : sr.rb;
      }
      if (wi == 0) {
        asm volatile("s_mov_b32 m0, %0\n\ts_nop 0\n\tbuffer_load_dword %1, %2, 0 offen sc1 lds" :: "s"(lds0 + L_DUMMY), "v"((TLBALL ? lane + 64 * (in & 3) : in * 8 + l8) * 65536 + 0xFF80), "s"(rws) : "memory");
      }
      if (wi == 1) {
        const rsrc_t ro = __builtin_amdgcn_make_buffer_rsrc((void*)out, 0, 2048 * 1024, 0x00020000);
        asm volatile("s_mov_b32 m0, %0\n\ts_nop 0\n\tbuffer_load_dword %1, %2, 0 offen sc1 lds" :: "s"(lds0 + L_DUMMY), "v"((TLBALL ? (lane & 31) : in) * 65536 + 0xFF80), "s"(ro) : "memory");
      }
      if (wi == 2) {
        const unsigned i = blockIdx.x * 32 + (lane & 31);
        const rsrc_t r = __builtin_amdgcn_make_buffer_rsrc((void*)(blockIdx.x & 128 ? wob : woa), 0, 512 * 1024, 0x00020000);
        asm volatile("s_mov_b32 m0, %0\n\ts_nop 0\n\tbuffer_load_dword %1, %2, 0 offen lds" :: "s"(lds0 + L_DUMMY), "v"((i & 4095) * 128), "s"(r) : "memory");
      }
    }
    if (wi == 3) {
      unsigned long long pc;
      unsigned len;
      asm volatile("s_getpc_b64 %0\n.Lgm_code_from:\n\ts_mov_b32 %1, .Lgm_code_to-.Lgm_code_from" : "=s"(pc), "=s"(len));
      const rsrc_t rc = __builtin_amdgcn_make_buffer_rsrc((void*)pc, 0, len, 0x00020000);
      asm volatile("s_mov_b32 m0, %0\n\ts_nop 0\n\tbuffer_load_dword %1, %2, 0 offen lds" :: "s"(lds0 + L_DUMMY), "v"((in + 32 * l8) * 128), "s"(rc) : "memory");
    }
  }

  const unsigned kvb = WS_KV + (unsigned)hd * 131072, kvh = kvb + grp * 65536;
  Prog pc;
  pc.ws = rws;
  pc.koff = kvh + lane * 16;
  {
    const unsigned vreal = kvh + h * 256 + (lane & 15) * 16, vones = WS_ONES + g * 256;
    const bool pad = lane & 16, one = (lane & 11) == 0;
    pc.voff0 = pad ? (one ? vones - 1024 : OOB) : vreal; pc.voff1 = pad ? (one ? vones - 1536 : OOB) : vreal;
  }
  const int tq = (lane & 15) >> 2, tp_ = lane & 3, tnb = (lane >> 4) & 1;
  const unsigned wlane = L_RING + (8 * h + tq) * 128 + (((2 * pr + tnb) ^ (tq & 2)) * 32) + tp_ * 8;
  u32x4 xa;
#pragma unroll
  for (int s = 0; s < 4; ++s) xa[s] = L_X + r32 * 128 + (((2 * s + h) ^ ((r32 >> 1) & 7)) * 16);
  unsigned vp[16];
  if (grp == 0) {
    const unsigned kb = WS_KV + (unsigned)(4 * g + 2 * pr) * 131072 + (unsigned)(2 * tb) * 2048, fl = WS_FKV + (g * 32 + tb) * FL;
    const int sw = (r32 & 19) | ((r32 & 4) << 1) | ((r32 & 8) >> 1);
    vp[0] = vp[1] = vp[2] = vp[3] = vp[4] = 0;
    vp[5] = wi < 2 ? kb + h * 512 + sw * 16 : kb + (r32 >> 4) * 131072 + 1024 + h * 256 + (r32 & 15) * 16;
    vp[6] = wi < 2 ? (r32 == 0 ? fl + (2 * pr + h) * 16 + 8 : OOB) : (wi == 2 && lane < 8 ? WS_ONES + tb * 2048 + g * 256 + lane * 16 : OOB);
    vp[7] = w == 0 ? WS_KR + ((4 * g + (tb & 3)) * 8 + (tb >> 2)) * 256 + lane * 4 : OOB;
    vp[8] = L_X + r32 * 128 + (((2 * wi + h) ^ ((r32 >> 1) & 7)) * 16);
    vp[9] = L_GW + rotx + wi * 32 + h * 16;
    vp[10] = L_GP + wi * 512 + lane * 4;
    vp[11] = tid < 4 ? fl + lane * 16 : OOB;
  } else {
    vp[0] = xv0; vp[1] = xv1; vp[4] = vp[7] = vp[9] = vp[11] = 0;
    vp[2] = L_WPHI + 2 * pr * 512 + (4 * h + tq) * 32 + tp_ * 8;
    vp[3] = tnb ? 0x80008000u : 0u;
    vp[5] = WS_KP + (unsigned)(((4 * g + 2 * pr) * 8 + (lane >> 4)) * 32 + tb) * 256 + (lane & 15) * 16;
    vp[6] = lane == 0 ? WS_FKP + ((g * 2 + pr) * 32 + tb) * FL : OOB;
    vp[8] = tnb ? 0x3F800000u : 0u;
    vp[10] = L_GP + 256 + lane * 4;
  }
  {
    unsigned val, hot;
    identity_seed(r32, h, val, hot);
    vp[12] = val; vp[13] = hot; vp[14] = WS_FKV + (g * 32 + r32) * FL + wi * 16; vp[15] = lane * 16;
  }
  pc.out = __builtin_amdgcn_make_buffer_rsrc((void*)out, 0, 2048 * 1024, 0x00020000);
  pc.wo = __builtin_amdgcn_make_buffer_rsrc((void*)(grp ? woa : wob), 0, 512 * 1024, 0x00020000);
  pc.wov = (unsigned)(128 * wi + l8) * 1024 + og * 128 + ((dcp ^ ((lane >> 2) & 4)) * 16);
  pc.wo0 = L_WO + wi * 32768 + (unsigned)(4 * h + tq) * 128 + ((tnb ^ (tq & 2)) * 32) + tp_ * 8;
  pc.red = L_RED + wi * 16384 + (grp * 32 + r32) * 256 + ((h ^ (r32 & 7)) * 16);

  unsigned bi = blockIdx.x, wv_ = w;
  asm volatile("" : "+s"(bi), "+s"(wv_));
  const Scalars sc = scalars(bi, wv_, lds0);
  program(sr, xa, wlane + (wi >> 1) * 8192,
          u32x16{vp[0], vp[1], vp[2], vp[3], vp[4], vp[5], vp[6], vp[7], vp[8], vp[9], vp[10], vp[11], vp[12], vp[13], vp[14], vp[15]}, sc.sp,
          sc.base, wv_, sc.outoff, pc ST_ARG);
}

__global__ void post_kernel(char* ws) { }
}

extern "C" void kernel_launch(void* const* d_in, const int* in_sizes, int n_in, void* d_out, int out_size, void* d_ws,
                              size_t ws_size, hipStream_t stream) {
  gated_mix_kernel<<<256, 512, 0, stream>>>((const char*)d_in[0], (const char*)d_in[1], (const char*)d_in[2], (const char*)d_in[4],
                                            (const char*)d_in[5], (const char*)d_in[6], (const char*)d_in[7], (char*)d_out, (char*)d_ws);
  post_kernel<<<8, 64, 0, stream>>>((char*)d_ws);
}
```

```cpp
#include <hip/hip_runtime.h>
#include <hip/hip_bf16.h>
#include <stdint.h>

namespace {

#define LAS __attribute__((address_space(3)))
#define DI __device__ __forceinline__

constexpr unsigned WS_EPOCH = 0;
constexpr unsigned FL = 128;
constexpr unsigned WS_KR = 0x10000;
constexpr unsigned WS_ONES = 0x40000;
constexpr unsigned WS_FKV = 0x60000;
constexpr unsigned WS_FKP = 0x70000;
constexpr unsigned WS_FOB = 0x80000;
constexpr unsigned WS_KP = 0x100000;
constexpr unsigned WS_OB = 0x500000;
constexpr unsigned WS_OA = 0x700000;
constexpr unsigned WS_KV = 0x900000;
constexpr unsigned WS_END = 0xD00000;
#define WS_LEN 0xD00000

constexpr unsigned L_X = 0;
constexpr unsigned L_RING = 65536;
constexpr unsigned L_WO = 0;
constexpr unsigned L_RED = 0;
constexpr unsigned L_PQ = 131072;
constexpr unsigned L_Q = 147456;
constexpr unsigned L_WPHI = L_Q;
constexpr unsigned L_GW = L_Q + 2048;
constexpr unsigned L_DUMMY = L_Q + 3072;
constexpr unsigned L_GP = L_Q + 4096;
constexpr unsigned L_VB = 155648;
constexpr unsigned L_BYTES = 163840;

#define EX(r) "v_exp_f32 v" #r ", v" #r "\n\t"
#define QK(s0, s1, k0, k1, q0, q1, c0, c1) "v_mfma_f32_32x32x16_bf16 v[" #s0 ":" #s1 "], v[" #k0 ":" #k1 "], v[" #q0 ":" #q1 "], v[" #c0 ":" #c1 "]\n\t"
#define PV(o0, o1, a0, a1, p0, p1) "v_smfmac_f32_32x32x32_bf16 v[" #o0 ":" #o1 "], v[" #a0 ":" #a1 "], v[" #p0 ":" #p1 "], v5\n\t"
#define LOADS(k0, k1, a0, a1, b0, b1)                                                     \
  "s_add_u32 s76, s76, 0x800\n\t"                                                         \
  "s_and_b32 s76, s76, 0xffff\n\t"                                                        \
  "buffer_load_dwordx4 v[" #k0 ":" #k1 "], v249, s[72:75], s76 offen sc1\n\t"             \
  "buffer_load_dwordx4 v[" #a0 ":" #a1 "], v250, s[72:75], s76 offen offset:1024 sc1\n\t" \
  "buffer_load_dwordx4 v[" #b0 ":" #b1 "], v251, s[72:75], s76 offen offset:1536 sc1\n\t"
#define WODMA                                                                             \
  "s_mov_b32 m0, s84\n\t"                                                                 \
  "s_add_u32 s84, s84, 0x400\n\t"                                                         \
  "buffer_load_dwordx4 v254, s[80:83], s85 offen lds\n\t"                                 \
  "s_add_u32 s85, s85, 0x2000\n\t"
#define ITER(ck0, ck1, ca0, ca1, cb0, cb1, pk0, pk1, pa0, pa1, pb0, pb1, nk0, nk1, EXTRA, WAIT) \
  EX(128) EX(129) PV(176, 191, pb0, pb1, 152, 159) EX(130) EX(131)                        \
  LOADS(pk0, pk1, pa0, pa1, pb0, pb1)                                                     \
  EX(132) EX(133) QK(144, 159, ck0, ck1, 196, 199, 112, 127) EX(134) EX(135) EXTRA EX(136) EX(137) EX(138) \
  PV(160, 175, ca0, ca1, 128, 135) EX(139) EX(140) EX(141) EX(142) EX(143)                 \
  EX(144) EX(145) PV(160, 175, cb0, cb1, 136, 143) EX(146) EX(147) EX(148) EX(149)         \
  WAIT                                                                                    \
  QK(128, 143, nk0, nk1, 192, 195, 96, 111) EX(150) EX(151) EX(152) EX(153) EX(154)      \
  PV(176, 191, ca0, ca1, 144, 151) EX(155) EX(156) EX(157) EX(158) EX(159)

#define ATTN_ROUTINE                                                                                  \
  "400:\n\t"                                                                                          \
  ITER(200, 203, 216, 219, 220, 223, 212, 215, 240, 243, 244, 247, 204, 207, WODMA, "")               \
  ITER(204, 207, 224, 227, 228, 231, 200, 203, 216, 219, 220, 223, 208, 211, "", "s_waitcnt vmcnt(7)\n\t")    \
  "s_cbranch_vccnz 402f\n\t401:\n\t"                                                                 \
  ITER(208, 211, 232, 235, 236, 239, 204, 207, 224, 227, 228, 231, 212, 215, WODMA, "s_waitcnt vmcnt(8)\n\t") \
  ITER(212, 215, 240, 243, 244, 247, 208, 211, 232, 235, 236, 239, 200, 203, "", "s_waitcnt vmcnt(7)\n\t")    \
  "s_sub_u32 s77, s77, 1\n\ts_waitcnt vmcnt(3)\n\ts_cbranch_scc0 400b\n\ts_setpc_b64 s[78:79]\n\t"  \
  "402:\n\ts_setprio 0\n\ts_branch 401b\n\t"
#define RUNV(N, V) "s_mov_b64 vcc, " V "\n\ts_mov_b32 s77, " N "\n\ts_swappc_b64 s[78:79], s[92:93]\n\t"
#define RUN(N) RUNV(N, "0")

#define STAMP(i) do { } while (0)
#define STAMP_RT(i) do { } while (0)
#define ST(i) ""
#define ST_END ""
#define ST_OP
#define ST_CL
#define ST_PAR
#define ST_ARG

#define G_BAR(N) "s_waitcnt vmcnt(" #N ") lgkmcnt(0)\n\ts_barrier\n\t"
#define G_LBAR "s_waitcnt lgkmcnt(0)\n\ts_barrier\n\t"
#define G_EPOCH "buffer_load_dwordx2 v[2:3], off, s[72:75], 0 sc1\n\t"
#define G_EPOCH_USE "v_readfirstlane_b32 s86, v2\n\tv_readfirstlane_b32 s87, v3\n\ts_add_u32 s86, s86, 1\n\ts_addc_u32 s87, s87, 0\n\t"
#define D_ROW(KC) "s_xor_b32 s52, s59, " KC "\n\ts_mul_i32 s52, s52, 196608\n\t"
#define D_A(R, COL, LOFF) "s_add_u32 m0, s57, " LOFF "\n\ts_nop 0\n\tbuffer_load_dwordx4 v[10+2*" #COL "], " R ", s52 offen lds\n\t"
#define D_B(R, COL) "buffer_load_dwordx4 v[11+2*" #COL "], " R ", s52 offen offset:1024 lds\n\t"
#define RA "s[40:43]"
#define RB "s[44:47]"
#define D_X(C)                                                                                        \
  "s_add_u32 m0, s58, 8192*" #C "\n\ts_xor_b32 s52, s60, 128*" #C "\n\t"                              \
  "buffer_load_dwordx4 v144, s[48:51], s52 offen lds\n\tbuffer_load_dwordx4 v145, s[48:51], s52 offen offset:1024 lds\n\t"
#define D_W(N, R0, C0, R1, C1) D_ROW(#N "&7") D_A(R0, C0, "16384*(" #N "&3)"), D_B(R0, C0), D_A(R1, C1, "16384*(" #N "&3)+8192"), D_B(R1, C1)
#define D_S0(N) D_W(N, RA, 1, RA, 2)
#define D_S1(N) D_W(N, RB, 0, RB, 1)
#define D_S2(N) D_W(N, RA, 0, RB, 2)
#define G_CAT(A, B, C, D) A B C D
#define G_APPLY(M, ...) M(__VA_ARGS__)
#define G_ALL(L) G_APPLY(G_CAT, L)

#define G_RW(W, C, S)                                                                                 \
  "ds_read_b64_tr_b16 v[" #W "+4*" #S ":" #W "+4*" #S "+1], v8 offset:16384*(" #C "&3)+2048*" #S "\n\t"       \
  "ds_read_b64_tr_b16 v[" #W "+4*" #S "+2:" #W "+4*" #S "+3], v8 offset:16384*(" #C "&3)+2048*" #S "+512\n\t"
#define G_RX(X, C, S, T) "ds_read_b128 v[" #X "+4*" #S ":" #X "+4*" #S "+3], v[4+" #S "] offset:8192*" #C "+4096*" #T "\n\t"
#define G_RS(W, X0, X1, C, S) G_RW(W, C, S) G_RX(X0, C, S, 0) G_RX(X1, C, S, 1)
#define G_READ(W, X0, X1, C) G_RS(W, X0, X1, C, 0) G_RS(W, X0, X1, C, 1) G_RS(W, X0, X1, C, 2) G_RS(W, X0, X1, C, 3)
#define G_M(ACC, W, X, S, CIN) \
  "v_mfma_f32_32x32x16_bf16 " ACC ", v[" #W "+4*" #S ":" #W "+4*" #S "+3], v[" #X "+4*" #S ":" #X "+4*" #S "+3], " CIN "\n\t"
#define G_A0 "v[16:31]"
#define G_A1 "v[32:47]"
#define G_MMA2(W, X0, X1, S) G_M(G_A0, W, X0, S, G_A0) G_M(G_A1, W, X1, S, G_A1)
#define G_STEP(RW, RX0, RX1, C, W, X0, X1, C0, C1, D0, D1, D2, D3)                                    \
  G_M(G_A0, W, X0, 0, C0) G_RW(RW, C, 0) G_RX(RX0, C, 0, 0) G_M(G_A1, W, X1, 0, C1) G_RX(RX1, C, 0, 1) G_RW(RW, C, 1) \
  G_M(G_A0, W, X0, 1, G_A0) G_RX(RX0, C, 1, 0) G_RX(RX1, C, 1, 1) D0 G_M(G_A1, W, X1, 1, G_A1) G_RW(RW, C, 2) G_RX(RX0, C, 2, 0) \
  G_M(G_A0, W, X0, 2, G_A0) G_RX(RX1, C, 2, 1) G_RW(RW, C, 3) D1 G_M(G_A1, W, X1, 2, G_A1) G_RX(RX0, C, 3, 0) G_RX(RX1, C, 3, 1) \
  G_M(G_A0, W, X0, 3, G_A0) D2 G_M(G_A1, W, X1, 3, G_A1) D3
#define G_EVEN(C, D0, D1, D2, D3) G_STEP(48, 64, 80, C, 96, 112, 128, G_A0, G_A1, D0, D1, D2, D3)
#define G_ODD(C, D0, D1, D2, D3) G_STEP(96, 112, 128, C, 48, 64, 80, G_A0, G_A1, D0, D1, D2, D3)
#define G_FIRST(D0, D1, D2, D3) G_STEP(96, 112, 128, 1, 48, 64, 80, "0", "0", D0, D1, D2, D3)
#define A_PACK                                                                                        \
  ".irp i,0,1,2,3\n\tv_cvt_pk_bf16_f32 v[48+\\i], v[16+2*\\i], v[17+2*\\i]\n\tv_cvt_pk_bf16_f32 v[56+\\i], v[24+2*\\i], v[25+2*\\i]\n\t.endr\n\t" \
  ".irp i,0,1,2,3\n\tv_cvt_pk_bf16_f32 v[52+\\i], v[32+2*\\i], v[33+2*\\i]\n\tv_cvt_pk_bf16_f32 v[60+\\i], v[40+2*\\i], v[41+2*\\i]\n\t.endr\n\t"
#define G_Z(L) ".irp r," L "\n\tv_mov_b64 v[\\r:\\r+1], 0\n\t.endr\n\t"
#define G_ZO G_Z("160,162,164,166,168,170,172,174,176,178,180,182,184,186,188,190,244,246")
#define G_QPRE "v_mov_b32 v254, v148\n\tv_add_u32 v6, s66, v159\n\tv_mov_b32 v4, v159\n\tv_xor_b32 v7, 0x400, v6\n\tv_mov_b32 v5, 0xdddddddd\n\t"
#define G_QRD "ds_read_b128 v[192:195], v7\n\tds_read_b128 v[196:199], v6\n\t"
#define G_C(L) ".irp r," L "\n\tv_mov_b32 v\\r, 0xc2400000\n\t.endr\n\t"
#define G_C0 G_C("96,97,98,99,100,101,102,103,104,105,106,107,108,109,110,111")
#define G_C1 G_C("112,113,114,115,116,117,118,119,120,121,122,123,124,125,126,127")
#define G_ST4(R, V, O) "buffer_store_dwordx4 v[" #R ":" #R "+3], v" #V ", s[72:75], " O " sc1\n\t"
#define G_POLL(R) "buffer_load_dwordx3 v[" #R ":" #R "+2], v158, s[72:75], 0 offen sc1\n\t"
#define G_SPIN(L, AGAIN) "s_cbranch_vccz " #L "1f\n\t" AGAIN "s_sub_u32 s53, s53, 1\n\ts_cbranch_scc0 " #L "0b\n\ts_mov_b32 s53, 0\n\t" #L "1:\n\t"
#define G_POLLWAIT(L, R)                                                                              \
  #L "0:\n\ts_waitcnt vmcnt(0)\n\tv_cmp_ne_u64 vcc, s[86:87], v[" #R ":" #R "+1]\n\t"                  \
  G_SPIN(L, "s_sleep 4\n\t" G_POLL(R)) "s_waitcnt vmcnt(0)\n\tv_mov_b32 v9, v[" #R "+2]\n\t"
#define G_KV1(K, V, T)                                                                                \
  "buffer_load_dwordx4 v[" #K ":" #K "+3], v249, s[72:75], s" #T " offen sc1\n\t"                      \
  "buffer_load_dwordx4 v[" #V ":" #V "+3], v250, s[72:75], s" #T " offen offset:1024 sc1\n\t"          \
  "buffer_load_dwordx4 v[" #V "+4:" #V "+7], v251, s[72:75], s" #T " offen offset:1536 sc1\n\t"
#define G_KV G_KV1(200, 216, 62) G_KV1(204, 224, 63) G_KV1(208, 232, 64)
#define DPPMAX(R, CTRL) "v_max_f32_dpp v" #R ", v" #R ", v" #R " " CTRL " row_mask:0xf bank_mask:0xf\n\t"
#define SIGMOID(R, OUT) \
  "v_mul_f32 v" #R ", 0xbfb8aa3b, v" #R "\n\tv_exp_f32 v" #R ", v" #R "\n\ts_nop 0\n\tv_add_f32 v" #R ", 1.0, v" #R "\n\tv_rcp_f32 v" #OUT ", v" #R "\n\t"
#define A_GREAD(C)                                                                                    \
  "v_xor_b32 v92, 128*" #C ", v153\n\tv_xor_b32 v93, 128*(" #C "+1), v153\n\t"                     \
  "ds_read_b128 v[72:75], v152 offset:8192*" #C "\n\tds_read_b128 v[80:83], v152 offset:8192*" #C "+4096\n\t" \
  "ds_read_b128 v[76:79], v152 offset:8192*(" #C "+1)\n\tds_read_b128 v[84:87], v152 offset:8192*(" #C "+1)+4096\n\t" \
  "ds_read_b128 v[64:67], v92\n\tds_read_b128 v[68:71], v93\n\t"
#define A_GDOT                                                                                        \
  ".irp j,0,1,2,3\n\t"                                                                                \
  "v_dot2c_f32_bf16 v88, v[72+\\j], v[64+\\j]\n\tv_dot2c_f32_bf16 v89, v[80+\\j], v[64+\\j]\n\t"       \
  "v_dot2c_f32_bf16 v90, v[76+\\j], v[68+\\j]\n\tv_dot2c_f32_bf16 v91, v[84+\\j], v[68+\\j]\n\t"       \
  ".endr\n\t"
#define A_DPPMAX(CTRL) "s_nop 1\n\tv_max_f32_dpp v96, v96, v96 " CTRL " row_mask:0xf bank_mask:0xf\n\t"
#define F_JOBS(M) M(16, 80, 48) M(32, 84, 56) M(96, 88, 64) M(112, 92, 72)
#define F_MAX1(E, T, P)                                                                               \
  "v_max3_f32 v[" #T "], v[" #E "], v[" #E "+1], v[" #E "+2]\n\t"                                      \
  "v_max3_f32 v[" #T "+2], v[" #E "+8], v[" #E "+9], v[" #E "+10]\n\t"                                 \
  "v_max3_f32 v[" #T "+1], v[" #E "+3], v[" #E "+4], v[" #E "+5]\n\t"                                  \
  "v_max3_f32 v[" #T "+3], v[" #E "+11], v[" #E "+12], v[" #E "+13]\n\t"                               \
  "v_max3_f32 v[" #T "], v[" #T "], v[" #E "+6], v[" #E "+7]\n\t"                                      \
  "v_max3_f32 v[" #T "+2], v[" #T "+2], v[" #E "+14], v[" #E "+15]\n\t"                                \
  "v_max_f32 v[" #T "], v[" #T "], v[" #T "+1]\n\t"                                                    \
  "v_max_f32 v[" #T "+2], v[" #T "+2], v[" #T "+3]\n\t"
#define F_SWAP(E, T, P) "v_permlane32_swap_b32 v[" #T "], v[" #T "+2]\n\t"
#define F_MAX2(E, T, P) "v_max_f32 v[" #T "], v[" #T "], v[" #T "+2]\n\t"
#define F_NEG(E, T, P) "v_mul_f32 v[" #T "], 0xbfb8aa3b, v[" #T "]\n\t"
#define F_DUP(E, T, P) "v_mov_b32 v[" #T "+2], v[" #T "]\n\t"
#define F_FMA(E, T)                                                                                   \
  ".irp i,0,1,2,3,4,5,6,7\n\t"                                                                        \
  "v_fma_f32 v[" #E "+\\i], v[" #E "+\\i], s71, v[" #T "]\n\t"                                      \
  "v_fma_f32 v[" #E "+8+\\i], v[" #E "+8+\\i], s71, v[" #T "+2]\n\t"                                \
  ".endr\n\t"
#define F_EX(E, L) ".irp i," L "\n\tv_exp_f32 v[" #E "+\\i], v[" #E "+\\i]\n\t.endr\n\t"
#define F_EXP(E, T) F_FMA(E, T) F_EX(E, "0,8,1,9,2,10,3,11,4,12,5,13,6,14,7,15")
#define F_SUM1(E, T, P) ".irp i,0,1,2,3,4,5,6,7\n\tv_add_f32 v[" #P "+\\i], v[" #E "+2*\\i], v[" #E "+2*\\i+1]\n\t.endr\n\t"
#define F_SUM2(E, T, P) ".irp i,0,2,4,6\n\tv_add_f32 v[" #P "+\\i], v[" #P "+\\i], v[" #P "+\\i+1]\n\t.endr\n\t"
#define F_SUM3(E, T, P) "v_add_f32 v[" #T "], v[" #P "], v[" #P "+2]\n\tv_add_f32 v[" #T "+2], v[" #P "+4], v[" #P "+6]\n\t"
#define F_ADD(E, T, P) "v_add_f32 v[" #T "], v[" #T "], v[" #T "+2]\n\t"
#define F_RCP(E, T, P) "v_rcp_f32 v[" #T "], v[" #T "]\n\t"
#define F_PACK(E, T, P) ".irp i,0,1,2,3,4,5,6,7\n\tv_cvt_pk_bf16_f32 v[" #P "+\\i], v[" #E "+2*\\i], v[" #E "+2*\\i+1]\n\t.endr\n\t"
#define F_SCALE(E, T, P)                                                                              \
  ".irp i,0,1,2,3,4,5,6,7\n\t"                                                                        \
  "v_mul_f32 v[" #E "+\\i], v[" #E "+\\i], v[" #T "]\n\t"                                              \
  "v_mul_f32 v[" #E "+8+\\i], v[" #E "+8+\\i], v[" #T "+2]\n\t"                                        \
  ".endr\n\t" F_PACK(E, T, P)
#define F_TR0(E, T, P) "v_mfma_f32_32x32x16_bf16 v[" #E ":" #E "+15], v[" #P ":" #P "+3], v[128:131], 0\n\t"
#define F_TR1(E, T, P) "v_mfma_f32_32x32x16_bf16 v[" #E ":" #E "+15], v[" #P "+4:" #P "+7], v[132:135], v[" #E ":" #E "+15]\n\t"
#define P_TEST(F, I)                                                                                  \
  "v_max3_u32 v80, v[" #F "+4*" #I "], v[" #F "+1+4*" #I "], v[" #F "+2+4*" #I "]\n\t"                 \
  "v_max_u32 v80, v80, v[" #F "+3+4*" #I "]\n\t"                                                       \
  "v_cmp_eq_u32 vcc, -1, v80\n\t"
#define P_LOAD(F, I, SO) "buffer_load_dwordx4 v[" #F "+4*" #I ":" #F "+3+4*" #I "], v4, s[72:75], s" #SO " offen offset:1024*(" #I "&3) sc1\n\t"
#define P_RD(R, I, OFF)                                                                               \
  "ds_read_b64_tr_b16 v[" R ":" R "+1], v0 offset:" #OFF "+2048*" #I "\n\t"                         \
  "ds_read_b64_tr_b16 v[" R "+2:" R "+3], v0 offset:" #OFF "+2048*" #I "+1024\n\t"                  \
  "ds_read_b64_tr_b16 v[" R "+4:" R "+5], v1 offset:" #OFF "+2048*" #I "\n\t"                       \
  "ds_read_b64_tr_b16 v[" R "+6:" R "+7], v1 offset:" #OFF "+2048*" #I "+1024\n\t"
#define P_RB(I) P_RD("192+8*(" #I "&3)", I, 0)
#define P_RA(I) P_RD("128+8*" #I, I, 16384)
#define P_MM(W, F, I, C0, C1)                                                                         \
  "v_mfma_f32_32x32x16_bf16 v[16:31], v[" W ":" W "+3], v[" #F "+4*" #I ":" #F "+3+4*" #I "], " C0 "\n\t"   \
  "v_mfma_f32_32x32x16_bf16 v[32:47], v[" W "+4:" W "+7], v[" #F "+4*" #I ":" #F "+3+4*" #I "], " C1 "\n\t"
#define P_USEB(I, N, C0, C1, MORE)                                                                    \
  "s_waitcnt lgkmcnt(" #N ")\n\t" P_MM("192+8*(" #I "&3)", 48, I, C0, C1) MORE
#define P_USEA(I, N) "s_waitcnt vmcnt(" #N ")\n\t" P_TEST(96, I) "s_cbranch_vccnz 57" #I "f\n\t56" #I ":\n\t" P_MM("128+8*" #I, 96, I, G_A0, G_A1)
#define P_STUB(A, B, I, R) #A #I ":\n\ts_call_b64 s[78:79], " #R "f\n\ts_branch " #B #I "b\n\t"
#define P_STUBS(A, B, R) P_STUB(A, B, 0, R) P_STUB(A, B, 1, R) P_STUB(A, B, 2, R) P_STUB(A, B, 3, R) P_STUB(A, B, 4, R) P_STUB(A, B, 5, R) P_STUB(A, B, 6, R) P_STUB(A, B, 7, R)
#define P_RETRY(F, I, SO) P_TEST(F, I) "s_cbranch_vccz 1f\n\ts_mov_b32 s52, 1\n\t" P_LOAD(F, I, SO) "1:\n\t"
#define P_SLOW(R, F, S0, S1)                                                                          \
  #R ":\n\ts_waitcnt vmcnt(0)\n\ts_mov_b32 s52, 0\n\t"                                             \
  P_RETRY(F, 0, S0) P_RETRY(F, 1, S0) P_RETRY(F, 2, S0) P_RETRY(F, 3, S0) P_RETRY(F, 4, S1) P_RETRY(F, 5, S1) P_RETRY(F, 6, S1) P_RETRY(F, 7, S1) \
  "s_sub_u32 s53, s53, 1\n\ts_cselect_b32 s52, 0, s52\n\ts_cselect_b32 s53, 0, s53\n\t" \
  "s_cmp_lg_u32 s52, 0\n\ts_cbranch_scc1 " #R "b\n\ts_waitcnt vmcnt(0)\n\ts_setpc_b64 s[78:79]\n\t"
#define P_LOAD8(F, S0, S1) P_LOAD(F, 0, S0) P_LOAD(F, 1, S0) P_LOAD(F, 2, S0) P_LOAD(F, 3, S0) P_LOAD(F, 4, S1) P_LOAD(F, 5, S1) P_LOAD(F, 6, S1) P_LOAD(F, 7, S1)
#define P_LOADA P_LOAD8(96, 54, 55)
#define OUT_POL " sc1"
#define KV_PRIO 3
#define SWITCH_AT 1
#define SW_A "0"
#define SW_B "s_nop 0\n\t"
#define SW_C "s_sub_u32 s4, s56, 1\n\ts_mov_b32 s5, s4\n\t" RUNV("0", "s[4:5]")
#define A_AT 3
#define P_AT(I) ".if " STR(A_AT) " == " #I "\n\t" P_LOADA ".endif\n\t"
#define STR_(x) #x
#define STR(x) STR_(x)
#define M_PAR0                                                                                        \
  "s_lshl_b32 s62, s17, 11\n\ts_add_u32 s63, s62, 0x800\n\ts_and_b32 s63, s63, 0xffff\n\ts_add_u32 s64, s62, 0x1000\n\ts_and_b32 s64, s64, 0xffff\n\t" \
  "s_lshl_b32 s24, s16, 5\n\ts_lshl_b32 s4, s3, 10\n\ts_add_u32 s24, s24, s4\n\t"                       \
  "s_lshr_b32 s25, s16, 2\n\ts_and_b32 s4, s17, 3\n\ts_lshl_b32 s4, s4, 3\n\ts_add_u32 s25, s25, s4\n\ts_lshr_b32 s4, s17, 2\n\ts_add_u32 s25, s25, s4\n\t" \
  "s_lshl_b32 s29, s25, 13\n\ts_add_u32 s29, s29, 0x100000\n\ts_lshl_b32 s30, s25, 8\n\ts_add_u32 s30, s30, 0x10000\n\t" \
  "s_lshr_b32 s28, s25, 4\n\ts_lshl_b32 s28, s28, 12\n\ts_add_u32 s28, s28, 0x70000\n\t"
#define M_PAR1                                                                                        \
  "s_lshl_b32 s31, s24, 1\n\ts_add_u32 s31, s31, 0x10000\n\ts_lshl_b32 s39, s24, 7\n\ts_add_u32 s39, s39, 0x900000\n\t" \
  "s_lshl_b32 s32, s62, 5\n\ts_add_u32 s32, s32, s24\n\ts_add_u32 s24, s32, 0x700000\n\ts_add_u32 s25, s32, 0x708000\n\t" \
  "s_add_u32 s26, s32, 0x500000\n\ts_add_u32 s27, s32, 0x508000\n\ts_lshr_b32 s32, s32, 3\n\ts_add_u32 s32, s32, 0x80000\n\t" \
  "s_lshl_b32 s4, s18, 16\n\ts_lshl_b32 s5, s3, 13\n\ts_add_u32 s5, s5, s4\n\ts_lshr_b32 s33, s5, 3\n\ts_add_u32 s33, s33, 0x80000\n\t" \
  "s_lshl_b32 s38, s56, 15\n\ts_add_u32 s34, s5, s38\n\ts_add_u32 s34, s34, 0x500000\n\t" \
  "s_lshl_b32 s91, s37, 13\n\ts_add_u32 s91, s91, s4\n\ts_add_u32 s91, s91, s19\n\t"
#define M_PAR2                                                                                        \
  "s_lshl_b32 s35, s58, 1\n\ts_add_u32 s35, s35, 0x20000\n\t"                                         \
  "s_add_u32 s36, s58, 0x26200\n\ts_lshl_b32 s66, s56, 10\n\t" \
  "s_add_u32 s66, s66, s58\n\ts_add_u32 s66, s66, 0x24000\n\ts_add_u32 s89, s58, 0x26000\n\ts_lshl_b32 s90, s37, 11\n\t" \
  "s_mov_b32 s76, s64\n\ts_lshl_b32 s84, s58, 4\n\ts_lshl_b32 s85, s56, 14\n\ts_add_u32 s84, s84, s85\n\ts_mov_b32 s85, 0\n\t" \
  "s_and_b32 s4, s3, 1\n\ts_lshl_b32 s4, s4, 12\n\ts_add_u32 s69, s4, 0x26000\n\ts_lshl_b32 s65, s61, 13\n\ts_add_u32 s65, s65, s4\n\t" \
  "s_add_u32 s65, s65, 0x24000\n\ts_lshl_b32 s67, s4, 1\n\ts_add_u32 s67, s67, 0x20000\n\ts_mov_b32 s71, 0x3fb8aa3b\n\t" \
  "s_or_b32 s4, s2, s37\n\ts_cselect_b32 s68, 0xf0000000, 0\n\t"
static_assert(WS_OB == 0x500000 && WS_OA == 0x700000 && WS_FOB == 0x80000 && WS_KP == 0x100000 && WS_KR == 0x10000 && WS_FKP == 0x70000 &&
              WS_KV == 0x900000 && L_PQ == 0x20000 && L_VB == 0x26000 && L_Q == 0x24000 && L_RING == 0x10000 && L_WPHI == 0x24000 && L_GW == 0x24800 && L_GP == 0x25000 &&
              L_DUMMY == 0x24C00 && WS_FKV == 0x60000 && WS_ONES == 0x40000 && WS_EPOCH == 0 && L_X == 0 && L_WO == 0 && L_RED == 0 && FL == 128, "literals above");
#define V_OOB(R, S, CMP) CMP "v_mov_b32 v" #R ", " S "\n\ts_nop 0\n\tv_cndmask_b32 v" #R ", v30, v" #R ", vcc\n\t"
DI void program(unsigned tid, unsigned bid, unsigned g128, unsigned tb, unsigned otb, unsigned og128, unsigned rot ST_PAR) {
  asm volatile(
      "s_load_dwordx2 s[48:49], s[0:1], 0x0\n\ts_load_dwordx2 s[40:41], s[0:1], 0x8\n\ts_load_dwordx2 s[44:45], s[0:1], 0x18\n\t"
      "v_readfirstlane_b32 s37, v0\n\t"
      "s_load_dwordx2 s[72:73], s[0:1], 0x40\n\ts_load_dwordx2 s[20:21], s[0:1], 0x38\n\t"
      "s_getpc_b64 s[92:93]\n\t"
      "99:\n\t"
      "v_and_b32 v16, 63, v0\n\t"
      "s_lshr_b32 s37, s37, 6\n\t"
      "s_cbranch_scc1 98f\n\t"
      "s_lshr_b32 s4, s2, 3\n\tv_bfe_u32 v254, v0, 2, 3\n\tv_lshl_add_u32 v254, v254, 5, s4\n\tv_cmp_gt_u32 vcc, 32, v0\n\t"
      "v_add_u32 v254, 32, v254\n\ts_nop 0\n\tv_cndmask_b32 v254, v254, v0, vcc\n\tv_lshlrev_b32 v254, 7, v254\n\t"
      "v_min_u32 v254, 599f-99b-4, v254\n\tglobal_load_dword v254, v254, s[92:93]\n\t"
      "98:\n\t"
      "s_and_b32 s3, s37, 3\n\ts_lshr_b32 s56, s37, 2\n\ts_lshr_b32 s61, s3, 1\n\t"
      "s_mul_i32 s4, s56, -24\n\ts_add_u32 s4, s4, 0x28\n\ts_load_dwordx2 s[80:81], s[0:1], s4\n\t"
      "s_lshl_b32 s4, s61, 4\n\ts_add_u32 s4, s4, 0x20\n\ts_load_dwordx2 s[28:29], s[0:1], s4\n\t"
      "v_lshrrev_b32 v17, 3, v16\n\tv_lshlrev_b32 v159, 4, v16\n\tv_lshlrev_b32 v19, 2, v16\n\tv_and_b32 v18, 0x70, v159\n\t"
      "s_mul_i32 s4, s3, 49152\n\ts_add_u32 s4, s4, s16\n\ts_lshl_b32 s58, s3, 11\n\ts_add_u32 s57, s58, 0x10000\n\t"
      "v_and_b32 v19, 64, v19\n\tv_mul_u32_u24 v10, 3072, v17\n\tv_xor_b32 v19, v19, v18\n\tv_and_b32 v144, 0x30, v16\n\t"
      "v_add3_u32 v10, v10, v19, s4\n\tv_xor_b32 v144, v144, v18\n\t"
      "s_lshl_b32 s4, s17, 16\n\ts_lshl_b32 s5, s3, 14\n\ts_add_u32 s4, s4, s5\n\ts_lshl_b32 s60, s59, 7\n\t"
      "v_lshl_or_b32 v144, v17, 10, v144\n\tv_add_u32 v12, 0x400, v10\n\tv_add_u32 v13, 0x6000, v10\n\tv_add_u32 v144, s4, v144\n\t"
      "v_add_u32 v14, 0x800, v10\n\tv_add_u32 v15, 0x6400, v10\n\tv_xor_b32 v145, 64, v144\n\tv_add_u32 v11, 0x5c00, v10\n\t"
      "v_add_u32 v145, 0x1c00, v145\n\t"
      "s_mov_b32 s50, 0x200000\n\ts_mov_b32 s51, 0x20000\n\ts_mov_b32 s42, 0x180000\n\ts_mov_b32 s43, s51\n\t"
      "s_cmp_eq_u32 s56, 0\n\t"
      "s_waitcnt lgkmcnt(0)\n\t"
      "s_cbranch_scc1 90f\n\t"
      D_X(0) G_ALL(D_S0(0)) D_X(1) G_ALL(D_S0(1))
      "s_branch 91f\n\t"
      "90:\n\t"
      "s_mov_b32 s74, " STR(WS_LEN) "\n\ts_mov_b32 s75, s51\n\ts_mov_b32 s31, s51\n\t"
      "s_lshr_b32 s4, s2, 3\n\ts_lshl_b32 s4, s4, 3\n\tv_add_u32 v20, s4, v17\n\tv_lshlrev_b32 v20, 16, v20\n\t"
      "s_cmp_eq_u32 s3, 3\n\ts_cbranch_scc1 92f\n\t"
      "s_lshl_b32 s4, s16, 4\n\ts_lshl_b32 s5, s3, 10\n\ts_add_u32 s4, s4, s5\n\ts_cmp_eq_u32 s61, 0\n\t"
      "s_cselect_b32 s4, s4, 0\n\ts_cselect_b32 s30, 16, 1\n\ts_add_u32 m0, s5, 0x24000\n\ts_lshl_b32 s30, s30, 10\n\t"
      "buffer_load_dwordx4 v159, s[28:31], s4 offen lds\n\t"
      "s_branch 91f\n\t"
      "92:\n\t"
      "s_mov_b32 m0, 0x24c00\n\ts_nop 0\n\tbuffer_load_dword v20, s[72:75], 0 offen offset:3968 sc1 lds\n\t"
      "91:\n\t"
      "s_mov_b32 s46, s42\n\ts_mov_b32 s47, s51\n\ts_mov_b32 s74, " STR(WS_LEN) "\n\ts_mov_b32 s75, s51\n\t"
      "s_mov_b32 s22, s50\n\ts_mov_b32 s23, s51\n\ts_mov_b32 s82, 0x80000\n\ts_mov_b32 s83, s51\n\t"
      "s_add_u32 s92, s92, 400f-99b\n\ts_addc_u32 s93, s93, 0\n\ts_mov_b32 s53, 0x40000\n\t"
      "v_and_b32 v20, 31, v16\n\tv_lshrrev_b32 v21, 5, v16\n\tv_bfe_u32 v22, v16, 1, 3\n\tv_bfrev_b32 v30, 15\n\t"
      "v_xor_b32 v22, v22, v21\n\tv_lshlrev_b32 v23, 7, v20\n\tv_lshl_or_b32 v4, v22, 4, v23\n\t"
      "v_xor_b32 v5, 32, v4\n\tv_xor_b32 v6, 64, v4\n\tv_xor_b32 v7, 96, v4\n\t"
      "v_lshlrev_b32 v24, 3, v16\n\tv_lshlrev_b32 v25, 1, v16\n\tv_lshlrev_b32 v26, 5, v16\n\tv_and_b32 v27, 0x58, v24\n\t"
      "v_and_or_b32 v27, v25, 32, v27\n\tv_and_b32 v26, 0x180, v26\n\tv_or_b32 v27, v27, v26\n\t"
      "s_and_b32 s6, s3, 1\n\ts_lshl_b32 s4, s6, 6\n\ts_lshl_b32 s5, s61, 13\n\ts_add_u32 s5, s5, 0x10000\n\t"
      "v_xor_b32 v8, s4, v27\n\tv_lshl_add_u32 v8, v21, 10, v8\n\tv_add_u32 v8, s5, v8\n\t"
      "s_lshl_b32 s4, s3, 15\n\tv_lshl_add_u32 v0, v21, 9, v27\n\tv_add_u32 v0, s4, v0\n\t"
      "s_lshl_b32 s4, s3, 14\n\ts_lshl_b32 s5, s56, 13\n\ts_add_u32 s4, s4, s5\n\t"
      "v_and_b32 v26, 7, v16\n\tv_xor_b32 v26, v26, v21\n\tv_lshlrev_b32 v26, 4, v26\n\tv_lshl_or_b32 v26, v20, 8, v26\n\tv_add_u32 v248, s4, v26\n\t"
      "s_lshl_b32 s4, s16, 12\n\ts_lshl_b32 s5, s3, 17\n\ts_add_u32 s7, s4, 0x900000\n\ts_add_u32 s4, s7, s5\n\t"
      "s_lshl_b32 s8, s56, 16\n\ts_add_u32 s4, s4, s8\n\ts_add_u32 s5, s5, s19\n\t"
      "v_and_b32 v28, 0xf0, v159\n\tv_add_u32 v249, s4, v159\n\tv_lshl_or_b32 v29, v21, 8, v28\n\tv_add_u32 v26, s4, v29\n\t"
      "v_lshl_add_u32 v148, v17, 10, v19\n\tv_add_u32 v148, s5, v148\n\t"
      "s_lshl_b32 s9, s16, 1\n\ts_add_u32 s4, s9, 0x3fc00\n\tv_and_b32 v25, 27, v16\n\tv_and_b32 v22, 16, v16\n\t"
      V_OOB(250, "s4", "v_cmp_eq_u32 vcc, 16, v25\n\t")
      "v_subrev_u32 v251, 0x200, v250\n\tv_cmp_eq_u32 vcc, 0, v22\n\tv_lshrrev_b32 v25, 2, v20\n\tv_mov_b32 v156, 0x3f80\n\t"
      "v_cndmask_b32 v250, v250, v26, vcc\n\tv_cndmask_b32 v251, v251, v26, vcc\n\t"
      "v_and_b32 v26, 1, v16\n\t"
      "v_lshlrev_b32 v26, 4, v26\n\tv_lshlrev_b32 v156, v26, v156\n\tv_xor_b32 v26, v25, v21\n\tv_and_b32 v25, 6, v25\n\t"
      "v_bfe_u32 v31, v16, 1, 1\n\tv_or_b32 v25, v25, v31\n\tv_not_b32 v26, v26\n\tv_and_b32 v26, 1, v26\n\tv_lshlrev_b32 v157, v25, v26\n\t"
      "s_lshl_b32 s4, s16, 5\n\ts_add_u32 s10, s4, 0x60000\n\ts_lshl_b32 s4, s3, 4\n\ts_add_u32 s4, s4, s10\n\tv_add_u32 v158, s4, v23\n\t"
      "v_lshlrev_b32 v154, 2, v16\n\t"
      "s_cmp_eq_u32 s56, 0\n\t"
      "s_cbranch_scc1 100f\n\t"
      "s_lshl_b32 s4, s6, 10\n\ts_add_u32 s4, s4, 0x24000\n\tv_and_b32 v146, 0x78, v24\n\tv_lshl_or_b32 v146, v21, 7, v146\n\tv_add_u32 v146, s4, v146\n\t"
      "v_mov_b32 v147, 0x80008000\n\tv_cndmask_b32 v147, v147, 0, vcc\n\tv_cndmask_b32 v152, 1.0, 0, vcc\n\t"
      "s_lshl_b32 s4, s16, 11\n\ts_lshl_b32 s5, s6, 17\n\ts_add_u32 s4, s4, s5\n\ts_lshl_b32 s5, s17, 8\n\ts_add_u32 s4, s4, s5\n\ts_add_u32 s4, s4, 0x100000\n\t"
      "v_lshrrev_b32 v25, 4, v16\n\tv_lshl_or_b32 v149, v25, 13, v28\n\tv_add_u32 v149, s4, v149\n\t"
      "s_lshl_b32 s4, s16, 6\n\ts_lshl_b32 s5, s6, 12\n\ts_add_u32 s4, s4, s5\n\ts_lshl_b32 s5, s17, 7\n\ts_add_u32 s4, s4, s5\n\ts_add_u32 s4, s4, 0x70000\n\t"
      V_OOB(150, "s4", "v_cmp_eq_u32 vcc, 0, v16\n\t")
      "v_add_u32 v154, 0x25000, v154\n\t"
      G_BAR(6) D_X(2) G_ALL(D_S0(2)) G_BAR(6) D_X(3) G_ALL(D_S0(3)) G_BAR(6) D_X(4) G_ALL(D_S0(4)) M_PAR0
      G_BAR(6) D_X(5) G_ALL(D_S0(5)) M_PAR1 G_BAR(6) D_X(6) G_ALL(D_S0(6)) M_PAR2 G_BAR(6) D_X(7) G_ALL(D_S0(7))
      G_BAR(6) G_ALL(D_S1(8)) G_ALL(D_S1(9)) G_BAR(8) G_ALL(D_S1(10))
      "s_branch 200f\n\t"
      "100:\n\t"
      G_EPOCH
      "s_lshl_b32 s4, s6, 18\n\ts_add_u32 s7, s7, s4\n\ts_lshl_b32 s4, s17, 12\n\ts_add_u32 s7, s7, s4\n\t"
      "s_lshl_b32 s4, s17, 7\n\ts_add_u32 s10, s10, s4\n\t"
      "s_lshl_b32 s4, s17, 11\n\ts_add_u32 s9, s9, s4\n\ts_add_u32 s9, s9, 0x40000\n\ts_cmp_eq_u32 s3, 2\n\ts_cselect_b32 s9, s9, 0xf0000000\n\t"
      "s_lshl_b32 s4, s16, 6\n\ts_and_b32 s5, s17, 3\n\ts_lshl_b32 s5, s5, 11\n\ts_add_u32 s4, s4, s5\n\ts_lshr_b32 s5, s17, 2\n\ts_lshl_b32 s5, s5, 8\n\t"
      "s_add_u32 s4, s4, s5\n\ts_add_u32 s4, s4, 0x10000\n\ts_cmp_eq_u32 s37, 0\n\ts_cselect_b32 s4, s4, 0xf0000000\n\ts_cselect_b32 s5, s10, 0xf0000000\n\t"
      "v_add_u32 v151, s4, v154\n\tv_add_u32 v155, s5, v159\n\tv_cmp_gt_u32 vcc, 4, v16\n\t"
      "s_lshl_b32 s4, s3, 5\n\tv_xor_b32 v152, s4, v4\n\ts_add_u32 s4, s4, s60\n\ts_add_u32 s4, s4, 0x24800\n\t"
      "v_cndmask_b32 v155, v30, v155, vcc\n\tv_lshl_add_u32 v153, v21, 4, s4\n\t"
      "s_lshl_b32 s4, s3, 9\n\ts_add_u32 s4, s4, 0x25000\n\tv_add_u32 v154, s4, v154\n\t"
      "s_cmp_lg_u32 s61, 0\n\t"
      "s_cbranch_scc1 104f\n\t"
      "v_and_b32 v149, 0x330, v159\n\tv_lshlrev_b32 v25, 1, v159\n\tv_lshrrev_b32 v26, 1, v159\n\tv_and_b32 v25, 0x80, v25\n\t"
      "v_and_or_b32 v149, v26, 64, v149\n\tv_add3_u32 v149, v149, v25, s7\n\t"
      "s_lshl_b32 s4, s6, 5\n\ts_add_u32 s4, s4, s10\n\ts_add_u32 s4, s4, 8\n\tv_cmp_eq_u32 vcc, 0, v20\n\t"
      "v_lshl_add_u32 v150, v21, 4, s4\n\ts_nop 0\n\tv_cndmask_b32 v150, v30, v150, vcc\n\t"
      "s_branch 105f\n\t"
      "104:\n\t"
      "s_add_u32 s7, s7, 0x400\n\tv_lshl_add_u32 v149, v22, 13, v29\n\tv_add_u32 v149, s7, v149\n\t"
      "v_cmp_gt_u32 vcc, 8, v16\n\tv_add_u32 v150, s9, v159\n\ts_nop 0\n\tv_cndmask_b32 v150, v30, v150, vcc\n\t"
      "105:\n\t"
      G_Z("16,18,20,22,24,26,28,30,32,34,36,38,40,42,44,46")
      "s_mov_b32 s52, 0\n\t"
      G_LBAR G_READ(48, 64, 80, 0)
      "s_branch 102f\n\t"
      "101:\n\t"
      G_BAR(0) G_EVEN(0, "", "", "", "")
      "102:\n\t"
      G_LBAR G_ODD(1, "", "", "", "") G_LBAR G_EVEN(2, "", "", "", "") G_LBAR G_ODD(3, "", "", "", "")
      ".irp r,4,5,6,7\n\tv_xor_b32 v\\r, 0x8000, v\\r\n\t.endr\n\t"
      "s_xor_b32 s52, s52, 1\n\t"
      "s_cbranch_scc1 101b\n\t"
      ST(1)
      G_LBAR
      G_MMA2(96, 112, 128, 0) G_MMA2(96, 112, 128, 1) G_MMA2(96, 112, 128, 2) G_MMA2(96, 112, 128, 3)
      G_EPOCH_USE G_Z("88,90,92,94")
      "s_cmp_lg_u32 s61, 0\n\t"
      "s_cbranch_scc1 110f\n\t"
      "s_nop 0\n\t"
      ".irp r,16,17,18,19,20,21,22,23,24,25,26,27,28,29,30,31\n\tv_mul_f32 v\\r, 0x3eb8aa3b, v\\r\n\t.endr\n\t"
      G_LBAR
      ".irp r,32,33,34,35,36,37,38,39,40,41,42,43,44,45,46,47\n\tv_mul_f32 v\\r, 0x3eb8aa3b, v\\r\n\t.endr\n\t"
      A_PACK
      G_ST4(48, 149, "0 offen") G_ST4(52, 149, "0 offen offset:2048")
      ".irp j,0,1,2,3\n\t"
      "v_dot2c_f32_bf16 v92, v[48+\\j], v[48+\\j]\n\tv_dot2c_f32_bf16 v93, v[52+\\j], v[52+\\j]\n\t"
      "v_dot2c_f32_bf16 v94, v[56+\\j], v[56+\\j]\n\tv_dot2c_f32_bf16 v95, v[60+\\j], v[60+\\j]\n\t"
      ".endr\n\t"
      "s_mov_b32 s54, 131072\n\t"
      G_LBAR
      G_ST4(56, 149, "s54 offen") G_ST4(60, 149, "s54 offen offset:2048")
      "v_permlane32_swap_b32 v92, v94\n\tv_permlane32_swap_b32 v93, v95\n\t"
      "v_add_f32 v96, v92, v94\n\tv_add_f32 v97, v93, v95\n\t"
      A_GREAD(0)
      "v_max_f32 v96, v96, v97\n\t"
      A_DPPMAX("quad_perm:[1,0,3,2]") A_DPPMAX("quad_perm:[2,3,0,1]") A_DPPMAX("row_half_mirror") A_DPPMAX("row_mirror")
      "v_mov_b32 v97, v96\n\tv_mov_b32 v100, -1\n\ts_nop 0\n\tv_permlane16_swap_b32 v96, v97\n\tv_max_f32 v96, v96, v97\n\t"
      "buffer_store_dword v96, v150, s[72:75], 0 offen sc1\n\t"
      "buffer_store_dword v100, v151, s[72:75], 0 offen sc1\n\t"
      "s_branch 111f\n\t"
      "110:\n\t"
      ".irp j,0,1,2,3,4,5,6,7\n\tv_bfe_i32 v[96+\\j], v157, \\j, 1\n\t.endr\n\t"
      A_PACK
      ".irp j,0,1,2,3,4,5,6,7\n\tv_and_b32 v[96+\\j], v156, v[96+\\j]\n\t.endr\n\t"
      G_LBAR
      "s_setprio 2\n\t"
      "v_mfma_f32_32x32x16_bf16 v[16:31], v[48:51], v[96:99], 0\n\t"
      "v_mfma_f32_32x32x16_bf16 v[32:47], v[52:55], v[96:99], 0\n\t"
      "v_mfma_f32_32x32x16_bf16 v[16:31], v[56:59], v[100:103], v[16:31]\n\t"
      "v_mfma_f32_32x32x16_bf16 v[32:47], v[60:63], v[100:103], v[32:47]\n\t"
      "s_setprio 0\n\t"
      ".irp r,104,105,106,107\n\tv_mov_b32 v\\r, 0x3f803f80\n\t.endr\n\t"
      G_ST4(104, 150, "0 offen")
      "s_nop 4\n\t"
      F_PACK(16, 0, 48)
      G_ST4(48, 149, "0 offen") G_ST4(52, 149, "0 offen offset:512")
      G_LBAR
      F_PACK(32, 0, 56)
      G_ST4(56, 149, "0 offen offset:2048") G_ST4(60, 149, "0 offen offset:2560")
      A_GREAD(0)
      "111:\n\t"
      G_LBAR A_GDOT A_GREAD(2) M_PAR0
      G_LBAR A_GDOT A_GREAD(4) M_PAR1
      G_LBAR A_GDOT A_GREAD(6) M_PAR2
      G_LBAR A_GDOT
      "v_mov_b32 v94, s86\n\tv_mov_b32 v95, s87\n\tv_and_b32 v92, 0xfffff0ff, v154\n\t"
      "v_add_f32 v88, v88, v90\n\tv_add_f32 v89, v89, v91\n\t"
      "ds_write2st64_b32 v154, v88, v89 offset1:1\n\t"
      G_BAR(0)
      "ds_read2st64_b32 v[64:65], v92 offset1:1\n\tds_read2st64_b32 v[66:67], v92 offset0:2 offset1:3\n\t"
      "ds_read2st64_b32 v[68:69], v92 offset0:4 offset1:5\n\tds_read2st64_b32 v[70:71], v92 offset0:6 offset1:7\n\t"
      "buffer_store_dwordx2 v[94:95], v155, s[72:75], 0 offen sc1\n\t"
      "s_waitcnt lgkmcnt(0)\n\t"
      "v_add_f32 v64, v64, v66\n\tv_add_f32 v65, v65, v67\n\tv_add_f32 v68, v68, v70\n\tv_add_f32 v69, v69, v71\n\t"
      "v_add_f32 v64, v64, v68\n\tv_add_f32 v65, v65, v69\n\t"
      "s_nop 1\n\tv_permlane32_swap_b32 v64, v65\n\t"
      "v_add_f32 v64, v64, v65\n\t" SIGMOID(64, 253)
      "s_nop 0\n\tv_mov_b32 v252, v253\n\t"
      ST(2)
      G_LBAR "s_setprio 1\n\t" "v_permlane32_swap_b32 v253, v252\n\t" G_READ(48, 64, 80, 0) G_ALL(D_S2(19))
      G_LBAR G_APPLY(G_FIRST, D_S2(20))
      G_LBAR G_APPLY(G_EVEN, 2, D_S2(21)) G_BAR(8) G_APPLY(G_ODD, 3, D_S2(22))
      G_BAR(8) G_APPLY(G_EVEN, 4, D_S2(23))
      G_BAR(8) G_ODD(5, "buffer_load_dword v9, v158, s[72:75], 0 offen offset:8 sc1\n\t", G_KV1(200, 216, 62), G_KV1(204, 224, 63), G_KV1(208, 232, 64))
      G_BAR(14) G_EVEN(6, G_Z("160,162,164,166,168"), G_Z("170,172,174,176,178"), G_Z("180,182,184,186"), G_Z("188,190,244,246"))
      G_BAR(9) G_ODD(7, DPPMAX(9, "quad_perm:[1,0,3,2]"), DPPMAX(9, "quad_perm:[2,3,0,1]"), DPPMAX(9, "row_half_mirror"), "s_nop 0\n\t" DPPMAX(9, "row_mirror"))
      "s_waitcnt lgkmcnt(0)\n\t"
      G_MMA2(96, 112, 128, 0) G_MMA2(96, 112, 128, 1) G_MMA2(96, 112, 128, 2) G_MMA2(96, 112, 128, 3)
      "v_add_u32 v1, s65, v159\n\t"
      "s_setprio 2\n\t"
      G_QPRE G_Z("152,154,156,158") "v_mov_b32 v8, v9\n\t"
      A_PACK
      ".irp i,0,1,2,3\n\tds_write_b128 v1, v[48+4*\\i:51+4*\\i] offset:1024*\\i\n\t.endr\n\t"
      G_C0 G_C1
      ST(3)
      G_LBAR
      G_QRD
      "s_branch 300f\n\t"
      ATTN_ROUTINE
      "200:\n\t"
      ST(1)
      G_BAR(8) "s_setprio 1\n\t" G_EPOCH G_READ(48, 64, 80, 0) G_ALL(D_S1(11))
      G_BAR(9) G_APPLY(G_FIRST, D_S1(12))
      G_BAR(9) G_APPLY(G_EVEN, 2, D_S1(13)) G_BAR(8) G_APPLY(G_ODD, 3, D_S1(14))
      G_BAR(8) G_APPLY(G_EVEN, 4, D_S1(15)) G_BAR(8) G_APPLY(G_ODD, 5, D_S2(16))
      G_BAR(8) G_APPLY(G_EVEN, 6, D_S2(17)) G_BAR(8) G_APPLY(G_ODD, 7, D_S2(18))
      ST(2)
      G_BAR(8)
      "s_setprio 0\n\t"
      "ds_read_b64_tr_b16 v[64:65], v146\n\tds_read_b64_tr_b16 v[66:67], v146 offset:256\n\t"
      "ds_read_b64_tr_b16 v[68:69], v146 offset:512\n\tds_read_b64_tr_b16 v[70:71], v146 offset:768\n\t"
      "ds_read2st64_b32 v[72:73], v154 offset1:1\n\tds_read2st64_b32 v[74:75], v154 offset0:2 offset1:3\n\t"
      "ds_read2st64_b32 v[76:77], v154 offset0:4 offset1:5\n\tds_read2st64_b32 v[78:79], v154 offset0:6 offset1:7\n\t"
      G_MMA2(96, 112, 128, 0) G_EPOCH_USE G_MMA2(96, 112, 128, 1)
      "s_waitcnt lgkmcnt(0)\n\t"
      "v_add_f32 v72, v72, v74\n\tv_add_f32 v73, v73, v75\n\tv_add_f32 v76, v76, v78\n\tv_add_f32 v77, v77, v79\n\t"
      ".irp r,64,65,66,67\n\tv_xor_b32 v\\r, v147, v\\r\n\t.endr\n\t"
      G_MMA2(96, 112, 128, 2)
      "v_add_f32 v72, v72, v76\n\tv_add_f32 v73, v73, v77\n\t"
      ".irp r,68,69,70,71\n\tv_xor_b32 v\\r, v147, v\\r\n\t.endr\n\t"
      "v_permlane32_swap_b32 v72, v73\n\t"
      G_MMA2(96, 112, 128, 3)
      "v_add_f32 v72, v72, v73\n\t" SIGMOID(72, 252)
      "s_nop 0\n\tv_mov_b32 v253, v252\n\ts_nop 3\n\t"
      A_PACK
      "v_permlane32_swap_b32 v252, v253\n\t"
      G_BAR(4)
      "s_setprio 2\n\t"
      "v_mfma_f32_32x32x16_bf16 v[16:31], v[64:67], v[48:51], 0\n\t"
      "v_mfma_f32_32x32x16_bf16 v[32:47], v[64:67], v[52:55], 0\n\t"
      ".irp j,0,1,2,3\n\tv_bfe_i32 v[128+\\j], v157, \\j, 1\n\t.endr\n\t"
      "v_mfma_f32_32x32x16_bf16 v[96:111], v[68:71], v[56:59], 0\n\t"
      ".irp j,4,5,6,7\n\tv_bfe_i32 v[128+\\j], v157, \\j, 1\n\t.endr\n\t"
      "v_mfma_f32_32x32x16_bf16 v[112:127], v[68:71], v[60:63], 0\n\t"
      "s_setprio 0\n\t"
      ".irp j,0,1,2,3,4,5,6,7\n\tv_and_b32 v[128+\\j], v156, v[128+\\j]\n\t.endr\n\t"
      F_JOBS(F_MAX1) "s_nop 0\n\t" F_JOBS(F_SWAP) F_JOBS(F_MAX2) F_JOBS(F_NEG) F_JOBS(F_DUP) F_JOBS(F_SWAP)
      G_BAR(0)
      F_EXP(16, 80) F_FMA(32, 84) F_EX(32, "0,8,1,9") G_Z("160,162,164,166,168,170")
      G_LBAR
      G_POLL(140) F_EX(32, "2,10,3,11,4,12,5,13,6,14,7,15") F_EXP(96, 88) G_Z("172,174,176,178,180,182")
      G_LBAR
      F_EXP(112, 92) F_SUM1(16, 80, 48) F_SUM1(32, 84, 56) G_POLLWAIT(22, 140)
      G_LBAR
      F_SUM1(96, 88, 64) DPPMAX(9, "quad_perm:[1,0,3,2]") F_SUM1(112, 92, 72) DPPMAX(9, "quad_perm:[2,3,0,1]") F_JOBS(F_SUM2)
      DPPMAX(9, "row_half_mirror") F_JOBS(F_SUM3) DPPMAX(9, "row_mirror") F_JOBS(F_SWAP) F_JOBS(F_ADD) F_JOBS(F_RCP) F_JOBS(F_DUP) F_JOBS(F_SWAP) G_Z("184,186,188,190,244,246")
      G_LBAR
      G_KV1(200, 216, 62) F_SCALE(16, 80, 48) G_KV1(204, 224, 63) F_SCALE(32, 84, 56) G_KV1(208, 232, 64) F_SCALE(96, 88, 64)
      G_LBAR
      F_SCALE(112, 92, 72)
      "s_cmp_lg_u32 s61, 0\n\t"
      "s_cbranch_scc1 230f\n\t"
      "v_add_u32 v136, s67, v159\n\t"
      ".irp i,0,1,2,3,4,5,6,7\n\tds_write_b128 v136, v[48+4*\\i:51+4*\\i] offset:1024*\\i\n\t.endr\n\t"
      G_C0 G_C1
      "s_branch 231f\n\t"
      "230:\n\t"
      F_JOBS(F_TR0) F_JOBS(F_TR1)
      "v_add_u32 v136, s69, v159\n\tv_mov_b32 v2, s86\n\tv_mov_b32 v3, s87\n\tv_mov_b32 v137, v152\n\t"
      "231:\n\t"
      G_QPRE G_Z("152,154,156,158") "v_mov_b32 v8, v9\n\t"
      ST(3)
      G_LBAR
      G_QRD
      "s_cmp_eq_u32 s61, 0\n\t"
      "s_cbranch_scc1 151f\n\t"
      "s_setprio " STR(KV_PRIO) "\n\t"
      ".irp i,0,1,2,3\n\tds_read_b128 v[80+4*\\i:83+4*\\i], v136 offset:1024*\\i\n\t.endr\n\t"
      F_JOBS(F_PACK)
      ".irp r,96,97,98,99,100,101,102,103,104,105,106,107,108,109,110,111\n\tv_mov_b32 v\\r, v137\n\t.endr\n\ts_nop 0\n\t"
      "s_waitcnt lgkmcnt(2)\n\t"
      "v_mfma_f32_32x32x16_bf16 v[16:31], v[80:83], v[128:131], v[96:111]\n\t"
      "v_mfma_f32_32x32x16_bf16 v[32:47], v[84:87], v[128:131], v[96:111]\n\t"
      "s_waitcnt lgkmcnt(0)\n\t"
      "v_mfma_f32_32x32x16_bf16 v[112:127], v[88:91], v[128:131], v[96:111]\n\t"
      "v_mfma_f32_32x32x16_bf16 v[96:111], v[92:95], v[128:131], v[96:111]\n\t"
      "s_mov_b32 s52, 32768\n\ts_mov_b32 s54, 65536\n\ts_mov_b32 s55, 98304\n\t"
      "s_nop 5\n\t"
      F_PACK(16, 0, 80) F_PACK(32, 0, 88)
      "v_mfma_f32_32x32x16_bf16 v[16:31], v[48:51], v[80:83], 0\n\t"
      F_PACK(112, 0, 128)
      "v_mfma_f32_32x32x16_bf16 v[16:31], v[52:55], v[84:87], v[16:31]\n\t"
      F_PACK(96, 0, 136)
      "v_mfma_f32_32x32x16_bf16 v[16:31], v[56:59], v[88:91], v[16:31]\n\t"
      "v_mfma_f32_32x32x16_bf16 v[32:47], v[64:67], v[128:131], 0\n\t"
      "v_mfma_f32_32x32x16_bf16 v[16:31], v[60:63], v[92:95], v[16:31]\n\t"
      "v_mfma_f32_32x32x16_bf16 v[32:47], v[68:71], v[132:135], v[32:47]\n\t"
      "v_mfma_f32_32x32x16_bf16 v[32:47], v[72:75], v[136:139], v[32:47]\n\t"
      "v_mfma_f32_32x32x16_bf16 v[32:47], v[76:79], v[140:143], v[32:47]\n\t"
      "s_nop 8\n\t"
      F_PACK(16, 0, 80)
      G_ST4(80, 149, "0 offen") G_ST4(84, 149, "s52 offen")
      "s_nop 0\n\t"
      F_PACK(32, 0, 88)
      G_ST4(88, 149, "s54 offen") G_ST4(92, 149, "s55 offen")
      "s_setprio 1\n\t"
      "s_waitcnt vmcnt(0)\n\t"
      "buffer_store_dwordx2 v[2:3], v150, s[72:75], 0 offen sc1\n\t"
      G_C0 G_C1
      "s_branch 301f\n\t"
      "151:\n\t"
      "s_setprio 1\n\t"
      "300:\n\t"
      "s_waitcnt vmcnt(0) lgkmcnt(0)\n\t"
      "301:\n\t"
      "v_mov_b64 v[16:17], 0\n\t"
      "v_permlane16_swap_b32 v9, v8\n\t"
      QK(128, 143, 200, 203, 192, 195, 96, 111)
      ".irp j,0,1,2,3\n\tv_dot2c_f32_bf16 v16, v[192+\\j], v[192+\\j]\n\tv_dot2c_f32_bf16 v17, v[196+\\j], v[196+\\j]\n\t.endr\n\t"
      "v_max_f32 v9, v9, v8\n\t"
      "s_nop 1\n\t"
      "v_permlane32_swap_b32 v16, v17\n\t"
      "v_add_f32 v16, v16, v17\n\t"
      "v_mul_f32 v16, v16, v9\n\t"
      "v_cmp_nge_f32 vcc, 0x45100000, v16\n\t"
      "s_cbranch_vccnz 310f\n\t"
      "311:\n\t"
      ST(4)
      "s_cmp_eq_u32 s56, 0\n\t"
      "s_cbranch_scc1 320f\n\t"
      RUN("5")
      "s_branch 330f\n\t"
      "310:\n\t"
      "v_mov_b32 v96, 0xff7fffff\n\tv_mov_b32 v112, 0xff7fffff\n\t"
      "s_mov_b32 s52, s39\n\ts_mov_b32 s54, 63\n\t"
      "312:\n\t"
      "buffer_load_dwordx4 v[16:19], v4, s[72:75], s52 offen sc1\n\t"
      "s_add_u32 s52, s52, 0x800\n\t"
      "s_waitcnt vmcnt(0)\n\t"
      "v_mfma_f32_32x32x16_bf16 v[32:47], v[16:19], v[192:195], 0\n\t"
      "v_mfma_f32_32x32x16_bf16 v[48:63], v[16:19], v[196:199], 0\n\t"
      "s_nop 11\n\t"
      ".irp i,0,2,4,6,8,10,12,14\n\tv_max3_f32 v96, v96, v[32+\\i], v[33+\\i]\n\tv_max3_f32 v112, v112, v[48+\\i], v[49+\\i]\n\t.endr\n\t"
      "s_sub_u32 s54, s54, 1\n\t"
      "s_cbranch_scc0 312b\n\t"
      "v_mov_b32 v97, v96\n\tv_mov_b32 v113, v112\n\ts_nop 1\n\t"
      "v_permlane32_swap_b32 v96, v97\n\tv_permlane32_swap_b32 v112, v113\n\t"
      "v_max_f32 v96, v96, v97\n\tv_max_f32 v112, v112, v113\n\t"
      "v_xor_b32 v96, 0x80000000, v96\n\tv_xor_b32 v112, 0x80000000, v112\n\t"
      ".irp r,1,2,3,4,5,6,7,8,9,10,11,12,13,14,15\n\tv_mov_b32 v[96+\\r], v96\n\tv_mov_b32 v[112+\\r], v112\n\t.endr\n\t"
      "s_nop 1\n\t"
      QK(128, 143, 200, 203, 192, 195, 96, 111)
      "s_nop 11\n\t"
      "s_branch 311b\n\t"
      "320:\n\t"
      RUN("0")
      ST(5)
      "v_mov_b64 v[16:17], -1\n\tv_mov_b64 v[18:19], -1\n\t"
      G_ST4(16, 4, "s24 offen")
      RUN("0")
      "s_cmp_lg_u32 s37, 0\n\t"
      "s_cbranch_scc1 322f\n\t"
      "v_and_b32 v20, 0x1f0, v4\n\tv_lshlrev_b32 v20, 3, v20\n\ts_add_u32 s52, s29, 4096\n\t"
      "3210:\n\tbuffer_load_dwordx2 v[22:23], v20, s[72:75], s28 offen sc1\n\ts_waitcnt vmcnt(0)\n\t"
      "v_cmp_ne_u64 vcc, s[86:87], v[22:23]\n\t" G_SPIN(321, "s_sleep 4\n\t")
      P_LOAD8(32, 29, 52)
      "322:\n\t"
      RUN("0")
      ST(6)
      G_ST4(16, 4, "s25 offen")
      "s_cmp_lg_u32 s37, 0\n\t"
      "s_cbranch_scc1 323f\n\t"
      G_Z("24,26,28,30")
      "v_mov_b32 v20, 0x3f80\n\tv_mov_b32 v21, 0x3f800000\n\t"
      ".irp i,0,1,2,3,4,5,6,7\n\t.irp j,0,1,2,3\n\t"
      "v_dot2c_f32_bf16 v[24+2*\\j], v[32+4*\\i+\\j], v20\n\tv_dot2c_f32_bf16 v[25+2*\\j], v[32+4*\\i+\\j], v21\n\t"
      ".endr\n\t.endr\n\t"
      "v_and_b32 v20, 0xf0, v4\n\tv_lshrrev_b32 v21, 5, v4\n\tv_lshrrev_b32 v22, 7, v4\n\t"
      ".irp e,0,1,2,3\n\tv_permlane16_swap_b32 v[24+\\e], v[28+\\e]\n\t.endr\n\t"
      ".irp e,0,1,2,3\n\tv_add_f32 v[24+\\e], v[24+\\e], v[28+\\e]\n\t.endr\n\t"
      "v_and_or_b32 v20, v21, 8, v20\n\tv_and_or_b32 v20, v22, 4, v20\n\t"
      "v_permlane32_swap_b32 v24, v26\n\tv_permlane32_swap_b32 v25, v27\n\t"
      "v_add_f32 v24, v24, v26\n\tv_add_f32 v25, v25, v27\n\t"
      "v_cvt_pk_bf16_f32 v24, v24, v25\n\tv_min_u32 v24, -2, v24\n\t"
      "buffer_store_dword v24, v20, s[72:75], s30 offen sc1\n\t"
      "323:\n\t"
      RUN("0")
      ST(7)
      "v_add_u32 v20, s35, v4\n\t"
      P_LOAD(16, 0, 31) P_LOAD(20, 1, 31)
      ".irp i,0,1,2,3\n\tds_read_b128 v[32+4*\\i:35+4*\\i], v20 offset:1024*\\i\n\t.endr\n\t"
      RUN("0")
      ST(8)
      "3240:\n\t"
      "v_max3_u32 v80, v16, v17, v18\n\tv_max3_u32 v81, v19, v24, v25\n\tv_max3_u32 v80, v80, v26, v27\n\tv_max_u32 v80, v80, v81\n\t"
      "v_cmp_eq_u32 vcc, -1, v80\n\t"
      G_SPIN(324, "s_sleep 4\n\t" P_LOAD(16, 0, 31) P_LOAD(20, 1, 31) "s_waitcnt vmcnt(0)\n\t")
      "s_waitcnt lgkmcnt(0)\n\t"
      "v_mfma_f32_32x32x16_bf16 v[48:63], v[16:19], v[32:35], 0\n\t"
      "v_mfma_f32_32x32x16_bf16 v[64:79], v[16:19], v[40:43], 0\n\t"
      "v_mfma_f32_32x32x16_bf16 v[48:63], v[24:27], v[36:39], v[48:63]\n\t"
      "v_mfma_f32_32x32x16_bf16 v[64:79], v[24:27], v[44:47], v[64:79]\n\t"
      "v_sub_f32 v80, 1.0, v253\n\tv_sub_f32 v81, 1.0, v252\n\t"
      "v_mov_b32 v82, s86\n\tv_mov_b32 v83, s87\n\t"
      "s_nop 6\n\t"
      "v_add_f32 v56, 0x358637bd, v56\n\tv_rcp_f32 v56, v56\n\t"
      "v_add_f32 v72, 0x358637bd, v72\n\tv_rcp_f32 v72, v72\n\t"
      "v_mul_f32 v80, v80, v56\n\t"
      ".irp r,48,49,50,51,52,53,54,55\n\tv_mul_f32 v\\r, v80, v\\r\n\t.endr\n\t"
      "v_mul_f32 v81, v81, v72\n\t"
      ".irp i,0,1,2,3\n\tv_cvt_pk_bf16_f32 v[16+\\i], v[48+2*\\i], v[49+2*\\i]\n\t.endr\n\t"
      G_ST4(16, 4, "s26 offen")
      ".irp r,64,65,66,67,68,69,70,71\n\tv_mul_f32 v\\r, v81, v\\r\n\t.endr\n\t"
      ".irp i,0,1,2,3\n\tv_cvt_pk_bf16_f32 v[20+\\i], v[64+2*\\i], v[65+2*\\i]\n\t.endr\n\t"
      G_ST4(20, 4, "s27 offen")
      RUNV("0", SW_A)
      "v_cmp_eq_u32 vcc, 0, v4\n\tv_mov_b32 v84, s32\n\tv_bfrev_b32 v80, 15\n\t"
      SW_B
      "v_cndmask_b32 v84, v80, v84, vcc\n\t"
      "buffer_store_dwordx2 v[82:83], v84, s[72:75], 0 offen sc1\n\t"
      "330:\n\t"
      ST(9)
      "v_and_b32 v20, 0x70, v4\n\tv_lshlrev_b32 v20, 3, v20\n\t"
      "buffer_load_dwordx2 v[22:23], v20, s[72:75], s33 offen sc1\n\t"
      SW_C
      ST(10)
      "v_mov_b32 v84, s36\n\ts_add_u32 s52, s34, 4096\n\t"
      "3320:\n\tv_cmp_ne_u64 vcc, s[86:87], v[22:23]\n\t"
      G_SPIN(332, "s_sleep 4\n\tbuffer_load_dwordx2 v[22:23], v20, s[72:75], s33 offen sc1\n\ts_waitcnt vmcnt(0)\n\t")
      P_LOAD8(48, 34, 52)
      "v_add_u32 v6, s35, v4\n\tv_lshrrev_b32 v7, 2, v4\n\tv_add_u32 v7, s89, v7\n\tv_xor_b32 v1, 64, v0\n\t"
      "v_mov_b32 v82, s86\n\tv_mov_b32 v83, s87\n\t"
      "s_add_u32 s54, s34, 0x200000\n\ts_add_u32 s55, s34, 0x201000\n\t"
      RUN("0")
      ST(12)
      PV(176, 191, 244, 247, 152, 159)
      "s_cmp_lg_u32 s56, 0\n\t"
      "s_cbranch_scc1 340f\n\t"
      "ds_write_b128 v6, v[160:163]\n\tds_write_b128 v6, v[164:167] offset:1024\n\tds_write_b32 v7, v168\n\t"
      "s_nop 6\n\t"
      "ds_write_b128 v6, v[176:179] offset:2048\n\tds_write_b128 v6, v[180:183] offset:3072\n\tds_write_b32 v7, v184 offset:256\n\t"
      "s_waitcnt lgkmcnt(0)\n\t"
      "ds_write_b64 v84, v[82:83] offset:8\n\t"
      G_BAR(0)
      "s_branch 345f\n\t"
      "340:\n\t"
      "3410:\n\t"
      "ds_read_b64 v[40:41], v84 offset:8\n\t"
      "ds_read_b32 v32, v7 offset:256\n\tds_read_b128 v[16:19], v6 offset:2048\n\tds_read_b128 v[20:23], v6 offset:3072\n\t"
      "ds_read_b32 v33, v7\n\tds_read_b128 v[24:27], v6\n\tds_read_b128 v[28:31], v6 offset:1024\n\t"
      "s_waitcnt lgkmcnt(6)\n\t"
      "v_cmp_ne_u64 vcc, s[86:87], v[40:41]\n\t"
      G_SPIN(341, "s_waitcnt lgkmcnt(0)\n\ts_sleep 1\n\t")
      "s_waitcnt lgkmcnt(3)\n\t"
      "v_add_f32 v32, v168, v32\n\tv_rcp_f32 v32, v32\n\t"
      ".irp i,0,1,2,3,4,5,6,7\n\tv_add_f32 v[16+\\i], v[160+\\i], v[16+\\i]\n\t.endr\n\t"
      "v_mul_f32 v32, v252, v32\n\t"
      "s_waitcnt lgkmcnt(0)\n\t"
      "v_add_f32 v33, v184, v33\n\tv_rcp_f32 v33, v33\n\t"
      ".irp i,0,1,2,3,4,5,6,7\n\tv_mul_f32 v[16+\\i], v32, v[16+\\i]\n\t.endr\n\t"
      ".irp i,0,1,2,3\n\tv_cvt_pk_bf16_f32 v[84+\\i], v[16+2*\\i], v[17+2*\\i]\n\t.endr\n\t"
      ".irp i,0,1,2,3\n\tv_min_u32 v[84+\\i], -2, v[84+\\i]\n\t.endr\n\t"
      G_ST4(84, 4, "s24 offen")
      "v_mul_f32 v33, v253, v33\n\t"
      ".irp i,0,1,2,3,4,5,6,7\n\tv_add_f32 v[24+\\i], v[176+\\i], v[24+\\i]\n\t.endr\n\t"
      ".irp i,0,1,2,3,4,5,6,7\n\tv_mul_f32 v[24+\\i], v33, v[24+\\i]\n\t.endr\n\t"
      ".irp i,0,1,2,3\n\tv_cvt_pk_bf16_f32 v[88+\\i], v[24+2*\\i], v[25+2*\\i]\n\t.endr\n\t"
      ".irp i,0,1,2,3\n\tv_min_u32 v[88+\\i], -2, v[88+\\i]\n\t.endr\n\t"
      G_ST4(88, 4, "s25 offen")
      G_BAR(2)
      "345:\n\t"
      ST(13)
      P_RB(0) P_RB(1)
      P_USEB(0, 4, "0", "0", P_RB(2) P_RA(0) P_AT(0)) P_USEB(1, 8, G_A0, G_A1, P_RB(3) P_RA(1) P_AT(1))
      P_USEB(2, 12, G_A0, G_A1, P_RB(4) P_RA(2) P_AT(2)) P_USEB(3, 12, G_A0, G_A1, P_RB(5) P_RA(3) P_AT(3))
      P_USEB(4, 12, G_A0, G_A1, P_RB(6) P_RA(4) P_AT(4)) P_USEB(5, 12, G_A0, G_A1, P_RB(7) P_RA(5) P_AT(5))
      P_USEB(6, 12, G_A0, G_A1, P_RA(6) P_AT(6)) P_USEB(7, 8, G_A0, G_A1, P_RA(7) P_AT(7))
      "v_xor_b32 v6, 32, v248\n\tv_xor_b32 v7, 64, v248\n\tv_xor_b32 v8, 96, v248\n\t"
      "v_lshrrev_b32 v9, 7, v4\n\tv_bfe_u32 v10, v4, 3, 4\n\tv_xor_b32 v10, v10, v9\n\tv_lshlrev_b32 v10, 4, v10\n\t"
      "v_lshl_add_u32 v10, v9, 8, v10\n\tv_add_u32 v10, s90, v10\n\tv_xor_b32 v11, 16, v10\n\t"
      "v_and_b32 v12, 0x70, v4\n\tv_lshl_or_b32 v12, v9, 10, v12\n\tv_add_u32 v12, s91, v12\n\t"
      G_LBAR
      P_USEA(0, 7) P_USEA(1, 6) P_USEA(2, 5) P_USEA(3, 4) P_USEA(4, 3) P_USEA(5, 2) P_USEA(6, 1) P_USEA(7, 0)
      "s_nop 10\n\t"
      "ds_write_b128 v248, v[16:19]\n\tds_write_b128 v6, v[20:23]\n\tds_write_b128 v7, v[24:27]\n\tds_write_b128 v8, v[28:31]\n\t"
      "ds_write_b128 v248, v[32:35] offset:128\n\tds_write_b128 v6, v[36:39] offset:128\n\t"
      "ds_write_b128 v7, v[40:43] offset:128\n\tds_write_b128 v8, v[44:47] offset:128\n\t"
      ST(15)
      G_LBAR
      ".irp k,0,1,2,3\n\tds_read_b128 v[16+8*\\k:19+8*\\k], v10 offset:16384*\\k\n\tds_read_b128 v[20+8*\\k:23+8*\\k], v11 offset:16384*\\k\n\t.endr\n\t"
      "s_waitcnt lgkmcnt(4)\n\t"
      ".irp i,0,1,2,3,4,5,6,7\n\tv_add_f32 v[16+\\i], v[16+\\i], v[24+\\i]\n\t.endr\n\t"
      "s_waitcnt lgkmcnt(0)\n\t"
      ".irp i,0,1,2,3,4,5,6,7\n\tv_add_f32 v[32+\\i], v[32+\\i], v[40+\\i]\n\t.endr\n\t"
      ".irp i,0,1,2,3,4,5,6,7\n\tv_add_f32 v[16+\\i], v[16+\\i], v[32+\\i]\n\t.endr\n\t"
      ".irp i,0,1,2,3\n\tv_cvt_pk_bf16_f32 v[16+\\i], v[16+2*\\i], v[17+2*\\i]\n\t.endr\n\t"
      "buffer_store_dwordx4 v[16:19], v12, s[20:23], 0 offen" OUT_POL "\n\t"
      "v_cmp_eq_u32 vcc, 0, v4\n\tv_mov_b32 v20, s68\n\tv_bfrev_b32 v21, 15\n\tv_mov_b32 v22, s86\n\tv_mov_b32 v23, s87\n\t"
      "v_cndmask_b32 v20, v21, v20, vcc\n\t"
      "buffer_store_dwordx2 v[22:23], v20, s[72:75], 0 offen sc1\n\t"
      ST_END
      "s_branch 599f\n\t"
      P_STUBS(57, 56, 580) P_SLOW(580, 96, 54, 55)
      "599:"
      : "+{v0}"(tid) ST_OP
      : "{s[0:1]}"(__builtin_amdgcn_kernarg_segment_ptr()), "{s2}"(bid), "{s16}"(g128), "{s17}"(tb), "{s18}"(otb), "{s19}"(og128), "{s59}"(rot)
      : "memory", "scc", "vcc",
#define S4(a, b, c, d) "s" #a, "s" #b, "s" #c, "s" #d
        "s3", S4(4, 5, 6, 7), S4(8, 9, 10, 20), S4(21, 22, 23, 24), S4(25, 26, 27, 28), S4(29, 30, 31, 32), S4(33, 34, 35, 36), S4(37, 38, 39, 40),
        S4(41, 42, 43, 44), S4(45, 46, 47, 48), S4(49, 50, 51, 52), S4(53, 54, 55, 56), S4(57, 58, 60, 61), S4(62, 63, 64, 65), S4(66, 67, 68, 69),
        S4(70, 71, 72, 73), S4(74, 75, 76, 77), S4(78, 79, 80, 81), S4(82, 83, 84, 85), S4(86, 87, 88, 89), S4(90, 91, 92, 93),
#define R8(a, b, c, d, e, f, g, h) "v" #a, "v" #b, "v" #c, "v" #d, "v" #e, "v" #f, "v" #g, "v" #h
        R8(1, 2, 3, 4, 5, 6, 7, 8), R8(9, 10, 11, 12, 13, 14, 15, 254), R8(144, 145, 146, 147, 148, 149, 150, 151),
        R8(152, 153, 154, 155, 156, 157, 158, 159), R8(248, 249, 250, 251, 252, 253, 252, 253),
        R8(16, 17, 18, 19, 20, 21, 22, 23), R8(24, 25, 26, 27, 28, 29, 30, 31), R8(32, 33, 34, 35, 36, 37, 38, 39),
        R8(40, 41, 42, 43, 44, 45, 46, 47), R8(48, 49, 50, 51, 52, 53, 54, 55), R8(56, 57, 58, 59, 60, 61, 62, 63),
        R8(64, 65, 66, 67, 68, 69, 70, 71), R8(72, 73, 74, 75, 76, 77, 78, 79), R8(80, 81, 82, 83, 84, 85, 86, 87),
        R8(88, 89, 90, 91, 92, 93, 94, 95), R8(96, 97, 98, 99, 100, 101, 102, 103), R8(104, 105, 106, 107, 108, 109, 110, 111),
        R8(112, 113, 114, 115, 116, 117, 118, 119), R8(120, 121, 122, 123, 124, 125, 126, 127), R8(128, 129, 130, 131, 132, 133, 134, 135),
        R8(136, 137, 138, 139, 140, 141, 142, 143), R8(160, 161, 162, 163, 164, 165, 166, 167), R8(168, 169, 170, 171, 172, 173, 174, 175),
        R8(176, 177, 178, 179, 180, 181, 182, 183), R8(184, 185, 186, 187, 188, 189, 190, 191), R8(192, 193, 194, 195, 196, 197, 198, 199),
        R8(200, 201, 202, 203, 204, 205, 206, 207), R8(208, 209, 210, 211, 212, 213, 214, 215), R8(216, 217, 218, 219, 220, 221, 222, 223),
        R8(224, 225, 226, 227, 228, 229, 230, 231), R8(232, 233, 234, 235, 236, 237, 238, 239), R8(240, 241, 242, 243, 244, 245, 246, 247) ST_CL);
}
#define HG 2

__global__ __launch_bounds__(512, 2) void gated_mix_kernel(const char* __restrict__ x, const char* __restrict__ wqa,
                                                           const char* __restrict__ woa, const char* __restrict__ wqb,
                                                           const char* __restrict__ wphi, const char* __restrict__ wob,
                                                           const char* __restrict__ gatew, char* __restrict__ out,
                                                           char* __restrict__ ws) {
  __shared__ __attribute__((aligned(16))) char lds_raw[L_BYTES];
  asm volatile("" :: "s"((unsigned)(uintptr_t)(LAS char*)lds_raw));
  STAMP(0); STAMP_RT(62);
  const unsigned xc = blockIdx.x & 7, in = blockIdx.x >> 3;
  const unsigned g = HG * (xc % (8 / HG)) + in % HG, tb = (32 / HG) * (xc / (8 / HG)) + in / HG;
  program(threadIdx.x, blockIdx.x, g * 128, tb, 4 * xc + (in & 3), (in >> 2) * 128, (in / HG + (8 / HG) * (in % HG)) & 7 ST_ARG);
}

}

extern "C" void kernel_launch(void* const* d_in, const int* in_sizes, int n_in, void* d_out, int out_size, void* d_ws,
                              size_t ws_size, hipStream_t stream) {
  gated_mix_kernel<<<256, 512, 0, stream>>>((const char*)d_in[0], (const char*)d_in[1], (const char*)d_in[2], (const char*)d_in[4],
                                            (const char*)d_in[5], (const char*)d_in[6], (const char*)d_in[7], (char*)d_out, (char*)d_ws);
}
```

```cpp
#include <hip/hip_runtime.h>
#include <hip/hip_bf16.h>
#include <stdint.h>

namespace {

#define LAS __attribute__((address_space(3)))
#define DI __device__ __forceinline__

constexpr unsigned WS_EPOCH = 0;
constexpr unsigned FL = 128;
constexpr unsigned WS_KR = 0x10000;
constexpr unsigned WS_ONES = 0x40000;
constexpr unsigned WS_FKV = 0x60000;
constexpr unsigned WS_FKP = 0x70000;
constexpr unsigned WS_FOB = 0x80000;
constexpr unsigned WS_KP = 0x100000;
constexpr unsigned WS_OB = 0x500000;
constexpr unsigned WS_OA = 0x700000;
constexpr unsigned WS_KV = 0x900000;
constexpr unsigned WS_END = 0xD00000;
#define WS_LEN 0xD00000

constexpr unsigned L_X = 0;
constexpr unsigned L_RING = 65536;
constexpr unsigned L_WO = 0;
constexpr unsigned L_RED = 0;
constexpr unsigned L_PQ = 131072;
constexpr unsigned L_Q = 147456;
constexpr unsigned L_WPHI = L_Q;
constexpr unsigned L_GW = L_Q + 2048;
constexpr unsigned L_DUMMY = L_Q + 3072;
constexpr unsigned L_GP = L_Q + 4096;
constexpr unsigned L_VB = 155648;
constexpr unsigned L_BYTES = 163840;

#define EX(r) "v_exp_f32 v" #r ", v" #r "\n\t"
#define QK(s0, s1, k0, k1, q0, q1, c0, c1) "v_mfma_f32_32x32x16_bf16 v[" #s0 ":" #s1 "], v[" #k0 ":" #k1 "], v[" #q0 ":" #q1 "], v[" #c0 ":" #c1 "]\n\t"
#define PV(o0, o1, a0, a1, p0, p1) "v_smfmac_f32_32x32x32_bf16 v[" #o0 ":" #o1 "], v[" #a0 ":" #a1 "], v[" #p0 ":" #p1 "], v5\n\t"
#define LOADS(k0, k1, a0, a1, b0, b1)                                                     \
  "s_add_u32 s76, s76, 0x800\n\t"                                                         \
  "s_and_b32 s76, s76, 0xffff\n\t"                                                        \
  "buffer_load_dwordx4 v[" #k0 ":" #k1 "], v249, s[72:75], s76 offen sc1\n\t"             \
  "buffer_load_dwordx4 v[" #a0 ":" #a1 "], v250, s[72:75], s76 offen offset:1024 sc1\n\t" \
  "buffer_load_dwordx4 v[" #b0 ":" #b1 "], v251, s[72:75], s76 offen offset:1536 sc1\n\t"
#define WODMA                                                                             \
  "s_mov_b32 m0, s84\n\t"                                                                 \
  "s_add_u32 s84, s84, 0x400\n\t"                                                         \
  "buffer_load_dwordx4 v254, s[80:83], s85 offen lds\n\t"                                 \
  "s_add_u32 s85, s85, 0x2000\n\t"
#define ITER(ck0, ck1, ca0, ca1, cb0, cb1, pk0, pk1, pa0, pa1, pb0, pb1, nk0, nk1, EXTRA, WAIT) \
  EX(128) EX(129) PV(176, 191, pb0, pb1, 152, 159) EX(130) EX(131)                        \
  LOADS(pk0, pk1, pa0, pa1, pb0, pb1)                                                     \
  EX(132) EX(133) QK(144, 159, ck0, ck1, 196, 199, 112, 127) EX(134) EX(135) EXTRA EX(136) EX(137) EX(138) \
  PV(160, 175, ca0, ca1, 128, 135) EX(139) EX(140) EX(141) EX(142) EX(143)                 \
  EX(144) EX(145) PV(160, 175, cb0, cb1, 136, 143) EX(146) EX(147) EX(148) EX(149)         \
  WAIT                                                                                    \
  QK(128, 143, nk0, nk1, 192, 195, 96, 111) EX(150) EX(151) EX(152) EX(153) EX(154)      \
  PV(176, 191, ca0, ca1, 144, 151) EX(155) EX(156) EX(157) EX(158) EX(159)

#define ATTN_ROUTINE                                                                                  \
  "400:\n\t"                                                                                          \
  ITER(200, 203, 216, 219, 220, 223, 212, 215, 240, 243, 244, 247, 204, 207, WODMA, "")               \
  ITER(204, 207, 224, 227, 228, 231, 200, 203, 216, 219, 220, 223, 208, 211, "", "s_waitcnt vmcnt(7)\n\t")    \
  "s_cbranch_vccnz 402f\n\t401:\n\t"                                                                 \
  ITER(208, 211, 232, 235, 236, 239, 204, 207, 224, 227, 228, 231, 212, 215, WODMA, "s_waitcnt vmcnt(8)\n\t") \
  ITER(212, 215, 240, 243, 244, 247, 208, 211, 232, 235, 236, 239, 200, 203, "", "s_waitcnt vmcnt(7)\n\t")    \
  "s_sub_u32 s77, s77, 1\n\ts_waitcnt vmcnt(3)\n\ts_cbranch_scc0 400b\n\ts_setpc_b64 s[78:79]\n\t"  \
  "402:\n\ts_setprio 0\n\ts_branch 401b\n\t"
#define RUNV(N, V) "s_mov_b64 vcc, " V "\n\ts_mov_b32 s77, " N "\n\ts_swappc_b64 s[78:79], s[92:93]\n\t"
#define RUN(N) RUNV(N, "0")

#define STAMP(i) do { } while (0)
#define STAMP_RT(i) do { } while (0)
#define ST(i) ""
#define ST_END ""
#define ST_OP
#define ST_CL
#define ST_PAR
#define ST_ARG

#define G_BAR(N) "s_waitcnt vmcnt(" #N ") lgkmcnt(0)\n\ts_barrier\n\t"
#define G_LBAR "s_waitcnt lgkmcnt(0)\n\ts_barrier\n\t"
#define G_EPOCH "buffer_load_dwordx2 v[2:3], off, s[72:75], 0 sc1\n\t"
#define G_EPOCH_USE "v_readfirstlane_b32 s86, v2\n\tv_readfirstlane_b32 s87, v3\n\ts_add_u32 s86, s86, 1\n\ts_addc_u32 s87, s87, 0\n\t"
#define D_ROW(KC) "s_xor_b32 s52, s59, " KC "\n\ts_mul_i32 s52, s52, 196608\n\t"
#define D_A(R, COL, LOFF) "s_add_u32 m0, s57, " LOFF "\n\ts_nop 0\n\tbuffer_load_dwordx4 v[10+2*" #COL "], " R ", s52 offen lds\n\t"
#define D_B(R, COL) "buffer_load_dwordx4 v[11+2*" #COL "], " R ", s52 offen offset:1024 lds\n\t"
#define RA "s[40:43]"
#define RB "s[44:47]"
#define D_X(C)                                                                                        \
  "s_add_u32 m0, s58, 8192*" #C "\n\ts_xor_b32 s52, s60, 128*" #C "\n\t"                              \
  "buffer_load_dwordx4 v144, s[48:51], s52 offen lds\n\tbuffer_load_dwordx4 v145, s[48:51], s52 offen offset:1024 lds\n\t"
#define D_W(N, R0, C0, R1, C1) D_ROW(#N "&7") D_A(R0, C0, "16384*(" #N "&3)"), D_B(R0, C0), D_A(R1, C1, "16384*(" #N "&3)+8192"), D_B(R1, C1)
#define D_S0(N) D_W(N, RA, 1, RA, 2)
#define D_S1(N) D_W(N, RB, 0, RB, 1)
#define D_S2(N) D_W(N, RA, 0, RB, 2)
#define G_CAT(A, B, C, D) A B C D
#define G_APPLY(M, ...) M(__VA_ARGS__)
#define G_ALL(L) G_APPLY(G_CAT, L)

#define G_RW(W, C, S)                                                                                 \
  "ds_read_b64_tr_b16 v[" #W "+4*" #S ":" #W "+4*" #S "+1], v8 offset:16384*(" #C "&3)+2048*" #S "\n\t"       \
  "ds_read_b64_tr_b16 v[" #W "+4*" #S "+2:" #W "+4*" #S "+3], v8 offset:16384*(" #C "&3)+2048*" #S "+512\n\t"
#define G_RX(X, C, S, T) "ds_read_b128 v[" #X "+4*" #S ":" #X "+4*" #S "+3], v[4+" #S "] offset:8192*" #C "+4096*" #T "\n\t"
#define G_RS(W, X0, X1, C, S) G_RW(W, C, S) G_RX(X0, C, S, 0) G_RX(X1, C, S, 1)
#define G_READ(W, X0, X1, C) G_RS(W, X0, X1, C, 0) G_RS(W, X0, X1, C, 1) G_RS(W, X0, X1, C, 2) G_RS(W, X0, X1, C, 3)
#define G_M(ACC, W, X, S, CIN) \
  "v_mfma_f32_32x32x16_bf16 " ACC ", v[" #W "+4*" #S ":" #W "+4*" #S "+3], v[" #X "+4*" #S ":" #X "+4*" #S "+3], " CIN "\n\t"
#define G_A0 "v[16:31]"
#define G_A1 "v[32:47]"
#define G_MMA2(W, X0, X1, S) G_M(G_A0, W, X0, S, G_A0) G_M(G_A1, W, X1, S, G_A1)
#define G_STEP(RW, RX0, RX1, C, W, X0, X1, C0, C1, D0, D1, D2, D3)                                    \
  G_M(G_A0, W, X0, 0, C0) G_RW(RW, C, 0) G_RX(RX0, C, 0, 0) G_M(G_A1, W, X1, 0, C1) G_RX(RX1, C, 0, 1) G_RW(RW, C, 1) \
  G_M(G_A0, W, X0, 1, G_A0) G_RX(RX0, C, 1, 0) G_RX(RX1, C, 1, 1) D0 G_M(G_A1, W, X1, 1, G_A1) G_RW(RW, C, 2) G_RX(RX0, C, 2, 0) \
  G_M(G_A0, W, X0, 2, G_A0) G_RX(RX1, C, 2, 1) G_RW(RW, C, 3) D1 G_M(G_A1, W, X1, 2, G_A1) G_RX(RX0, C, 3, 0) G_RX(RX1, C, 3, 1) \
  G_M(G_A0, W, X0, 3, G_A0) D2 G_M(G_A1, W, X1, 3, G_A1) D3
#define G_EVEN(C, D0, D1, D2, D3) G_STEP(48, 64, 80, C, 96, 112, 128, G_A0, G_A1, D0, D1, D2, D3)
#define G_ODD(C, D0, D1, D2, D3) G_STEP(96, 112, 128, C, 48, 64, 80, G_A0, G_A1, D0, D1, D2, D3)
#define G_FIRST(D0, D1, D2, D3) G_STEP(96, 112, 128, 1, 48, 64, 80, "0", "0", D0, D1, D2, D3)
#define A_PACK                                                                                        \
  ".irp i,0,1,2,3\n\tv_cvt_pk_bf16_f32 v[48+\\i], v[16+2*\\i], v[17+2*\\i]\n\tv_cvt_pk_bf16_f32 v[56+\\i], v[24+2*\\i], v[25+2*\\i]\n\t.endr\n\t" \
  ".irp i,0,1,2,3\n\tv_cvt_pk_bf16_f32 v[52+\\i], v[32+2*\\i], v[33+2*\\i]\n\tv_cvt_pk_bf16_f32 v[60+\\i], v[40+2*\\i], v[41+2*\\i]\n\t.endr\n\t"
#define G_Z(L) ".irp r," L "\n\tv_mov_b64 v[\\r:\\r+1], 0\n\t.endr\n\t"
#define G_ZO G_Z("160,162,164,166,168,170,172,174,176,178,180,182,184,186,188,190,244,246")
#define G_QPRE "v_mov_b32 v254, v148\n\tv_add_u32 v6, s66, v159\n\tv_mov_b32 v4, v159\n\tv_xor_b32 v7, 0x400, v6\n\tv_mov_b32 v5, 0xdddddddd\n\t"
#define G_QRD "ds_read_b128 v[192:195], v7\n\tds_read_b128 v[196:199], v6\n\t"
#define G_C(L) ".irp r," L "\n\tv_mov_b32 v\\r, 0xc2400000\n\t.endr\n\t"
#define G_C0 G_C("96,97,98,99,100,101,102,103,104,105,106,107,108,109,110,111")
#define G_C1 G_C("112,113,114,115,116,117,118,119,120,121,122,123,124,125,126,127")
#define G_ST4(R, V, O) "buffer_store_dwordx4 v[" #R ":" #R "+3], v" #V ", s[72:75], " O " sc1\n\t"
#define G_POLL(R) "buffer_load_dwordx3 v[" #R ":" #R "+2], v158, s[72:75], 0 offen sc1\n\t"
#define G_SPIN(L, AGAIN) "s_cbranch_vccz " #L "1f\n\t" AGAIN "s_sub_u32 s53, s53, 1\n\ts_cbranch_scc0 " #L "0b\n\ts_mov_b32 s53, 0\n\t" #L "1:\n\t"
#define G_POLLWAIT(L, R)                                                                              \
  #L "0:\n\ts_waitcnt vmcnt(0)\n\tv_cmp_ne_u64 vcc, s[86:87], v[" #R ":" #R "+1]\n\t"                  \
  G_SPIN(L, "s_sleep 4\n\t" G_POLL(R)) "s_waitcnt vmcnt(0)\n\tv_mov_b32 v9, v[" #R "+2]\n\t"
#define G_KV1(K, V, T)                                                                                \
  "buffer_load_dwordx4 v[" #K ":" #K "+3], v249, s[72:75], s" #T " offen sc1\n\t"                      \
  "buffer_load_dwordx4 v[" #V ":" #V "+3], v250, s[72:75], s" #T " offen offset:1024 sc1\n\t"          \
  "buffer_load_dwordx4 v[" #V "+4:" #V "+7], v251, s[72:75], s" #T " offen offset:1536 sc1\n\t"
#define G_KV G_KV1(200, 216, 62) G_KV1(204, 224, 63) G_KV1(208, 232, 64)
#define DPPMAX(R, CTRL) "v_max_f32_dpp v" #R ", v" #R ", v" #R " " CTRL " row_mask:0xf bank_mask:0xf\n\t"
#define SIGMOID(R, OUT) \
  "v_mul_f32 v" #R ", 0xbfb8aa3b, v" #R "\n\tv_exp_f32 v" #R ", v" #R "\n\ts_nop 0\n\tv_add_f32 v" #R ", 1.0, v" #R "\n\tv_rcp_f32 v" #OUT ", v" #R "\n\t"
#define A_GREAD(C)                                                                                    \
  "v_xor_b32 v92, 128*" #C ", v153\n\tv_xor_b32 v93, 128*(" #C "+1), v153\n\t"                     \
  "ds_read_b128 v[72:75], v152 offset:8192*" #C "\n\tds_read_b128 v[80:83], v152 offset:8192*" #C "+4096\n\t" \
  "ds_read_b128 v[76:79], v152 offset:8192*(" #C "+1)\n\tds_read_b128 v[84:87], v152 offset:8192*(" #C "+1)+4096\n\t" \
  "ds_read_b128 v[64:67], v92\n\tds_read_b128 v[68:71], v93\n\t"
#define A_GDOT                                                                                        \
  ".irp j,0,1,2,3\n\t"                                                                                \
  "v_dot2c_f32_bf16 v88, v[72+\\j], v[64+\\j]\n\tv_dot2c_f32_bf16 v89, v[80+\\j], v[64+\\j]\n\t"       \
  "v_dot2c_f32_bf16 v90, v[76+\\j], v[68+\\j]\n\tv_dot2c_f32_bf16 v91, v[84+\\j], v[68+\\j]\n\t"       \
  ".endr\n\t"
#define A_DPPMAX(CTRL) "s_nop 1\n\tv_max_f32_dpp v96, v96, v96 " CTRL " row_mask:0xf bank_mask:0xf\n\t"
#define F_JOBS(M) M(16, 80, 48) M(32, 84, 56) M(96, 88, 64) M(112, 92, 72)
#define F_MAX1(E, T, P)                                                                               \
  "v_max3_f32 v[" #T "], v[" #E "], v[" #E "+1], v[" #E "+2]\n\t"                                      \
  "v_max3_f32 v[" #T "+2], v[" #E "+8], v[" #E "+9], v[" #E "+10]\n\t"                                 \
  "v_max3_f32 v[" #T "+1], v[" #E "+3], v[" #E "+4], v[" #E "+5]\n\t"                                  \
  "v_max3_f32 v[" #T "+3], v[" #E "+11], v[" #E "+12], v[" #E "+13]\n\t"                               \
  "v_max3_f32 v[" #T "], v[" #T "], v[" #E "+6], v[" #E "+7]\n\t"                                      \
  "v_max3_f32 v[" #T "+2], v[" #T "+2], v[" #E "+14], v[" #E "+15]\n\t"                                \
  "v_max_f32 v[" #T "], v[" #T "], v[" #T "+1]\n\t"                                                    \
  "v_max_f32 v[" #T "+2], v[" #T "+2], v[" #T "+3]\n\t"
#define F_SWAP(E, T, P) "v_permlane32_swap_b32 v[" #T "], v[" #T "+2]\n\t"
#define F_MAX2(E, T, P) "v_max_f32 v[" #T "], v[" #T "], v[" #T "+2]\n\t"
#define F_NEG(E, T, P) "v_mul_f32 v[" #T "], 0xbfb8aa3b, v[" #T "]\n\t"
#define F_DUP(E, T, P) "v_mov_b32 v[" #T "+2], v[" #T "]\n\t"
#define F_FMA(E, T)                                                                                   \
  ".irp i,0,1,2,3,4,5,6,7\n\t"                                                                        \
  "v_fma_f32 v[" #E "+\\i], v[" #E "+\\i], s71, v[" #T "]\n\t"                                      \
  "v_fma_f32 v[" #E "+8+\\i], v[" #E "+8+\\i], s71, v[" #T "+2]\n\t"                                \
  ".endr\n\t"
#define F_EX(E, L) ".irp i," L "\n\tv_exp_f32 v[" #E "+\\i], v[" #E "+\\i]\n\t.endr\n\t"
#define F_EXP(E, T) F_FMA(E, T) F_EX(E, "0,8,1,9,2,10,3,11,4,12,5,13,6,14,7,15")
#define F_SUM1(E, T, P) ".irp i,0,1,2,3,4,5,6,7\n\tv_add_f32 v[" #P "+\\i], v[" #E "+2*\\i], v[" #E "+2*\\i+1]\n\t.endr\n\t"
#define F_SUM2(E, T, P) ".irp i,0,2,4,6\n\tv_add_f32 v[" #P "+\\i], v[" #P "+\\i], v[" #P "+\\i+1]\n\t.endr\n\t"
#define F_SUM3(E, T, P) "v_add_f32 v[" #T "], v[" #P "], v[" #P "+2]\n\tv_add_f32 v[" #T "+2], v[" #P "+4], v[" #P "+6]\n\t"
#define F_ADD(E, T, P) "v_add_f32 v[" #T "], v[" #T "], v[" #T "+2]\n\t"
#define F_RCP(E, T, P) "v_rcp_f32 v[" #T "], v[" #T "]\n\t"
#define F_PACK(E, T, P) ".irp i,0,1,2,3,4,5,6,7\n\tv_cvt_pk_bf16_f32 v[" #P "+\\i], v[" #E "+2*\\i], v[" #E "+2*\\i+1]\n\t.endr\n\t"
#define F_SCALE(E, T, P)                                                                              \
  ".irp i,0,1,2,3,4,5,6,7\n\t"                                                                        \
  "v_mul_f32 v[" #E "+\\i], v[" #E "+\\i], v[" #T "]\n\t"                                              \
  "v_mul_f32 v[" #E "+8+\\i], v[" #E "+8+\\i], v[" #T "+2]\n\t"                                        \
  ".endr\n\t" F_PACK(E, T, P)
#define F_TR0(E, T, P) "v_mfma_f32_32x32x16_bf16 v[" #E ":" #E "+15], v[" #P ":" #P "+3], v[128:131], 0\n\t"
#define F_TR1(E, T, P) "v_mfma_f32_32x32x16_bf16 v[" #E ":" #E "+15], v[" #P "+4:" #P "+7], v[132:135], v[" #E ":" #E "+15]\n\t"
#define P_TEST(F, I)                                                                                  \
  "v_max3_u32 v80, v[" #F "+4*" #I "], v[" #F "+1+4*" #I "], v[" #F "+2+4*" #I "]\n\t"                 \
  "v_max_u32 v80, v80, v[" #F "+3+4*" #I "]\n\t"                                                       \
  "v_cmp_eq_u32 vcc, -1, v80\n\t"
#define P_LOAD(F, I, SO) "buffer_load_dwordx4 v[" #F "+4*" #I ":" #F "+3+4*" #I "], v4, s[72:75], s" #SO " offen offset:1024*(" #I "&3) sc1\n\t"
#define P_RD(R, I, OFF)                                                                               \
  "ds_read_b64_tr_b16 v[" R ":" R "+1], v0 offset:" #OFF "+2048*" #I "\n\t"                         \
  "ds_read_b64_tr_b16 v[" R "+2:" R "+3], v0 offset:" #OFF "+2048*" #I "+1024\n\t"                  \
  "ds_read_b64_tr_b16 v[" R "+4:" R "+5], v1 offset:" #OFF "+2048*" #I "\n\t"                       \
  "ds_read_b64_tr_b16 v[" R "+6:" R "+7], v1 offset:" #OFF "+2048*" #I "+1024\n\t"
#define P_RB(I) P_RD("192+8*(" #I "&3)", I, 0)
#define P_RA(I) P_RD("128+8*" #I, I, 16384)
#define P_MM(W, F, I, C0, C1)                                                                         \
  "v_mfma_f32_32x32x16_bf16 v[16:31], v[" W ":" W "+3], v[" #F "+4*" #I ":" #F "+3+4*" #I "], " C0 "\n\t"   \
  "v_mfma_f32_32x32x16_bf16 v[32:47], v[" W "+4:" W "+7], v[" #F "+4*" #I ":" #F "+3+4*" #I "], " C1 "\n\t"
#define P_USEB(I, N, C0, C1, MORE)                                                                    \
  "s_waitcnt lgkmcnt(" #N ")\n\t" P_MM("192+8*(" #I "&3)", 48, I, C0, C1) MORE
#define P_USEA(I, N) "s_waitcnt vmcnt(" #N ")\n\t" P_TEST(96, I) "s_cbranch_vccnz 57" #I "f\n\t56" #I ":\n\t" P_MM("128+8*" #I, 96, I, G_A0, G_A1)
#define P_STUB(A, B, I, R) #A #I ":\n\ts_call_b64 s[78:79], " #R "f\n\ts_branch " #B #I "b\n\t"
#define P_STUBS(A, B, R) P_STUB(A, B, 0, R) P_STUB(A, B, 1, R) P_STUB(A, B, 2, R) P_STUB(A, B, 3, R) P_STUB(A, B, 4, R) P_STUB(A, B, 5, R) P_STUB(A, B, 6, R) P_STUB(A, B, 7, R)
#define P_RETRY(F, I, SO) P_TEST(F, I) "s_cbranch_vccz 1f\n\ts_mov_b32 s52, 1\n\t" P_LOAD(F, I, SO) "1:\n\t"
#define P_SLOW(R, F, S0, S1)                                                                          \
  #R ":\n\ts_waitcnt vmcnt(0)\n\ts_mov_b32 s52, 0\n\t"                                             \
  P_RETRY(F, 0, S0) P_RETRY(F, 1, S0) P_RETRY(F, 2, S0) P_RETRY(F, 3, S0) P_RETRY(F, 4, S1) P_RETRY(F, 5, S1) P_RETRY(F, 6, S1) P_RETRY(F, 7, S1) \
  "s_sub_u32 s53, s53, 1\n\ts_cselect_b32 s52, 0, s52\n\ts_cselect_b32 s53, 0, s53\n\t" \
  "s_cmp_lg_u32 s52, 0\n\ts_cbranch_scc1 " #R "b\n\ts_waitcnt vmcnt(0)\n\ts_setpc_b64 s[78:79]\n\t"
#define P_LOAD8(F, S0, S1) P_LOAD(F, 0, S0) P_LOAD(F, 1, S0) P_LOAD(F, 2, S0) P_LOAD(F, 3, S0) P_LOAD(F, 4, S1) P_LOAD(F, 5, S1) P_LOAD(F, 6, S1) P_LOAD(F, 7, S1)
#define P_LOADA P_LOAD8(96, 54, 55)
#define OUT_POL " sc1"
#define HALF_SWITCH 0
#define A_AT 7
#define P_AT(I) ".if " STR(A_AT) " == " #I "\n\t" P_LOADA ".endif\n\t"
#define STR_(x) #x
#define STR(x) STR_(x)
#define M_PAR0                                                                                        \
  "s_lshl_b32 s62, s17, 11\n\ts_add_u32 s63, s62, 0x800\n\ts_and_b32 s63, s63, 0xffff\n\ts_add_u32 s64, s62, 0x1000\n\ts_and_b32 s64, s64, 0xffff\n\t" \
  "s_lshl_b32 s24, s16, 5\n\ts_lshl_b32 s4, s3, 10\n\ts_add_u32 s24, s24, s4\n\t"                       \
  "s_lshr_b32 s25, s16, 2\n\ts_and_b32 s4, s17, 3\n\ts_lshl_b32 s4, s4, 3\n\ts_add_u32 s25, s25, s4\n\ts_lshr_b32 s4, s17, 2\n\ts_add_u32 s25, s25, s4\n\t" \
  "s_lshl_b32 s29, s25, 13\n\ts_add_u32 s29, s29, 0x100000\n\ts_lshl_b32 s30, s25, 8\n\ts_add_u32 s30, s30, 0x10000\n\t" \
  "s_lshr_b32 s28, s25, 4\n\ts_lshl_b32 s28, s28, 12\n\ts_add_u32 s28, s28, 0x70000\n\t"
#define M_PAR1                                                                                        \
  "s_lshl_b32 s31, s24, 1\n\ts_add_u32 s31, s31, 0x10000\n\ts_lshl_b32 s39, s24, 7\n\ts_add_u32 s39, s39, 0x900000\n\t" \
  "s_lshl_b32 s32, s62, 5\n\ts_add_u32 s32, s32, s24\n\ts_add_u32 s24, s32, 0x700000\n\ts_add_u32 s25, s32, 0x708000\n\t" \
  "s_add_u32 s26, s32, 0x500000\n\ts_add_u32 s27, s32, 0x508000\n\ts_lshr_b32 s32, s32, 3\n\ts_add_u32 s32, s32, 0x80000\n\t" \
  "s_lshl_b32 s4, s18, 16\n\ts_lshl_b32 s5, s3, 13\n\ts_add_u32 s5, s5, s4\n\ts_lshr_b32 s33, s5, 3\n\ts_add_u32 s33, s33, 0x80000\n\t" \
  "s_lshl_b32 s38, s56, 15\n\ts_add_u32 s34, s5, s38\n\ts_add_u32 s34, s34, 0x500000\n\ts_add_u32 s38, s38, s24\n\t" \
  "s_lshl_b32 s91, s37, 13\n\ts_add_u32 s91, s91, s4\n\ts_add_u32 s91, s91, s19\n\t"
#define M_PAR2                                                                                        \
  "s_lshl_b32 s35, s58, 1\n\ts_add_u32 s35, s35, 0x20000\n\t"                                         \
  "s_add_u32 s36, s58, 0x26200\n\ts_lshl_b32 s88, s56, 11\n\ts_add_u32 s88, s88, s35\n\ts_lshl_b32 s89, s56, 10\n\t" \
  "s_add_u32 s89, s89, s58\n\ts_add_u32 s66, s89, 0x24000\n\ts_add_u32 s89, s89, 0x26000\n\ts_lshl_b32 s90, s37, 11\n\t" \
  "s_mov_b32 s76, s64\n\ts_lshl_b32 s84, s58, 4\n\ts_lshl_b32 s85, s56, 14\n\ts_add_u32 s84, s84, s85\n\ts_mov_b32 s85, 0\n\t" \
  "s_and_b32 s4, s3, 1\n\ts_lshl_b32 s4, s4, 12\n\ts_add_u32 s69, s4, 0x26000\n\ts_lshl_b32 s65, s61, 13\n\ts_add_u32 s65, s65, s4\n\t" \
  "s_add_u32 s65, s65, 0x24000\n\ts_lshl_b32 s67, s4, 1\n\ts_add_u32 s67, s67, 0x20000\n\ts_mov_b32 s71, 0x3fb8aa3b\n\t" \
  "s_or_b32 s4, s2, s37\n\ts_cselect_b32 s68, 0xf0000000, 0\n\t"
static_assert(WS_OB == 0x500000 && WS_OA == 0x700000 && WS_FOB == 0x80000 && WS_KP == 0x100000 && WS_KR == 0x10000 && WS_FKP == 0x70000 &&
              WS_KV == 0x900000 && L_PQ == 0x20000 && L_VB == 0x26000 && L_Q == 0x24000 && L_RING == 0x10000 && L_WPHI == 0x24000 && L_GW == 0x24800 && L_GP == 0x25000 &&
              L_DUMMY == 0x24C00 && WS_FKV == 0x60000 && WS_ONES == 0x40000 && WS_EPOCH == 0 && L_X == 0 && L_WO == 0 && L_RED == 0 && FL == 128, "literals above");
#define V_OOB(R, S, CMP) CMP "v_mov_b32 v" #R ", " S "\n\ts_nop 0\n\tv_cndmask_b32 v" #R ", v30, v" #R ", vcc\n\t"
DI void program(unsigned tid, unsigned bid, unsigned g128, unsigned tb, unsigned otb, unsigned og128, unsigned rot ST_PAR) {
  asm volatile(
      "s_load_dwordx2 s[48:49], s[0:1], 0x0\n\ts_load_dwordx2 s[40:41], s[0:1], 0x8\n\ts_load_dwordx2 s[44:45], s[0:1], 0x18\n\t"
      "v_readfirstlane_b32 s37, v0\n\t"
      "s_load_dwordx2 s[72:73], s[0:1], 0x40\n\ts_load_dwordx2 s[20:21], s[0:1], 0x38\n\t"
      "s_getpc_b64 s[92:93]\n\t"
      "99:\n\t"
      "v_and_b32 v16, 63, v0\n\t"
      "s_lshr_b32 s37, s37, 6\n\t"
      "s_cbranch_scc1 98f\n\t"
      "s_lshr_b32 s4, s2, 3\n\tv_bfe_u32 v254, v0, 2, 3\n\tv_lshl_add_u32 v254, v254, 5, s4\n\tv_cmp_gt_u32 vcc, 32, v0\n\t"
      "v_add_u32 v254, 32, v254\n\ts_nop 0\n\tv_cndmask_b32 v254, v254, v0, vcc\n\tv_lshlrev_b32 v254, 7, v254\n\t"
      "v_min_u32 v254, 599f-99b-4, v254\n\tglobal_load_dword v254, v254, s[92:93]\n\t"
      "98:\n\t"
      "s_and_b32 s3, s37, 3\n\ts_lshr_b32 s56, s37, 2\n\ts_lshr_b32 s61, s3, 1\n\t"
      "s_mul_i32 s4, s56, -24\n\ts_add_u32 s4, s4, 0x28\n\ts_load_dwordx2 s[80:81], s[0:1], s4\n\t"
      "s_lshl_b32 s4, s61, 4\n\ts_add_u32 s4, s4, 0x20\n\ts_load_dwordx2 s[28:29], s[0:1], s4\n\t"
      "v_lshrrev_b32 v17, 3, v16\n\tv_lshlrev_b32 v159, 4, v16\n\tv_lshlrev_b32 v19, 2, v16\n\tv_and_b32 v18, 0x70, v159\n\t"
      "s_mul_i32 s4, s3, 49152\n\ts_add_u32 s4, s4, s16\n\ts_lshl_b32 s58, s3, 11\n\ts_add_u32 s57, s58, 0x10000\n\t"
      "v_and_b32 v19, 64, v19\n\tv_mul_u32_u24 v10, 3072, v17\n\tv_xor_b32 v19, v19, v18\n\tv_and_b32 v144, 0x30, v16\n\t"
      "v_add3_u32 v10, v10, v19, s4\n\tv_xor_b32 v144, v144, v18\n\t"
      "s_lshl_b32 s4, s17, 16\n\ts_lshl_b32 s5, s3, 14\n\ts_add_u32 s4, s4, s5\n\ts_lshl_b32 s60, s59, 7\n\t"
      "v_lshl_or_b32 v144, v17, 10, v144\n\tv_add_u32 v12, 0x400, v10\n\tv_add_u32 v13, 0x6000, v10\n\tv_add_u32 v144, s4, v144\n\t"
      "v_add_u32 v14, 0x800, v10\n\tv_add_u32 v15, 0x6400, v10\n\tv_xor_b32 v145, 64, v144\n\tv_add_u32 v11, 0x5c00, v10\n\t"
      "v_add_u32 v145, 0x1c00, v145\n\t"
      "s_mov_b32 s50, 0x200000\n\ts_mov_b32 s51, 0x20000\n\ts_mov_b32 s42, 0x180000\n\ts_mov_b32 s43, s51\n\t"
      "s_cmp_eq_u32 s56, 0\n\t"
      "s_waitcnt lgkmcnt(0)\n\t"
      "s_cbranch_scc1 90f\n\t"
      D_X(0) G_ALL(D_S0(0)) D_X(1) G_ALL(D_S0(1))
      "s_branch 91f\n\t"
      "90:\n\t"
      "s_mov_b32 s74, " STR(WS_LEN) "\n\ts_mov_b32 s75, s51\n\ts_mov_b32 s31, s51\n\t"
      "s_lshr_b32 s4, s2, 3\n\ts_lshl_b32 s4, s4, 3\n\tv_add_u32 v20, s4, v17\n\tv_lshlrev_b32 v20, 16, v20\n\t"
      "s_cmp_eq_u32 s3, 3\n\ts_cbranch_scc1 92f\n\t"
      "s_lshl_b32 s4, s16, 4\n\ts_lshl_b32 s5, s3, 10\n\ts_add_u32 s4, s4, s5\n\ts_cmp_eq_u32 s61, 0\n\t"
      "s_cselect_b32 s4, s4, 0\n\ts_cselect_b32 s30, 16, 1\n\ts_add_u32 m0, s5, 0x24000\n\ts_lshl_b32 s30, s30, 10\n\t"
      "buffer_load_dwordx4 v159, s[28:31], s4 offen lds\n\t"
      "s_branch 91f\n\t"
      "92:\n\t"
      "s_mov_b32 m0, 0x24c00\n\ts_nop 0\n\tbuffer_load_dword v20, s[72:75], 0 offen offset:3968 sc1 lds\n\t"
      "91:\n\t"
      "s_mov_b32 s46, s42\n\ts_mov_b32 s47, s51\n\ts_mov_b32 s74, " STR(WS_LEN) "\n\ts_mov_b32 s75, s51\n\t"
      "s_mov_b32 s22, s50\n\ts_mov_b32 s23, s51\n\ts_mov_b32 s82, 0x80000\n\ts_mov_b32 s83, s51\n\t"
      "s_add_u32 s92, s92, 400f-99b\n\ts_addc_u32 s93, s93, 0\n\ts_mov_b32 s53, 0x40000\n\t"
      "v_and_b32 v20, 31, v16\n\tv_lshrrev_b32 v21, 5, v16\n\tv_bfe_u32 v22, v16, 1, 3\n\tv_bfrev_b32 v30, 15\n\t"
      "v_xor_b32 v22, v22, v21\n\tv_lshlrev_b32 v23, 7, v20\n\tv_lshl_or_b32 v4, v22, 4, v23\n\t"
      "v_xor_b32 v5, 32, v4\n\tv_xor_b32 v6, 64, v4\n\tv_xor_b32 v7, 96, v4\n\t"
      "v_lshlrev_b32 v24, 3, v16\n\tv_lshlrev_b32 v25, 1, v16\n\tv_lshlrev_b32 v26, 5, v16\n\tv_and_b32 v27, 0x58, v24\n\t"
      "v_and_or_b32 v27, v25, 32, v27\n\tv_and_b32 v26, 0x180, v26\n\tv_or_b32 v27, v27, v26\n\t"
      "s_and_b32 s6, s3, 1\n\ts_lshl_b32 s4, s6, 6\n\ts_lshl_b32 s5, s61, 13\n\ts_add_u32 s5, s5, 0x10000\n\t"
      "v_xor_b32 v8, s4, v27\n\tv_lshl_add_u32 v8, v21, 10, v8\n\tv_add_u32 v8, s5, v8\n\t"
      "s_lshl_b32 s4, s3, 15\n\tv_lshl_add_u32 v0, v21, 9, v27\n\tv_add_u32 v0, s4, v0\n\t"
      "s_lshl_b32 s4, s3, 14\n\ts_lshl_b32 s5, s56, 13\n\ts_add_u32 s4, s4, s5\n\t"
      "v_and_b32 v26, 7, v16\n\tv_xor_b32 v26, v26, v21\n\tv_lshlrev_b32 v26, 4, v26\n\tv_lshl_or_b32 v26, v20, 8, v26\n\tv_add_u32 v248, s4, v26\n\t"
      "s_lshl_b32 s4, s16, 12\n\ts_lshl_b32 s5, s3, 17\n\ts_add_u32 s7, s4, 0x900000\n\ts_add_u32 s4, s7, s5\n\t"
      "s_lshl_b32 s8, s56, 16\n\ts_add_u32 s4, s4, s8\n\ts_add_u32 s5, s5, s19\n\t"
      "v_and_b32 v28, 0xf0, v159\n\tv_add_u32 v249, s4, v159\n\tv_lshl_or_b32 v29, v21, 8, v28\n\tv_add_u32 v26, s4, v29\n\t"
      "v_lshl_add_u32 v148, v17, 10, v19\n\tv_add_u32 v148, s5, v148\n\t"
      "s_lshl_b32 s9, s16, 1\n\ts_add_u32 s4, s9, 0x3fc00\n\tv_and_b32 v25, 27, v16\n\tv_and_b32 v22, 16, v16\n\t"
      V_OOB(250, "s4", "v_cmp_eq_u32 vcc, 16, v25\n\t")
      "v_subrev_u32 v251, 0x200, v250\n\tv_cmp_eq_u32 vcc, 0, v22\n\tv_lshrrev_b32 v25, 2, v20\n\tv_mov_b32 v156, 0x3f80\n\t"
      "v_cndmask_b32 v250, v250, v26, vcc\n\tv_cndmask_b32 v251, v251, v26, vcc\n\t"
      "v_and_b32 v26, 1, v16\n\t"
      "v_lshlrev_b32 v26, 4, v26\n\tv_lshlrev_b32 v156, v26, v156\n\tv_xor_b32 v26, v25, v21\n\tv_and_b32 v25, 6, v25\n\t"
      "v_bfe_u32 v31, v16, 1, 1\n\tv_or_b32 v25, v25, v31\n\tv_not_b32 v26, v26\n\tv_and_b32 v26, 1, v26\n\tv_lshlrev_b32 v157, v25, v26\n\t"
      "s_lshl_b32 s4, s16, 5\n\ts_add_u32 s10, s4, 0x60000\n\ts_lshl_b32 s4, s3, 4\n\ts_add_u32 s4, s4, s10\n\tv_add_u32 v158, s4, v23\n\t"
      "v_lshlrev_b32 v154, 2, v16\n\t"
      "s_cmp_eq_u32 s56, 0\n\t"
      "s_cbranch_scc1 100f\n\t"
      "s_lshl_b32 s4, s6, 10\n\ts_add_u32 s4, s4, 0x24000\n\tv_and_b32 v146, 0x78, v24\n\tv_lshl_or_b32 v146, v21, 7, v146\n\tv_add_u32 v146, s4, v146\n\t"
      "v_mov_b32 v147, 0x80008000\n\tv_cndmask_b32 v147, v147, 0, vcc\n\tv_cndmask_b32 v152, 1.0, 0, vcc\n\t"
      "s_lshl_b32 s4, s16, 11\n\ts_lshl_b32 s5, s6, 17\n\ts_add_u32 s4, s4, s5\n\ts_lshl_b32 s5, s17, 8\n\ts_add_u32 s4, s4, s5\n\ts_add_u32 s4, s4, 0x100000\n\t"
      "v_lshrrev_b32 v25, 4, v16\n\tv_lshl_or_b32 v149, v25, 13, v28\n\tv_add_u32 v149, s4, v149\n\t"
      "s_lshl_b32 s4, s16, 6\n\ts_lshl_b32 s5, s6, 12\n\ts_add_u32 s4, s4, s5\n\ts_lshl_b32 s5, s17, 7\n\ts_add_u32 s4, s4, s5\n\ts_add_u32 s4, s4, 0x70000\n\t"
      V_OOB(150, "s4", "v_cmp_eq_u32 vcc, 0, v16\n\t")
      "v_add_u32 v154, 0x25100, v154\n\t"
      G_BAR(6) D_X(2) G_ALL(D_S0(2)) G_BAR(6) D_X(3) G_ALL(D_S0(3)) G_BAR(6) D_X(4) G_ALL(D_S0(4)) M_PAR0
      G_BAR(6) D_X(5) G_ALL(D_S0(5)) M_PAR1 G_BAR(6) D_X(6) G_ALL(D_S0(6)) M_PAR2 G_BAR(6) D_X(7) G_ALL(D_S0(7))
      G_BAR(6) G_ALL(D_S1(8)) G_ALL(D_S1(9)) G_BAR(8) G_ALL(D_S1(10))
      "s_branch 200f\n\t"
      "100:\n\t"
      G_EPOCH
      "s_lshl_b32 s4, s6, 18\n\ts_add_u32 s7, s7, s4\n\ts_lshl_b32 s4, s17, 12\n\ts_add_u32 s7, s7, s4\n\t"
      "s_lshl_b32 s4, s17, 7\n\ts_add_u32 s10, s10, s4\n\t"
      "s_lshl_b32 s4, s17, 11\n\ts_add_u32 s9, s9, s4\n\ts_add_u32 s9, s9, 0x40000\n\ts_cmp_eq_u32 s3, 2\n\ts_cselect_b32 s9, s9, 0xf0000000\n\t"
      "s_lshl_b32 s4, s16, 6\n\ts_and_b32 s5, s17, 3\n\ts_lshl_b32 s5, s5, 11\n\ts_add_u32 s4, s4, s5\n\ts_lshr_b32 s5, s17, 2\n\ts_lshl_b32 s5, s5, 8\n\t"
      "s_add_u32 s4, s4, s5\n\ts_add_u32 s4, s4, 0x10000\n\ts_cmp_eq_u32 s37, 0\n\ts_cselect_b32 s4, s4, 0xf0000000\n\ts_cselect_b32 s5, s10, 0xf0000000\n\t"
      "v_add_u32 v151, s4, v154\n\tv_add_u32 v155, s5, v159\n\tv_cmp_gt_u32 vcc, 4, v16\n\t"
      "s_lshl_b32 s4, s3, 5\n\tv_xor_b32 v152, s4, v4\n\ts_add_u32 s4, s4, s60\n\ts_add_u32 s4, s4, 0x24800\n\t"
      "v_cndmask_b32 v155, v30, v155, vcc\n\tv_lshl_add_u32 v153, v21, 4, s4\n\t"
      "s_lshl_b32 s4, s3, 9\n\ts_add_u32 s4, s4, 0x25000\n\tv_add_u32 v154, s4, v154\n\t"
      "s_cmp_lg_u32 s61, 0\n\t"
      "s_cbranch_scc1 104f\n\t"
      "v_and_b32 v149, 0x330, v159\n\tv_lshlrev_b32 v25, 1, v159\n\tv_lshrrev_b32 v26, 1, v159\n\tv_and_b32 v25, 0x80, v25\n\t"
      "v_and_or_b32 v149, v26, 64, v149\n\tv_add3_u32 v149, v149, v25, s7\n\t"
      "s_lshl_b32 s4, s6, 5\n\ts_add_u32 s4, s4, s10\n\ts_add_u32 s4, s4, 8\n\tv_cmp_eq_u32 vcc, 0, v20\n\t"
      "v_lshl_add_u32 v150, v21, 4, s4\n\ts_nop 0\n\tv_cndmask_b32 v150, v30, v150, vcc\n\t"
      "s_branch 105f\n\t"
      "104:\n\t"
      "s_add_u32 s7, s7, 0x400\n\tv_lshl_add_u32 v149, v22, 13, v29\n\tv_add_u32 v149, s7, v149\n\t"
      "v_cmp_gt_u32 vcc, 8, v16\n\tv_add_u32 v150, s9, v159\n\ts_nop 0\n\tv_cndmask_b32 v150, v30, v150, vcc\n\t"
      "105:\n\t"
      G_Z("16,18,20,22,24,26,28,30,32,34,36,38,40,42,44,46")
      "s_mov_b32 s52, 0\n\t"
      G_LBAR G_READ(48, 64, 80, 0)
      "s_branch 102f\n\t"
      "101:\n\t"
      G_BAR(0) G_EVEN(0, "", "", "", "")
      "102:\n\t"
      G_LBAR G_ODD(1, "", "", "", "") G_LBAR G_EVEN(2, "", "", "", "") G_LBAR G_ODD(3, "", "", "", "")
      ".irp r,4,5,6,7\n\tv_xor_b32 v\\r, 0x8000, v\\r\n\t.endr\n\t"
      "s_xor_b32 s52, s52, 1\n\t"
      "s_cbranch_scc1 101b\n\t"
      ST(1)
      G_LBAR
      G_MMA2(96, 112, 128, 0) G_MMA2(96, 112, 128, 1) G_MMA2(96, 112, 128, 2) G_MMA2(96, 112, 128, 3)
      G_EPOCH_USE G_Z("88,90,92,94")
      "s_cmp_lg_u32 s61, 0\n\t"
      "s_cbranch_scc1 110f\n\t"
      "s_nop 0\n\t"
      ".irp r,16,17,18,19,20,21,22,23,24,25,26,27,28,29,30,31\n\tv_mul_f32 v\\r, 0x3eb8aa3b, v\\r\n\t.endr\n\t"
      G_LBAR
      ".irp r,32,33,34,35,36,37,38,39,40,41,42,43,44,45,46,47\n\tv_mul_f32 v\\r, 0x3eb8aa3b, v\\r\n\t.endr\n\t"
      A_PACK
      G_ST4(48, 149, "0 offen") G_ST4(52, 149, "0 offen offset:2048")
      ".irp j,0,1,2,3\n\t"
      "v_dot2c_f32_bf16 v92, v[48+\\j], v[48+\\j]\n\tv_dot2c_f32_bf16 v93, v[52+\\j], v[52+\\j]\n\t"
      "v_dot2c_f32_bf16 v94, v[56+\\j], v[56+\\j]\n\tv_dot2c_f32_bf16 v95, v[60+\\j], v[60+\\j]\n\t"
      ".endr\n\t"
      "s_mov_b32 s54, 131072\n\t"
      G_LBAR
      G_ST4(56, 149, "s54 offen") G_ST4(60, 149, "s54 offen offset:2048")
      "v_permlane32_swap_b32 v92, v94\n\tv_permlane32_swap_b32 v93, v95\n\t"
      "v_add_f32 v96, v92, v94\n\tv_add_f32 v97, v93, v95\n\t"
      A_GREAD(0)
      "v_max_f32 v96, v96, v97\n\t"
      A_DPPMAX("quad_perm:[1,0,3,2]") A_DPPMAX("quad_perm:[2,3,0,1]") A_DPPMAX("row_half_mirror") A_DPPMAX("row_mirror")
      "v_mov_b32 v97, v96\n\tv_mov_b32 v100, -1\n\ts_nop 0\n\tv_permlane16_swap_b32 v96, v97\n\tv_max_f32 v96, v96, v97\n\t"
      "buffer_store_dword v96, v150, s[72:75], 0 offen sc1\n\t"
      "buffer_store_dword v100, v151, s[72:75], 0 offen sc1\n\t"
      "s_branch 111f\n\t"
      "110:\n\t"
      ".irp j,0,1,2,3,4,5,6,7\n\tv_bfe_i32 v[96+\\j], v157, \\j, 1\n\t.endr\n\t"
      A_PACK
      ".irp j,0,1,2,3,4,5,6,7\n\tv_and_b32 v[96+\\j], v156, v[96+\\j]\n\t.endr\n\t"
      G_LBAR
      "s_setprio 2\n\t"
      "v_mfma_f32_32x32x16_bf16 v[16:31], v[48:51], v[96:99], 0\n\t"
      "v_mfma_f32_32x32x16_bf16 v[32:47], v[52:55], v[96:99], 0\n\t"
      "v_mfma_f32_32x32x16_bf16 v[16:31], v[56:59], v[100:103], v[16:31]\n\t"
      "v_mfma_f32_32x32x16_bf16 v[32:47], v[60:63], v[100:103], v[32:47]\n\t"
      "s_setprio 0\n\t"
      ".irp r,104,105,106,107\n\tv_mov_b32 v\\r, 0x3f803f80\n\t.endr\n\t"
      G_ST4(104, 150, "0 offen")
      "s_nop 4\n\t"
      F_PACK(16, 0, 48)
      G_ST4(48, 149, "0 offen") G_ST4(52, 149, "0 offen offset:512")
      G_LBAR
      F_PACK(32, 0, 56)
      G_ST4(56, 149, "0 offen offset:2048") G_ST4(60, 149, "0 offen offset:2560")
      A_GREAD(0)
      "111:\n\t"
      G_LBAR A_GDOT A_GREAD(2) M_PAR0
      G_LBAR A_GDOT A_GREAD(4) M_PAR1
      G_LBAR A_GDOT A_GREAD(6) M_PAR2
      G_LBAR A_GDOT
      "v_mov_b32 v94, s86\n\tv_mov_b32 v95, s87\n\tv_and_b32 v92, 0xfffff0ff, v154\n\t"
      "v_add_f32 v88, v88, v90\n\tv_add_f32 v89, v89, v91\n\t"
      "ds_write2st64_b32 v154, v88, v89 offset1:1\n\t"
      G_BAR(0)
      "ds_read2st64_b32 v[64:65], v92 offset1:1\n\tds_read2st64_b32 v[66:67], v92 offset0:2 offset1:3\n\t"
      "ds_read2st64_b32 v[68:69], v92 offset0:4 offset1:5\n\tds_read2st64_b32 v[70:71], v92 offset0:6 offset1:7\n\t"
      "buffer_store_dwordx2 v[94:95], v155, s[72:75], 0 offen sc1\n\t"
      "s_waitcnt lgkmcnt(0)\n\t"
      "v_add_f32 v64, v64, v66\n\tv_add_f32 v65, v65, v67\n\tv_add_f32 v68, v68, v70\n\tv_add_f32 v69, v69, v71\n\t"
      "v_add_f32 v64, v64, v68\n\tv_add_f32 v65, v65, v69\n\t"
      "s_nop 1\n\tv_permlane32_swap_b32 v64, v65\n\t"
      "v_add_f32 v64, v64, v65\n\t" SIGMOID(64, 253)
      "s_nop 0\n\tv_mov_b32 v252, v253\n\t"
      ST(2)
      G_LBAR "s_setprio 1\n\t" "v_permlane32_swap_b32 v253, v252\n\t" G_READ(48, 64, 80, 0) G_ALL(D_S2(19))
      G_LBAR G_APPLY(G_FIRST, D_S2(20))
      G_LBAR G_APPLY(G_EVEN, 2, D_S2(21)) G_BAR(8) G_APPLY(G_ODD, 3, D_S2(22))
      G_BAR(8) G_APPLY(G_EVEN, 4, D_S2(23))
      G_BAR(8) G_ODD(5, "buffer_load_dword v9, v158, s[72:75], 0 offen offset:8 sc1\n\t", G_KV1(200, 216, 62), G_KV1(204, 224, 63), G_KV1(208, 232, 64))
      G_BAR(14) G_EVEN(6, G_Z("160,162,164,166,168"), G_Z("170,172,174,176,178"), G_Z("180,182,184,186"), G_Z("188,190,244,246"))
      G_BAR(9) G_ODD(7, DPPMAX(9, "quad_perm:[1,0,3,2]"), DPPMAX(9, "quad_perm:[2,3,0,1]"), DPPMAX(9, "row_half_mirror"), "s_nop 0\n\t" DPPMAX(9, "row_mirror"))
      "s_waitcnt lgkmcnt(0)\n\t"
      G_MMA2(96, 112, 128, 0) G_MMA2(96, 112, 128, 1) G_MMA2(96, 112, 128, 2) G_MMA2(96, 112, 128, 3)
      "v_add_u32 v1, s65, v159\n\t"
      "s_setprio 2\n\t"
      G_QPRE G_Z("152,154,156,158") "v_mov_b32 v8, v9\n\t"
      A_PACK
      ".irp i,0,1,2,3\n\tds_write_b128 v1, v[48+4*\\i:51+4*\\i] offset:1024*\\i\n\t.endr\n\t"
      G_C0 G_C1
      ST(3)
      G_LBAR
      G_QRD
      "s_branch 300f\n\t"
      ATTN_ROUTINE
      "200:\n\t"
      ST(1)
      G_BAR(8) "s_setprio 1\n\t" G_EPOCH G_READ(48, 64, 80, 0) G_ALL(D_S1(11))
      G_BAR(9) G_APPLY(G_FIRST, D_S1(12))
      G_BAR(9) G_APPLY(G_EVEN, 2, D_S1(13)) G_BAR(8) G_APPLY(G_ODD, 3, D_S1(14))
      G_BAR(8) G_APPLY(G_EVEN, 4, D_S1(15)) G_BAR(8) G_APPLY(G_ODD, 5, D_S2(16))
      G_BAR(8) G_APPLY(G_EVEN, 6, D_S2(17)) G_BAR(8) G_APPLY(G_ODD, 7, D_S2(18))
      ST(2)
      G_BAR(8)
      "s_setprio 0\n\t"
      "ds_read_b64_tr_b16 v[64:65], v146\n\tds_read_b64_tr_b16 v[66:67], v146 offset:256\n\t"
      "ds_read_b64_tr_b16 v[68:69], v146 offset:512\n\tds_read_b64_tr_b16 v[70:71], v146 offset:768\n\t"
      "ds_read2st64_b32 v[72:73], v154 offset1:2\n\tds_read2st64_b32 v[74:75], v154 offset0:4 offset1:6\n\t"
      G_MMA2(96, 112, 128, 0) G_EPOCH_USE G_MMA2(96, 112, 128, 1)
      "s_waitcnt lgkmcnt(0)\n\t"
      "v_add_f32 v72, v72, v74\n\tv_add_f32 v73, v73, v75\n\t"
      ".irp r,64,65,66,67\n\tv_xor_b32 v\\r, v147, v\\r\n\t.endr\n\t"
      "v_add_f32 v72, v72, v73\n\t"
      G_MMA2(96, 112, 128, 2)
      "v_mov_b32 v73, v72\n\t"
      ".irp r,68,69,70,71\n\tv_xor_b32 v\\r, v147, v\\r\n\t.endr\n\t"
      "v_permlane32_swap_b32 v72, v73\n\t"
      G_MMA2(96, 112, 128, 3)
      "v_add_f32 v72, v72, v73\n\t" SIGMOID(72, 253)
      "s_nop 5\n\t"
      A_PACK
      G_BAR(4)
      "s_setprio 2\n\t"
      "v_mfma_f32_32x32x16_bf16 v[16:31], v[64:67], v[48:51], 0\n\t"
      "v_mfma_f32_32x32x16_bf16 v[32:47], v[64:67], v[52:55], 0\n\t"
      ".irp j,0,1,2,3\n\tv_bfe_i32 v[128+\\j], v157, \\j, 1\n\t.endr\n\t"
      "v_mfma_f32_32x32x16_bf16 v[96:111], v[68:71], v[56:59], 0\n\t"
      ".irp j,4,5,6,7\n\tv_bfe_i32 v[128+\\j], v157, \\j, 1\n\t.endr\n\t"
      "v_mfma_f32_32x32x16_bf16 v[112:127], v[68:71], v[60:63], 0\n\t"
      "s_setprio 0\n\t"
      ".irp j,0,1,2,3,4,5,6,7\n\tv_and_b32 v[128+\\j], v156, v[128+\\j]\n\t.endr\n\t"
      F_JOBS(F_MAX1) "s_nop 0\n\t" F_JOBS(F_SWAP) F_JOBS(F_MAX2) F_JOBS(F_NEG) F_JOBS(F_DUP) F_JOBS(F_SWAP)
      G_BAR(0)
      F_EXP(16, 80) F_FMA(32, 84) F_EX(32, "0,8,1,9")
      G_LBAR
      G_POLL(140) F_EX(32, "2,10,3,11,4,12,5,13,6,14,7,15") F_EXP(96, 88)
      G_LBAR
      F_EXP(112, 92) F_SUM1(16, 80, 48) F_SUM1(32, 84, 56) G_POLLWAIT(22, 140)
      G_LBAR
      F_SUM1(96, 88, 64) DPPMAX(9, "quad_perm:[1,0,3,2]") F_SUM1(112, 92, 72) DPPMAX(9, "quad_perm:[2,3,0,1]") F_JOBS(F_SUM2)
      DPPMAX(9, "row_half_mirror") F_JOBS(F_SUM3) DPPMAX(9, "row_mirror") F_JOBS(F_SWAP) F_JOBS(F_ADD) F_JOBS(F_RCP) F_JOBS(F_DUP) F_JOBS(F_SWAP)
      G_LBAR
      G_KV1(200, 216, 62) F_SCALE(16, 80, 48) G_KV1(204, 224, 63) F_SCALE(32, 84, 56) G_KV1(208, 232, 64) F_SCALE(96, 88, 64)
      G_LBAR
      F_SCALE(112, 92, 72)
      "s_cmp_lg_u32 s61, 0\n\t"
      "s_cbranch_scc1 230f\n\t"
      "v_add_u32 v136, s67, v159\n\t"
      ".irp i,0,1,2,3,4,5,6,7\n\tds_write_b128 v136, v[48+4*\\i:51+4*\\i] offset:1024*\\i\n\t.endr\n\t"
      G_ZO G_C0
      "s_branch 231f\n\t"
      "230:\n\t"
      F_JOBS(F_TR0) F_JOBS(F_TR1) G_ZO F_JOBS(F_PACK)
      ".irp r,96,97,98,99,100,101,102,103,104,105,106,107,108,109,110,111\n\tv_mov_b32 v\\r, v152\n\t.endr\n\t"
      "v_add_u32 v136, s69, v159\n\tv_mov_b32 v2, s86\n\tv_mov_b32 v3, s87\n\t"
      "231:\n\t"
      G_QPRE G_Z("152,154,156,158") "v_mov_b32 v8, v9\n\t" G_C1
      ST(3)
      G_LBAR
      G_QRD
      "s_cmp_eq_u32 s61, 0\n\t"
      "s_cbranch_scc1 151f\n\t"
      "s_setprio 3\n\t"
      ".irp i,0,1,2,3\n\tds_read_b128 v[80+4*\\i:83+4*\\i], v136 offset:1024*\\i\n\t.endr\n\t"
      "s_waitcnt lgkmcnt(2)\n\t"
      "v_mfma_f32_32x32x16_bf16 v[16:31], v[80:83], v[128:131], v[96:111]\n\t"
      "v_mfma_f32_32x32x16_bf16 v[32:47], v[84:87], v[128:131], v[96:111]\n\t"
      "s_waitcnt lgkmcnt(0)\n\t"
      "v_mfma_f32_32x32x16_bf16 v[112:127], v[88:91], v[128:131], v[96:111]\n\t"
      "v_mfma_f32_32x32x16_bf16 v[96:111], v[92:95], v[128:131], v[96:111]\n\t"
      "s_mov_b32 s52, 32768\n\ts_mov_b32 s54, 65536\n\ts_mov_b32 s55, 98304\n\t"
      "s_nop 5\n\t"
      F_PACK(16, 0, 80) F_PACK(32, 0, 88)
      "v_mfma_f32_32x32x16_bf16 v[16:31], v[48:51], v[80:83], 0\n\t"
      F_PACK(112, 0, 128)
      "v_mfma_f32_32x32x16_bf16 v[16:31], v[52:55], v[84:87], v[16:31]\n\t"
      F_PACK(96, 0, 136)
      "v_mfma_f32_32x32x16_bf16 v[16:31], v[56:59], v[88:91], v[16:31]\n\t"
      "v_mfma_f32_32x32x16_bf16 v[32:47], v[64:67], v[128:131], 0\n\t"
      "v_mfma_f32_32x32x16_bf16 v[16:31], v[60:63], v[92:95], v[16:31]\n\t"
      "v_mfma_f32_32x32x16_bf16 v[32:47], v[68:71], v[132:135], v[32:47]\n\t"
      "v_mfma_f32_32x32x16_bf16 v[32:47], v[72:75], v[136:139], v[32:47]\n\t"
      "v_mfma_f32_32x32x16_bf16 v[32:47], v[76:79], v[140:143], v[32:47]\n\t"
      "s_nop 8\n\t"
      F_PACK(16, 0, 80)
      G_ST4(80, 149, "0 offen") G_ST4(84, 149, "s52 offen")
      "s_nop 0\n\t"
      F_PACK(32, 0, 88)
      G_ST4(88, 149, "s54 offen") G_ST4(92, 149, "s55 offen")
      "s_setprio 1\n\t"
      "s_waitcnt vmcnt(0)\n\t"
      "buffer_store_dwordx2 v[2:3], v150, s[72:75], 0 offen sc1\n\t"
      G_C0 G_C1
      "s_branch 301f\n\t"
      "151:\n\t"
      "s_setprio 1\n\t"
      "300:\n\t"
      "s_waitcnt vmcnt(0) lgkmcnt(0)\n\t"
      "301:\n\t"
      "v_mov_b64 v[16:17], 0\n\t"
      "v_permlane16_swap_b32 v9, v8\n\t"
      QK(128, 143, 200, 203, 192, 195, 96, 111)
      ".irp j,0,1,2,3\n\tv_dot2c_f32_bf16 v16, v[192+\\j], v[192+\\j]\n\tv_dot2c_f32_bf16 v17, v[196+\\j], v[196+\\j]\n\t.endr\n\t"
      "v_max_f32 v9, v9, v8\n\t"
      "s_nop 1\n\t"
      "v_permlane32_swap_b32 v16, v17\n\t"
      "v_add_f32 v16, v16, v17\n\t"
      "v_mul_f32 v16, v16, v9\n\t"
      "v_cmp_nge_f32 vcc, 0x45100000, v16\n\t"
      "s_cbranch_vccnz 310f\n\t"
      "311:\n\t"
      ST(4)
      "s_cmp_eq_u32 s56, 0\n\t"
      "s_cbranch_scc1 320f\n\t"
      RUN("5")
      "s_branch 330f\n\t"
      "310:\n\t"
      "v_mov_b32 v96, 0xff7fffff\n\tv_mov_b32 v112, 0xff7fffff\n\t"
      "s_mov_b32 s52, s39\n\ts_mov_b32 s54, 63\n\t"
      "312:\n\t"
      "buffer_load_dwordx4 v[16:19], v4, s[72:75], s52 offen sc1\n\t"
      "s_add_u32 s52, s52, 0x800\n\t"
      "s_waitcnt vmcnt(0)\n\t"
      "v_mfma_f32_32x32x16_bf16 v[32:47], v[16:19], v[192:195], 0\n\t"
      "v_mfma_f32_32x32x16_bf16 v[48:63], v[16:19], v[196:199], 0\n\t"
      "s_nop 11\n\t"
      ".irp i,0,2,4,6,8,10,12,14\n\tv_max3_f32 v96, v96, v[32+\\i], v[33+\\i]\n\tv_max3_f32 v112, v112, v[48+\\i], v[49+\\i]\n\t.endr\n\t"
      "s_sub_u32 s54, s54, 1\n\t"
      "s_cbranch_scc0 312b\n\t"
      "v_mov_b32 v97, v96\n\tv_mov_b32 v113, v112\n\ts_nop 1\n\t"
      "v_permlane32_swap_b32 v96, v97\n\tv_permlane32_swap_b32 v112, v113\n\t"
      "v_max_f32 v96, v96, v97\n\tv_max_f32 v112, v112, v113\n\t"
      "v_xor_b32 v96, 0x80000000, v96\n\tv_xor_b32 v112, 0x80000000, v112\n\t"
      ".irp r,1,2,3,4,5,6,7,8,9,10,11,12,13,14,15\n\tv_mov_b32 v[96+\\r], v96\n\tv_mov_b32 v[112+\\r], v112\n\t.endr\n\t"
      "s_nop 1\n\t"
      QK(128, 143, 200, 203, 192, 195, 96, 111)
      "s_nop 11\n\t"
      "s_branch 311b\n\t"
      "320:\n\t"
      RUN("0")
      ST(5)
      "v_mov_b64 v[16:17], -1\n\tv_mov_b64 v[18:19], -1\n\t"
      G_ST4(16, 4, "s24 offen")
      RUN("0")
      "s_cmp_lg_u32 s37, 0\n\t"
      "s_cbranch_scc1 322f\n\t"
      "v_and_b32 v20, 0x1f0, v4\n\tv_lshlrev_b32 v20, 3, v20\n\ts_add_u32 s52, s29, 4096\n\t"
      "3210:\n\tbuffer_load_dwordx2 v[22:23], v20, s[72:75], s28 offen sc1\n\ts_waitcnt vmcnt(0)\n\t"
      "v_cmp_ne_u64 vcc, s[86:87], v[22:23]\n\t" G_SPIN(321, "s_sleep 4\n\t")
      P_LOAD8(32, 29, 52)
      "322:\n\t"
      RUN("0")
      ST(6)
      G_ST4(16, 4, "s25 offen")
      "s_cmp_lg_u32 s37, 0\n\t"
      "s_cbranch_scc1 323f\n\t"
      G_Z("24,26,28,30")
      "v_mov_b32 v20, 0x3f80\n\tv_mov_b32 v21, 0x3f800000\n\t"
      ".irp i,0,1,2,3,4,5,6,7\n\t.irp j,0,1,2,3\n\t"
      "v_dot2c_f32_bf16 v[24+2*\\j], v[32+4*\\i+\\j], v20\n\tv_dot2c_f32_bf16 v[25+2*\\j], v[32+4*\\i+\\j], v21\n\t"
      ".endr\n\t.endr\n\t"
      "v_and_b32 v20, 0xf0, v4\n\tv_lshrrev_b32 v21, 5, v4\n\tv_lshrrev_b32 v22, 7, v4\n\t"
      ".irp e,0,1,2,3\n\tv_permlane16_swap_b32 v[24+\\e], v[28+\\e]\n\t.endr\n\t"
      ".irp e,0,1,2,3\n\tv_add_f32 v[24+\\e], v[24+\\e], v[28+\\e]\n\t.endr\n\t"
      "v_and_or_b32 v20, v21, 8, v20\n\tv_and_or_b32 v20, v22, 4, v20\n\t"
      "v_permlane32_swap_b32 v24, v26\n\tv_permlane32_swap_b32 v25, v27\n\t"
      "v_add_f32 v24, v24, v26\n\tv_add_f32 v25, v25, v27\n\t"
      "v_cvt_pk_bf16_f32 v24, v24, v25\n\tv_min_u32 v24, -2, v24\n\t"
      "buffer_store_dword v24, v20, s[72:75], s30 offen sc1\n\t"
      "323:\n\t"
      RUN("0")
      ST(7)
      "v_add_u32 v20, s35, v4\n\t"
      P_LOAD(16, 0, 31) P_LOAD(20, 1, 31)
      ".irp i,0,1,2,3\n\tds_read_b128 v[32+4*\\i:35+4*\\i], v20 offset:1024*\\i\n\t.endr\n\t"
      RUN("0")
      ST(8)
      "3240:\n\t"
      "v_max3_u32 v80, v16, v17, v18\n\tv_max3_u32 v81, v19, v24, v25\n\tv_max3_u32 v80, v80, v26, v27\n\tv_max_u32 v80, v80, v81\n\t"
      "v_cmp_eq_u32 vcc, -1, v80\n\t"
      G_SPIN(324, "s_sleep 4\n\t" P_LOAD(16, 0, 31) P_LOAD(20, 1, 31) "s_waitcnt vmcnt(0)\n\t")
      "s_waitcnt lgkmcnt(0)\n\t"
      "v_mfma_f32_32x32x16_bf16 v[48:63], v[16:19], v[32:35], 0\n\t"
      "v_mfma_f32_32x32x16_bf16 v[64:79], v[16:19], v[40:43], 0\n\t"
      "v_mfma_f32_32x32x16_bf16 v[48:63], v[24:27], v[36:39], v[48:63]\n\t"
      "v_mfma_f32_32x32x16_bf16 v[64:79], v[24:27], v[44:47], v[64:79]\n\t"
      "v_sub_f32 v80, 1.0, v253\n\tv_sub_f32 v81, 1.0, v252\n\t"
      "v_mov_b32 v82, s86\n\tv_mov_b32 v83, s87\n\tv_mov_b32 v84, s36\n\t"
      "ds_write_b64 v84, v[82:83]\n\t"
      "s_nop 4\n\t"
      "v_add_f32 v56, 0x358637bd, v56\n\tv_rcp_f32 v56, v56\n\t"
      "v_add_f32 v72, 0x358637bd, v72\n\tv_rcp_f32 v72, v72\n\t"
      "v_mul_f32 v80, v80, v56\n\t"
      ".irp r,48,49,50,51,52,53,54,55\n\tv_mul_f32 v\\r, v80, v\\r\n\t.endr\n\t"
      "v_mul_f32 v81, v81, v72\n\t"
      ".irp i,0,1,2,3\n\tv_cvt_pk_bf16_f32 v[16+\\i], v[48+2*\\i], v[49+2*\\i]\n\t.endr\n\t"
      G_ST4(16, 4, "s26 offen")
      ".irp r,64,65,66,67,68,69,70,71\n\tv_mul_f32 v\\r, v81, v\\r\n\t.endr\n\t"
      ".irp i,0,1,2,3\n\tv_cvt_pk_bf16_f32 v[20+\\i], v[64+2*\\i], v[65+2*\\i]\n\t.endr\n\t"
      G_ST4(20, 4, "s27 offen")
      RUNV("0", STR(HALF_SWITCH))
      "v_cmp_eq_u32 vcc, 0, v4\n\tv_mov_b32 v84, s32\n\tv_bfrev_b32 v80, 15\n\t"
      "s_setprio 0\n\t"
      "v_cndmask_b32 v84, v80, v84, vcc\n\t"
      "buffer_store_dwordx2 v[82:83], v84, s[72:75], 0 offen sc1\n\t"
      "330:\n\t"
      ST(9)
      "v_and_b32 v20, 0x70, v4\n\tv_lshlrev_b32 v20, 3, v20\n\t"
      "buffer_load_dwordx2 v[22:23], v20, s[72:75], s33 offen sc1\n\t"
      RUN("0")
      ST(10)
      "v_mov_b32 v84, s36\n\ts_add_u32 s52, s34, 4096\n\t"
      "3310:\n\tds_read_b64 v[24:25], v84\n\ts_waitcnt lgkmcnt(0)\n\tv_cmp_ne_u64 vcc, s[86:87], v[24:25]\n\t" G_SPIN(331, "s_sleep 2\n\t")
      "3320:\n\tv_cmp_ne_u64 vcc, s[86:87], v[22:23]\n\t"
      G_SPIN(332, "s_sleep 4\n\tbuffer_load_dwordx2 v[22:23], v20, s[72:75], s33 offen sc1\n\ts_waitcnt vmcnt(0)\n\t")
      P_LOAD8(48, 34, 52)
      "v_add_u32 v6, s88, v4\n\tv_lshrrev_b32 v7, 2, v4\n\tv_add_u32 v7, s89, v7\n\t"
      "v_xor_b32 v8, 0x800, v6\n\tv_xor_b32 v9, 0x400, v7\n\tv_xor_b32 v1, 64, v0\n\t"
      "s_add_u32 s54, s34, 0x200000\n\ts_add_u32 s55, s34, 0x201000\n\t"
      RUN("0")
      ST(12)
      PV(176, 191, 244, 247, 152, 159)
      "ds_write_b128 v6, v[160:163]\n\tds_write_b128 v6, v[164:167] offset:1024\n\tds_write_b32 v7, v168\n\t"
      G_BAR(0)
      "ds_read_b32 v24, v9\n\tds_read_b128 v[16:19], v8\n\tds_read_b128 v[20:23], v8 offset:1024\n\t"
      "s_nop 3\n\t"
      "s_waitcnt lgkmcnt(2)\n\t"
      "v_add_f32 v24, v184, v24\n\tv_rcp_f32 v24, v24\n\t"
      "s_waitcnt lgkmcnt(0)\n\t"
      ".irp i,0,1,2,3,4,5,6,7\n\tv_add_f32 v[16+\\i], v[176+\\i], v[16+\\i]\n\t.endr\n\t"
      "v_mul_f32 v24, v253, v24\n\t"
      ".irp i,0,1,2,3,4,5,6,7\n\tv_mul_f32 v[16+\\i], v24, v[16+\\i]\n\t.endr\n\t"
      ".irp i,0,1,2,3\n\tv_cvt_pk_bf16_f32 v[84+\\i], v[16+2*\\i], v[17+2*\\i]\n\t.endr\n\t"
      ".irp i,0,1,2,3\n\tv_min_u32 v[84+\\i], -2, v[84+\\i]\n\t.endr\n\t"
      G_ST4(84, 4, "s38 offen")
      ST(13)
      P_RB(0) P_RB(1)
      P_USEB(0, 4, "0", "0", P_RB(2) P_RA(0) P_AT(0)) P_USEB(1, 8, G_A0, G_A1, P_RB(3) P_RA(1) P_AT(1))
      P_USEB(2, 12, G_A0, G_A1, P_RB(4) P_RA(2) P_AT(2)) P_USEB(3, 12, G_A0, G_A1, P_RB(5) P_RA(3) P_AT(3))
      P_USEB(4, 12, G_A0, G_A1, P_RB(6) P_RA(4) P_AT(4)) P_USEB(5, 12, G_A0, G_A1, P_RB(7) P_RA(5) P_AT(5))
      P_USEB(6, 12, G_A0, G_A1, P_RA(6) P_AT(6)) P_USEB(7, 8, G_A0, G_A1, P_RA(7) P_AT(7))
      "v_xor_b32 v6, 32, v248\n\tv_xor_b32 v7, 64, v248\n\tv_xor_b32 v8, 96, v248\n\t"
      "v_lshrrev_b32 v9, 7, v4\n\tv_bfe_u32 v10, v4, 3, 4\n\tv_xor_b32 v10, v10, v9\n\tv_lshlrev_b32 v10, 4, v10\n\t"
      "v_lshl_add_u32 v10, v9, 8, v10\n\tv_add_u32 v10, s90, v10\n\tv_xor_b32 v11, 16, v10\n\t"
      "v_and_b32 v12, 0x70, v4\n\tv_lshl_or_b32 v12, v9, 10, v12\n\tv_add_u32 v12, s91, v12\n\t"
      G_LBAR
      P_USEA(0, 7) P_USEA(1, 6) P_USEA(2, 5) P_USEA(3, 4) P_USEA(4, 3) P_USEA(5, 2) P_USEA(6, 1) P_USEA(7, 0)
      "s_nop 10\n\t"
      "ds_write_b128 v248, v[16:19]\n\tds_write_b128 v6, v[20:23]\n\tds_write_b128 v7, v[24:27]\n\tds_write_b128 v8, v[28:31]\n\t"
      "ds_write_b128 v248, v[32:35] offset:128\n\tds_write_b128 v6, v[36:39] offset:128\n\t"
      "ds_write_b128 v7, v[40:43] offset:128\n\tds_write_b128 v8, v[44:47] offset:128\n\t"
      ST(15)
      G_LBAR
      ".irp k,0,1,2,3\n\tds_read_b128 v[16+8*\\k:19+8*\\k], v10 offset:16384*\\k\n\tds_read_b128 v[20+8*\\k:23+8*\\k], v11 offset:16384*\\k\n\t.endr\n\t"
      "s_waitcnt lgkmcnt(4)\n\t"
      ".irp i,0,1,2,3,4,5,6,7\n\tv_add_f32 v[16+\\i], v[16+\\i], v[24+\\i]\n\t.endr\n\t"
      "s_waitcnt lgkmcnt(0)\n\t"
      ".irp i,0,1,2,3,4,5,6,7\n\tv_add_f32 v[32+\\i], v[32+\\i], v[40+\\i]\n\t.endr\n\t"
      ".irp i,0,1,2,3,4,5,6,7\n\tv_add_f32 v[16+\\i], v[16+\\i], v[32+\\i]\n\t.endr\n\t"
      ".irp i,0,1,2,3\n\tv_cvt_pk_bf16_f32 v[16+\\i], v[16+2*\\i], v[17+2*\\i]\n\t.endr\n\t"
      "buffer_store_dwordx4 v[16:19], v12, s[20:23], 0 offen" OUT_POL "\n\t"
      "v_cmp_eq_u32 vcc, 0, v4\n\tv_mov_b32 v20, s68\n\tv_bfrev_b32 v21, 15\n\tv_mov_b32 v22, s86\n\tv_mov_b32 v23, s87\n\t"
      "v_cndmask_b32 v20, v21, v20, vcc\n\t"
      "buffer_store_dwordx2 v[22:23], v20, s[72:75], 0 offen sc1\n\t"
      ST_END
      "s_branch 599f\n\t"
      P_STUBS(57, 56, 580) P_SLOW(580, 96, 54, 55)
      "599:"
      : "+{v0}"(tid) ST_OP
      : "{s[0:1]}"(__builtin_amdgcn_kernarg_segment_ptr()), "{s2}"(bid), "{s16}"(g128), "{s17}"(tb), "{s18}"(otb), "{s19}"(og128), "{s59}"(rot)
      : "memory", "scc", "vcc",
#define S4(a, b, c, d) "s" #a, "s" #b, "s" #c, "s" #d
        "s3", S4(4, 5, 6, 7), S4(8, 9, 10, 20), S4(21, 22, 23, 24), S4(25, 26, 27, 28), S4(29, 30, 31, 32), S4(33, 34, 35, 36), S4(37, 38, 39, 40),
        S4(41, 42, 43, 44), S4(45, 46, 47, 48), S4(49, 50, 51, 52), S4(53, 54, 55, 56), S4(57, 58, 60, 61), S4(62, 63, 64, 65), S4(66, 67, 68, 69),
        S4(70, 71, 72, 73), S4(74, 75, 76, 77), S4(78, 79, 80, 81), S4(82, 83, 84, 85), S4(86, 87, 88, 89), S4(90, 91, 92, 93),
#define R8(a, b, c, d, e, f, g, h) "v" #a, "v" #b, "v" #c, "v" #d, "v" #e, "v" #f, "v" #g, "v" #h
        R8(1, 2, 3, 4, 5, 6, 7, 8), R8(9, 10, 11, 12, 13, 14, 15, 254), R8(144, 145, 146, 147, 148, 149, 150, 151),
        R8(152, 153, 154, 155, 156, 157, 158, 159), R8(248, 249, 250, 251, 252, 253, 252, 253),
        R8(16, 17, 18, 19, 20, 21, 22, 23), R8(24, 25, 26, 27, 28, 29, 30, 31), R8(32, 33, 34, 35, 36, 37, 38, 39),
        R8(40, 41, 42, 43, 44, 45, 46, 47), R8(48, 49, 50, 51, 52, 53, 54, 55), R8(56, 57, 58, 59, 60, 61, 62, 63),
        R8(64, 65, 66, 67, 68, 69, 70, 71), R8(72, 73, 74, 75, 76, 77, 78, 79), R8(80, 81, 82, 83, 84, 85, 86, 87),
        R8(88, 89, 90, 91, 92, 93, 94, 95), R8(96, 97, 98, 99, 100, 101, 102, 103), R8(104, 105, 106, 107, 108, 109, 110, 111),
        R8(112, 113, 114, 115, 116, 117, 118, 119), R8(120, 121, 122, 123, 124, 125, 126, 127), R8(128, 129, 130, 131, 132, 133, 134, 135),
        R8(136, 137, 138, 139, 140, 141, 142, 143), R8(160, 161, 162, 163, 164, 165, 166, 167), R8(168, 169, 170, 171, 172, 173, 174, 175),
        R8(176, 177, 178, 179, 180, 181, 182, 183), R8(184, 185, 186, 187, 188, 189, 190, 191), R8(192, 193, 194, 195, 196, 197, 198, 199),
        R8(200, 201, 202, 203, 204, 205, 206, 207), R8(208, 209, 210, 211, 212, 213, 214, 215), R8(216, 217, 218, 219, 220, 221, 222, 223),
        R8(224, 225, 226, 227, 228, 229, 230, 231), R8(232, 233, 234, 235, 236, 237, 238, 239), R8(240, 241, 242, 243, 244, 245, 246, 247) ST_CL);
}
#define HG 2

__global__ __launch_bounds__(512, 2) void gated_mix_kernel(const char* __restrict__ x, const char* __restrict__ wqa,
                                                           const char* __restrict__ woa, const char* __restrict__ wqb,
                                                           const char* __restrict__ wphi, const char* __restrict__ wob,
                                                           const char* __restrict__ gatew, char* __restrict__ out,
                                                           char* __restrict__ ws) {
  __shared__ __attribute__((aligned(16))) char lds_raw[L_BYTES];
  asm volatile("" :: "s"((unsigned)(uintptr_t)(LAS char*)lds_raw));
  STAMP(0); STAMP_RT(62);
  const unsigned xc = blockIdx.x & 7, in = blockIdx.x >> 3;
  const unsigned g = HG * (xc % (8 / HG)) + in % HG, tb = (32 / HG) * (xc / (8 / HG)) + in / HG;
  program(threadIdx.x, blockIdx.x, g * 128, tb, 4 * xc + (in & 3), (in >> 2) * 128, (in / HG + (8 / HG) * (in % HG)) & 7 ST_ARG);
}

}

extern "C" void kernel_launch(void* const* d_in, const int* in_sizes, int n_in, void* d_out, int out_size, void* d_ws,
                              size_t ws_size, hipStream_t stream) {
  gated_mix_kernel<<<256, 512, 0, stream>>>((const char*)d_in[0], (const char*)d_in[1], (const char*)d_in[2], (const char*)d_in[4],
                                            (const char*)d_in[5], (const char*)d_in[6], (const char*)d_in[7], (char*)d_out, (char*)d_ws);
}
```
